```python
import torch

def _softmax(x, axis=-1):
    return torch.softmax(x, dim=axis)
def _gelu(x):
    return torch.nn.functional.gelu(x, approximate="tanh")
def _silu(x):
    return torch.nn.functional.silu(x)
def _sigmoid(x):
    return torch.sigmoid(x)
def _layernorm(x, axis=-1, eps=1e-5):
    m = x.mean(dim=axis, keepdim=True)
    v = x.var(dim=axis, keepdim=True, unbiased=False)
    return (x - m) / torch.sqrt(v + eps)
def _scatter_add(tgt, idx, src, axis=0):
    return tgt.clone().index_add_(axis, idx, src)
def _segment_sum(src, seg, n):
    out = torch.zeros((n,) + src.shape[1:], dtype=src.dtype)
    return out.index_add_(0, seg, src)
def _pad(x, pw):
    flat = tuple(v for pair in reversed(pw) for v in pair)
    return torch.nn.functional.pad(x, flat)

kernel_name = 'gated_mix__sliding_local_dilated__grouped_kv_mqa__prompt'

def setup_inputs():

    _w = (lambda a: (lambda t: t.to(torch.bfloat16) if t.is_floating_point() else t)(torch.as_tensor(a)))
    def _A_setup():
        B, S, D = 1, 2048, 640
        return {
            "x": _w((lambda *s: torch.randn(*s, dtype=torch.bfloat16) * 0.05)(B, S, D)),
            "wqkv": _w((lambda *s: torch.randn(*s, dtype=torch.bfloat16) * 0.8)(D, 3 * D)),
            "wo": _w((lambda *s: torch.randn(*s, dtype=torch.bfloat16) * 0.05)(D, D)),
        }
    _A_in = _A_setup()

    def _B_setup():
        B, S, D, H = 1, 2048, 640, 32
        Dh = D // H
        return {
            "x": _w((lambda *s: torch.randn(*s, dtype=torch.bfloat16) * 0.05)(B, S, D)),
            "wq": _w((lambda *s: torch.randn(*s, dtype=torch.bfloat16) * 1.6)(D, H * Dh)),
            "wk": _w((lambda *s: torch.randn(*s, dtype=torch.bfloat16) * 1.6)(D, Dh)),
            "wv": _w((lambda *s: torch.randn(*s, dtype=torch.bfloat16) * 0.2)(D, Dh)),
            "wo": _w((lambda *s: torch.randn(*s, dtype=torch.bfloat16) * 0.2)(H * Dh, D)),
        }
    _B_in = _B_setup()

    return {
        "A_x": _A_in["x"],
        "A_wqkv": _A_in["wqkv"],
        "A_wo": _A_in["wo"],
        "B_x": _B_in["x"],
        "B_wq": _B_in["wq"],
        "B_wk": _B_in["wk"],
        "B_wv": _B_in["wv"],
        "B_wo": _B_in["wo"],
        "gate_w": _w((lambda *s: torch.randn(*s, dtype=torch.bfloat16) * 0.05)(640) * 16.0),
    }

def reference(A_x, A_wqkv, A_wo, B_x, B_wq, B_wk, B_wv, B_wo, gate_w):
    def _A_ref(x, wqkv, wo):
        B, S, D = x.shape
        H = 32; Dh = D // H
        W, dil = 8, 2
        qkv = (lambda x, *d: x.permute(*d))((x @ wqkv).reshape(B, S, 3, H, Dh), 2, 0, 3, 1, 4)
        q, k, v = qkv[0], qkv[1], qkv[2]
        scale = Dh ** -0.5
        pos = torch.arange(S)
        off = torch.arange(W) * dil
        idx = pos[:, None] - off[None, :]
        mask = idx >= 0
        idx_c = torch.where(mask, idx, 0).reshape(-1)
        kg = k[:, :, idx_c, :].reshape(B, H, S, W, Dh)
        vg = v[:, :, idx_c, :].reshape(B, H, S, W, Dh)
        logits = torch.einsum("bhsd,bhswd->bhsw", q, kg) * scale
        logits = torch.where(mask[None, None, :, :], logits, -1e9)
        attn = _softmax(logits, axis=-1)
        o = torch.einsum("bhsw,bhswd->bhsd", attn, vg)
        return (lambda x, *d: x.permute(*d))(o, 0, 2, 1, 3).reshape(B, S, D) @ wo

    def _B_ref(x, wq, wk, wv, wo):
        B, S, D = x.shape
        H = 32
        Dh = D // H
        q = (lambda x, *d: x.permute(*d))((x @ wq).reshape(B, S, H, Dh), 0, 2, 1, 3)
        k = x @ wk
        v = x @ wv
        scores = torch.einsum("bhqd,bkd->bhqk", q, k) * (Dh ** -0.5)
        p = _softmax(scores, axis=-1)
        o = torch.einsum("bhqk,bkd->bhqd", p, v)
        return (lambda x, *d: x.permute(*d))(o, 0, 2, 1, 3).reshape(B, S, H * Dh) @ wo

    x = A_x
    g = _sigmoid((x * gate_w).sum(-1, keepdims=True))
    a = _A_ref(x, A_wqkv, A_wo)
    b = _B_ref(x, B_wq, B_wk, B_wv, B_wo)
    return g * a + (1 - g) * b
```

```cpp
#include <hip/hip_runtime.h>
#include <hip/hip_bf16.h>
#include <stdint.h>

typedef unsigned short u16;
__device__ __forceinline__ float bf2f(u16 b) { return __uint_as_float(((unsigned)b) << 16); }
__device__ __forceinline__ u16 f2bf(float f) { unsigned u = __float_as_uint(f); u += 0x7fffu + ((u >> 16) & 1u); return (u16)(u >> 16); }

constexpr int S = 2048, D = 640, H = 32, DH = 20;

__global__ void gemm_naive(const u16* A, const u16* B, float* C, int M, int N, int K, int ldc) {
    int n = blockIdx.x * blockDim.x + threadIdx.x; int m = blockIdx.y;
    if (n >= N) return;
    float acc = 0.f;
    for (int k = 0; k < K; ++k) acc += bf2f(A[(size_t)m * K + k]) * bf2f(B[(size_t)k * N + n]);
    C[(size_t)m * ldc + n] = acc;
}
__global__ void gemm_naive_f(const float* A, const u16* B, float* C, int M, int N, int K) {
    int n = blockIdx.x * blockDim.x + threadIdx.x; int m = blockIdx.y;
    if (n >= N) return;
    float acc = 0.f;
    for (int k = 0; k < K; ++k) acc += A[(size_t)m * K + k] * bf2f(B[(size_t)k * N + n]);
    C[(size_t)m * N + n] = acc;
}
__global__ void local_attn(const float* qkv, float* o) {
    int idx = blockIdx.x * blockDim.x + threadIdx.x;
    int s = idx / H, h = idx % H;
    if (s >= S) return;
    const float scale = rsqrtf((float)DH);
    float lg[8]; float m = -1e30f;
    for (int w = 0; w < 8; ++w) {
        int t = s - 2 * w;
        float a = -1e9f;
        if (t >= 0) { a = 0.f; for (int d = 0; d < DH; ++d) a += qkv[(size_t)s * 1920 + h * DH + d] * qkv[(size_t)t * 1920 + 640 + h * DH + d]; a *= scale; }
        lg[w] = a; m = fmaxf(m, a);
    }
    float l = 0.f; for (int w = 0; w < 8; ++w) { lg[w] = __expf(lg[w] - m); l += lg[w]; }
    for (int d = 0; d < DH; ++d) {
        float a = 0.f;
        for (int w = 0; w < 8; ++w) { int t = s - 2 * w; if (t < 0) t = 0; a += lg[w] * qkv[(size_t)t * 1920 + 1280 + h * DH + d]; }
        o[(size_t)s * D + h * DH + d] = a / l;
    }
}
__global__ void mqa_attn(const float* q, const float* k, const float* v, float* o) {
    int idx = blockIdx.x * blockDim.x + threadIdx.x;
    int s = idx / H, h = idx % H;
    const float scale = rsqrtf((float)DH);
    float qq[DH]; for (int d = 0; d < DH; ++d) qq[d] = q[(size_t)s * D + h * DH + d] * scale;
    float m = -1e30f, l = 0.f; float acc[DH]; for (int d = 0; d < DH; ++d) acc[d] = 0.f;
    for (int t = 0; t < S; ++t) {
        float a = 0.f; for (int d = 0; d < DH; ++d) a += qq[d] * k[t * DH + d];
        float mn = fmaxf(m, a); float c = __expf(m - mn); float p = __expf(a - mn);
        l = l * c + p; for (int d = 0; d < DH; ++d) acc[d] = acc[d] * c + p * v[t * DH + d];
        m = mn;
    }
    for (int d = 0; d < DH; ++d) o[(size_t)s * D + h * DH + d] = acc[d] / l;
}
__global__ void final_mix(const u16* x, const u16* gw, const float* a, const float* b, u16* out) {
    int s = blockIdx.x; __shared__ float red[256];
    float acc = 0.f; for (int d = threadIdx.x; d < D; d += 256) acc += bf2f(x[(size_t)s * D + d]) * bf2f(gw[d]);
    red[threadIdx.x] = acc; __syncthreads();
    for (int o = 128; o > 0; o >>= 1) { if (threadIdx.x < o) red[threadIdx.x] += red[threadIdx.x + o]; __syncthreads(); }
    float g = 1.f / (1.f + __expf(-red[0]));
    for (int d = threadIdx.x; d < D; d += 256) out[(size_t)s * D + d] = f2bf(g * a[(size_t)s * D + d] + (1.f - g) * b[(size_t)s * D + d]);
}

extern "C" void kernel_launch(void* const* d_in, const int* in_sizes, int n_in, void* d_out, int out_size, void* d_ws, size_t ws_size, hipStream_t stream) {
    const u16* Ax = (const u16*)d_in[0]; const u16* Awqkv = (const u16*)d_in[1]; const u16* Awo = (const u16*)d_in[2];
    const u16* Bx = (const u16*)d_in[3]; const u16* Bwq = (const u16*)d_in[4]; const u16* Bwk = (const u16*)d_in[5]; const u16* Bwv = (const u16*)d_in[6]; const u16* Bwo = (const u16*)d_in[7];
    const u16* gw = (const u16*)d_in[8];
    float* ws = (float*)d_ws;
    float* qkv = ws; float* oA = qkv + (size_t)S * 1920; float* a = oA + (size_t)S * D; float* qB = a + (size_t)S * D; float* kB = qB + (size_t)S * D; float* vB = kB + S * DH;
    float* oB = vB + S * DH; float* b = oB + (size_t)S * D;
    gemm_naive<<<dim3(1920 / 64, S), 64, 0, stream>>>(Ax, Awqkv, qkv, S, 1920, D, 1920);
    local_attn<<<S * H / 256, 256, 0, stream>>>(qkv, oA);
    gemm_naive_f<<<dim3(D / 64, S), 64, 0, stream>>>(oA, Awo, a, S, D, D);
    gemm_naive<<<dim3(D / 64, S), 64, 0, stream>>>(Ax, Bwq, qB, S, D, D, D);
    gemm_naive<<<dim3(1, S), 64, 0, stream>>>(Ax, Bwk, kB, S, DH, D, DH);
    gemm_naive<<<dim3(1, S), 64, 0, stream>>>(Ax, Bwv, vB, S, DH, D, DH);
    mqa_attn<<<S * H / 256, 256, 0, stream>>>(qB, kB, vB, oB);
    gemm_naive_f<<<dim3(D / 64, S), 64, 0, stream>>>(oB, Bwo, b, S, D, D);
    final_mix<<<S, 256, 0, stream>>>(Ax, gw, a, b, (u16*)d_out);
}
```

```cpp
#include <hip/hip_runtime.h>
#include <stdint.h>

typedef unsigned short u16;
typedef short bf16x8 __attribute__((ext_vector_type(8)));
typedef short s16x4 __attribute__((ext_vector_type(4)));
typedef float f32x16 __attribute__((ext_vector_type(16)));
typedef float f32x4 __attribute__((ext_vector_type(4)));
typedef float f32x2 __attribute__((ext_vector_type(2)));
typedef unsigned u32x4 __attribute__((ext_vector_type(4)));
typedef unsigned u32x2 __attribute__((ext_vector_type(2)));
typedef __bf16 bf2_t __attribute__((ext_vector_type(2)));
#define LAS __attribute__((address_space(3)))
typedef LAS const char* lds_cptr;
typedef LAS char* lds_ptr;

__device__ __forceinline__ float bf2f(u16 b) { return __uint_as_float(((unsigned)b) << 16); }
__device__ __forceinline__ unsigned pk2(float lo, float hi) { f32x2 v = {lo, hi}; bf2_t r = __builtin_convertvector(v, bf2_t); return __builtin_bit_cast(unsigned, r); }
__device__ __forceinline__ u16 f2bf(float f) { return (u16)(pk2(f, 0.f) & 0xffffu); }
__device__ __forceinline__ float lo2f(unsigned u) { return __uint_as_float(u << 16); }
__device__ __forceinline__ float hi2f(unsigned u) { return __uint_as_float(u & 0xffff0000u); }
__device__ __forceinline__ float dot2(unsigned a, unsigned b, float c) { return __builtin_amdgcn_fdot2_f32_bf16(__builtin_bit_cast(bf2_t, a), __builtin_bit_cast(bf2_t, b), c, false); }

constexpr int S = 2048, D = 640, H = 32, DH = 20;
constexpr float SCALE = 0.22360679774997896f, LOG2E = 1.4426950408889634f;
constexpr float QSCALE = SCALE * LOG2E;

constexpr int STG_KM = 0, STG_KT = 8192, STG_VT = 12288, VT_ROW = 528, STG_BYTES = 24576;
constexpr size_t WS_QM = 0;
constexpr size_t WS_QT = WS_QM + (size_t)H * S * 16 * 2;
constexpr size_t WS_KV = WS_QT + (size_t)H * S * 4 * 2;
constexpr size_t WS_NRM = WS_KV + 8 * STG_BYTES;
constexpr size_t WS_G = WS_NRM + 2048;
constexpr size_t WS_O = WS_G + 8192;

#define MFMA32(a, b, c) __builtin_amdgcn_mfma_f32_32x32x16_bf16((a), (b), (c), 0, 0, 0)
#define MFMA16(a, b, c) __builtin_amdgcn_mfma_f32_16x16x32_bf16((a), (b), (c), 0, 0, 0)
#define SBAR() __builtin_amdgcn_sched_barrier(0)
#define EXP2(x) __builtin_amdgcn_exp2f(x)
#define WAIT_BAR(N) asm volatile("s_waitcnt vmcnt(" #N ")\n\ts_barrier" ::: "memory")
#define LBAR() asm volatile("s_waitcnt lgkmcnt(0)\n\ts_barrier" ::: "memory")
__device__ __forceinline__ int swap23(int x) { return (x & ~12) | ((x & 4) << 1) | ((x & 8) >> 1); }
__device__ __forceinline__ void glds16(const void* g, unsigned lds_base) {
    unsigned sv; asm volatile("s_mov_b32 %0, m0\n\ts_mov_b32 m0, %2\n\ts_nop 0\n\tglobal_load_lds_dwordx4 %1, off\n\ts_mov_b32 m0, %0" : "=&s"(sv) : "v"(g), "s"(lds_base) : "memory"); }
__device__ __forceinline__ bf16x8 lds128(lds_cptr p) { return *(const LAS bf16x8*)p; }
typedef short v4i16_t __attribute__((ext_vector_type(4)));
__device__ __forceinline__ s16x4 ldstr(lds_cptr p) { return __builtin_bit_cast(s16x4, __builtin_amdgcn_ds_read_tr16_b64_v4i16((LAS v4i16_t*)p)); }
__device__ __forceinline__ bf16x8 ldstr2(lds_cptr p0, lds_cptr p1) { const s16x4 a = ldstr(p0), b = ldstr(p1); return __builtin_shufflevector(a, b, 0, 1, 2, 3, 4, 5, 6, 7); }
__device__ __forceinline__ unsigned rfl(unsigned x) { return (unsigned)__builtin_amdgcn_readfirstlane(x); }

constexpr int A_WIMG = 20480, A_STG = 40960, A_P0 = 3 * A_STG;
constexpr int A_LDS = A_P0 + 25600 + 64;
constexpr int QK_ROW = 208, V_ROW = 176, ST_QK = 0, ST_V = 144 * QK_ROW;

struct Inputs { const u16 *x, *wqkv, *woA, *wq, *wk, *wv, *woB, *gw; };

__device__ __forceinline__ void phase0(const Inputs& in, char* ws, char* lds, int b, int wid, int lane) {
    const int rt16 = b >> 1, part = b & 1, r0 = rt16 * 16;
    const int i16 = lane & 15, g = lane >> 4;
    const int ks0 = r0 & 255; char* img = ws + WS_KV + (size_t)(r0 >> 8) * STG_BYTES;
    float* nrm = (float*)(ws + WS_NRM) + rt16 * 4;
    if (wid < 2 || (wid == 2 && part == 0)) {
        bf16x8 xf[20];
        const u16* xr = in.x + (size_t)(r0 + i16) * D + 8 * g;
#pragma unroll
        for (int ks = 0; ks < 20; ++ks) xf[ks] = *(const bf16x8*)(xr + 32 * ks);
        f32x4 acc = {0.f, 0.f, 0.f, 0.f};
        if (wid == 2) {
            const bf16x8 z = {0, 0, 0, 0, 0, 0, 0, 0};
#pragma unroll
            for (int ks = 0; ks < 20; ++ks) { const bf16x8 gv = *(const bf16x8*)(in.gw + 32 * ks + 8 * g); acc = MFMA16(i16 == 0 ? gv : z, xf[ks], acc); }
            if (g == 0) ((float*)(ws + WS_G))[r0 + i16] = 1.f / (1.f + __expf(-acc[0]));
            asm volatile("s_waitcnt vmcnt(0)\n\ts_barrier" ::: "memory");
        } else {
            asm volatile("s_waitcnt vmcnt(0)\n\ts_barrier" ::: "memory");
            const int q = i16 >> 2, p = i16 & 3;
            const lds_cptr wb = (lds_cptr)lds + A_P0 + (8 * g + q) * 40 + (16 * wid + 4 * p) * 2;
            if (part == 0) {
#pragma unroll
                for (int ks = 0; ks < 20; ++ks) acc = MFMA16(ldstr2(wb + ks * 1280, wb + ks * 1280 + 160), xf[ks], acc);
                const unsigned w0 = pk2(acc[0], acc[1]), w1 = pk2(acc[2], acc[3]);
                const int key = ks0 + i16, tile = key >> 5, r = swap23(key & 31);
                float n2 = lo2f(w0) * lo2f(w0) + hi2f(w0) * hi2f(w0) + lo2f(w1) * lo2f(w1) + hi2f(w1) * hi2f(w1);
                if (wid == 0) {
                    *(u32x2*)(img + STG_KM + tile * 1024 + (g >> 1) * 512 + r * 16 + (g & 1) * 8) = (u32x2){w0, w1};
                    n2 += __shfl_xor(n2, 16); n2 += __shfl_xor(n2, 32);
                } else {
                    if (g == 0) *(u32x4*)(img + STG_KT + (tile >> 1) * 1024 + (tile & 1) * 512 + r * 16) = (u32x4){w0, w1, 0x3f80u, 0u};
                    if (g != 0) n2 = 0.f;
                }
#pragma unroll
                for (int o = 1; o < 64; o <<= 1) n2 = fmaxf(n2, __shfl_xor(n2, o));
                if (lane == 0) nrm[wid] = n2;
            } else {
#pragma unroll
                for (int ks = 0; ks < 20; ++ks) acc = MFMA16(xf[ks], ldstr2(wb + ks * 1280, wb + ks * 1280 + 160), acc);
                unsigned w0 = pk2(acc[0], acc[1]), w1 = pk2(acc[2], acc[3]);
                const int d = 16 * wid + i16;
                float vm = fmaxf(fmaxf(fabsf(lo2f(w0)), fabsf(hi2f(w0))), fmaxf(fabsf(lo2f(w1)), fabsf(hi2f(w1))));
                if (d >= 20) vm = 0.f;
                if (d == 20) { w0 = 0x3f803f80u; w1 = w0; }
                if (d <= 20) *(u32x2*)(img + STG_VT + d * VT_ROW + (ks0 + 4 * g) * 2) = (u32x2){w0, w1};
                if (wid == 1 && ks0 == 0 && lane < 40) *(u32x4*)(img + STG_VT + 21 * VT_ROW + lane * 16) = (u32x4){0u, 0u, 0u, 0u};
#pragma unroll
                for (int o = 1; o < 64; o <<= 1) vm = fmaxf(vm, __shfl_xor(vm, o));
                if (lane == 0) nrm[2 + wid] = vm;
            }
        }
    } else {
        asm volatile("s_waitcnt vmcnt(0)\n\ts_barrier" ::: "memory");
    }
}

__global__ __launch_bounds__(512, 2) void k_proj(Inputs in, char* ws) {
    extern __shared__ __attribute__((aligned(16))) char lds[];
    const int tid = threadIdx.x, lane = tid & 63, wid = __builtin_amdgcn_readfirstlane(tid >> 6);
    const int b = blockIdx.x;
    const int xg = b & 7, li = b >> 3;
    const int rt = (xg >> 2) * 8 + (li >> 2), c = (xg & 3) * 4 + (li & 3);
    const int s0 = 128 * rt;
    const unsigned lds0 = (unsigned)(uintptr_t)lds;
    { const char* wsrc = (const char*)((b & 1) ? in.wv : in.wk) + lane * 16;
#pragma unroll
      for (int i = 0; i < 4; ++i) { const int piece = wid + 8 * i; if (piece < 25) glds16(wsrc + piece * 1024, rfl(lds0 + A_P0 + piece * 1024)); } }
    const char* src[5]; unsigned stride[5];
#pragma unroll
    for (int j = 0; j < 5; ++j) {
        const int id = wid + 8 * j;
        if (id < 20) {
            const int p = id * 64 + lane, half = p & 1, rowp = (p >> 1) & 7, sub = p >> 4, kb = sub / 10, nb = sub - kb * 10;
            const int k = 8 * kb + (rowp ^ (4 * (kb & 1))), col = 16 * nb + 8 * half, seg = col / 40, within = col - seg * 40;
            if (seg == 0) { src[j] = (const char*)(in.wq + (size_t)k * 640 + 40 * c + within); stride[j] = 64 * 640 * 2; }
            else { src[j] = (const char*)(in.wqkv + (size_t)k * 1920 + (seg - 1) * 640 + 40 * c + within); stride[j] = 64 * 1920 * 2; }
        } else {
            const int p = (id - 20) * 64 + lane, row = p >> 3, cpos = p & 7, chunk = cpos ^ ((row >> 1) & 7);
            int gr = s0 - 16 + row; gr = gr < 0 ? 0 : (gr > S - 1 ? S - 1 : gr);
            src[j] = (const char*)(in.x + (size_t)gr * D + chunk * 8); stride[j] = 128;
        }
    }
#define A_ISSUE(buf) do { _Pragma("unroll") for (int j = 0; j < 5; ++j) { glds16(src[j], rfl(lds0 + (buf) * A_STG + (wid + 8 * j) * 1024)); src[j] += stride[j]; } } while (0)
    A_ISSUE(0); A_ISSUE(1);
    phase0(in, ws, lds, b, wid, lane);
    const int cg = wid >> 2, rg = wid & 3, i16 = lane & 15, g = lane >> 4, q4 = i16 >> 2, p4 = i16 & 3;
    const lds_cptr wb0 = (lds_cptr)lds + g * 2560 + (q4 + 4 * (0 ^ (g & 1))) * 32 + p4 * 8 + cg * 5 * 256;
    const lds_cptr wb1 = (lds_cptr)lds + g * 2560 + (q4 + 4 * (1 ^ (g & 1))) * 32 + p4 * 8 + cg * 5 * 256;
    const lds_cptr xb0 = (lds_cptr)lds + A_WIMG + i16 * 128 + ((g ^ (i16 >> 1)) * 16);
    const lds_cptr xb1 = (lds_cptr)lds + A_WIMG + i16 * 128 + (((4 + g) ^ (i16 >> 1)) * 16);
    f32x4 acc[2][5], hacc[2];
#pragma unroll
    for (int i = 0; i < 2; ++i) { hacc[i] = (f32x4){0.f, 0.f, 0.f, 0.f};
#pragma unroll
        for (int j = 0; j < 5; ++j) acc[i][j] = (f32x4){0.f, 0.f, 0.f, 0.f}; }
    int buf = 0;
#pragma unroll 1
    for (int t = 0; t < 10; ++t) {
        if (t < 9) WAIT_BAR(5); else WAIT_BAR(0);
        if (t + 2 < 10) { const int nb = buf >= 1 ? buf - 1 : 2; A_ISSUE(nb); }
        const int bo = buf * A_STG;
#pragma unroll
        for (int ks = 0; ks < 2; ++ks) {
            const lds_cptr xb = (ks ? xb1 : xb0) + bo;
            const bf16x8 x0 = lds128(xb + (1 + 2 * rg) * 2048), x1 = lds128(xb + (2 + 2 * rg) * 2048);
#pragma unroll
            for (int j = 0; j < 5; ++j) {
                const bf16x8 wf = ldstr2(wb0 + bo + ks * 10240 + j * 256, wb1 + bo + ks * 10240 + j * 256);
                acc[0][j] = MFMA16(wf, x0, acc[0][j]); acc[1][j] = MFMA16(wf, x1, acc[1][j]);
            }
            if (cg == 1) {
                const bf16x8 xh = lds128(xb);
                hacc[0] = MFMA16(ldstr2(wb0 + bo + ks * 10240 + rg * 256, wb1 + bo + ks * 10240 + rg * 256), xh, hacc[0]);
                if (rg == 0) hacc[1] = MFMA16(ldstr2(wb0 + bo + ks * 10240 + 4 * 256, wb1 + bo + ks * 10240 + 4 * 256), xh, hacc[1]);
            }
        }
        buf = buf == 2 ? 0 : buf + 1;
    }
    LBAR();
    lds_ptr st = (lds_ptr)lds;
    auto put = [&](const f32x4& a, int row, int nb) {
        const int cq = 4 * nb + g, seg = (cq * 13) >> 6, dq = cq - 5 * seg;
        if (seg < 2) {
            const int s = s0 + row - 16, h = 2 * c + seg;
            const u32x2 w = {pk2(a[0] * QSCALE, a[1] * QSCALE), pk2(a[2] * QSCALE, a[3] * QSCALE)};
            if (dq < 4) *(u32x2*)(ws + WS_QM + (((size_t)h * S + s) * 16 + 4 * dq) * 2) = w; else *(u32x2*)(ws + WS_QT + ((size_t)h * S + s) * 8) = w;
        } else if (seg < 6) {
            *(LAS u32x2*)(st + ST_QK + row * QK_ROW + (seg - 2) * 48 + dq * 8) = (u32x2){pk2(a[0], a[1]), pk2(a[2], a[3])};
        } else {
            *(LAS f32x4*)(st + ST_V + row * V_ROW + (seg - 6) * 80 + dq * 16) = a;
        }
    };
#pragma unroll
    for (int i = 0; i < 2; ++i)
#pragma unroll
        for (int j = 0; j < 5; ++j) put(acc[i][j], 16 + 32 * rg + 16 * i + i16, 5 * cg + j);
    if (cg == 1) { put(hacc[0], i16, 5 + rg); if (rg == 0) put(hacc[1], i16, 9); }
    LBAR();
    if (tid < 256) {
        const int h = tid >> 7, sl = tid & 127, row = 16 + sl, s = s0 + sl;
        const lds_cptr qp = (lds_cptr)lds + ST_QK + row * QK_ROW + h * 48;
        const u32x4 qa = *(const LAS u32x4*)qp, qb = *(const LAS u32x4*)(qp + 16); const u32x2 qc = *(const LAS u32x2*)(qp + 32);
        float lg[8]; float m = -3.0e38f;
#pragma unroll
        for (int w = 0; w < 8; ++w) {
            const lds_cptr kp = qp + 96 - 2 * w * QK_ROW;
            const u32x4 ka = *(const LAS u32x4*)kp, kb = *(const LAS u32x4*)(kp + 16); const u32x2 kc = *(const LAS u32x2*)(kp + 32);
            float a = dot2(qa[0], ka[0], 0.f), a2 = dot2(qa[1], ka[1], 0.f);
            a = dot2(qa[2], ka[2], a); a2 = dot2(qa[3], ka[3], a2); a = dot2(qb[0], kb[0], a); a2 = dot2(qb[1], kb[1], a2);
            a = dot2(qb[2], kb[2], a); a2 = dot2(qb[3], kb[3], a2); a = dot2(qc[0], kc[0], a); a2 = dot2(qc[1], kc[1], a2);
            a = (a + a2) * QSCALE;
            lg[w] = (s - 2 * w >= 0) ? a : -1.0e9f * LOG2E; m = fmaxf(m, lg[w]);
        }
        float l = 0.f;
#pragma unroll
        for (int w = 0; w < 8; ++w) { lg[w] = EXP2(lg[w] - m); l += lg[w]; }
        const float inv = 1.f / l;
        f32x4 o[5];
#pragma unroll
        for (int j = 0; j < 5; ++j) o[j] = (f32x4){0.f, 0.f, 0.f, 0.f};
        const lds_cptr vp = (lds_cptr)lds + ST_V + row * V_ROW + h * 80;
#pragma unroll
        for (int w = 0; w < 8; ++w) {
            const float pw = lg[w] * inv;
#pragma unroll
            for (int j = 0; j < 5; ++j) o[j] += pw * *(const LAS f32x4*)(vp - 2 * w * V_ROW + j * 16);
        }
        u16* og = (u16*)(ws + WS_O) + (size_t)s * 1280 + (2 * c + h) * DH;
#pragma unroll
        for (int j = 0; j < 5; ++j) *(u32x2*)(og + 4 * j) = (u32x2){pk2(o[j][0], o[j][1]), pk2(o[j][2], o[j][3])};
    }
}

constexpr int B_LDS = 6 * STG_BYTES + 64;
__device__ __forceinline__ void att_stage_load(const char* kv, int st, unsigned ldsbuf, int w4, int lane) {
    const char* src = kv + (size_t)st * STG_BYTES + lane * 16;
#pragma unroll
    for (int i = 0; i < 6; ++i) { const int piece = w4 + 4 * i; glds16(src + piece * 1024, rfl(ldsbuf + piece * 1024)); }
}
__device__ __forceinline__ void lds_signal(LAS unsigned* cnt, int lane) { if (lane == 0) __hip_atomic_fetch_add(cnt, 1u, __ATOMIC_RELAXED, __HIP_MEMORY_SCOPE_WORKGROUP); }
__device__ __forceinline__ void lds_wait(LAS unsigned* cnt, unsigned n) {
    while (rfl(__hip_atomic_load(cnt, __ATOMIC_RELAXED, __HIP_MEMORY_SCOPE_WORKGROUP)) < n) __builtin_amdgcn_s_sleep(1);
    asm volatile("" ::: "memory");
}

__device__ __forceinline__ void att_maxpass(const char* kv, char* ldsh, int kh, int w4, int lane, const bf16x8 (&qm)[2], const bf16x8 (&qt)[2][2], float (&mx)[2]) {
    const unsigned lds0 = (unsigned)(uintptr_t)ldsh;
    const lds_cptr kbase = (lds_cptr)ldsh + lane * 16;
    const f32x16 zero = {};
    WAIT_BAR(0);
#pragma unroll 1
    for (int st = 0; st < 4; ++st) {
        if (st == 3) { WAIT_BAR(0); att_stage_load(kv, 4 * kh + 3, lds0, w4, lane); WAIT_BAR(0); }
        const lds_cptr kb = kbase + (st == 3 ? 0 : st) * STG_BYTES;
#pragma unroll 1
        for (int pr = 0; pr < 4; ++pr) {
            const bf16x8 kt = lds128(kb + STG_KT + pr * 1024), k0 = lds128(kb + STG_KM + (2 * pr) * 1024), k1 = lds128(kb + STG_KM + (2 * pr + 1) * 1024);
#pragma unroll
            for (int h = 0; h < 2; ++h) {
                f32x16 s0 = MFMA32(k0, qm[h], zero); s0 = MFMA32(kt, qt[h][0], s0);
                f32x16 s1 = MFMA32(k1, qm[h], zero); s1 = MFMA32(kt, qt[h][1], s1);
#pragma unroll
                for (int i = 0; i < 16; ++i) mx[h] = fmaxf(mx[h], fmaxf(s0[i], s1[i]));
            }
        }
    }
    WAIT_BAR(0);
    att_stage_load(kv, 4 * kh, lds0, w4, lane);
    WAIT_BAR(0);
}

__device__ __forceinline__ void att_main(const char* kv, char* ldsh, LAS unsigned* cnt, int kh, int w4, int lane, const bf16x8 (&qm)[2], const bf16x8 (&qt)[2][2], f32x16& O0, f32x16& O1) {
    const unsigned lds0 = (unsigned)(uintptr_t)ldsh;
    const int r = lane & 31, hh = lane >> 5;
    const int voff = r <= 20 ? r * VT_ROW : (r < 28 ? 21 * VT_ROW : 21 * VT_ROW + 112);
    const lds_cptr kbase = (lds_cptr)ldsh + lane * 16;
    const lds_cptr vbase = (lds_cptr)ldsh + STG_VT + voff + hh * 16;
    const f32x16 zero = {};
    f32x16 sA, sB; u32x4 pA0, pA1, pB0, pB1; bf16x8 kmA, kmB, ktA, ktB, vA0, vA1, vB0, vB1;
    ktA = lds128(kbase + STG_KT); kmA = lds128(kbase + STG_KM);
    sA = MFMA32(kmA, qm[0], zero); sA = MFMA32(ktA, qt[0][0], sA);
    pB0 = (u32x4){0u, 0u, 0u, 0u}; pB1 = pB0; vB0 = kmA; vB1 = kmA; ktB = ktA; vA0 = kmA; vA1 = kmA; kmB = kmA;
    float ee0 = 0.f, ee1 = 0.f, ee2 = 0.f, ee3 = 0.f;
#define GRP(MF, LD, SC, I, PD, J) do { MF; LD; const float e0_ = EXP2(SC[I]); unsigned a_ = pk2(ee0, ee1); const float e1_ = EXP2(SC[I + 1]); unsigned b_ = pk2(ee2, ee3); const float e2_ = EXP2(SC[I + 2]), e3_ = EXP2(SC[I + 3]); \
        ee0 = e0_; ee1 = e1_; ee2 = e2_; ee3 = e3_; asm volatile("" : "+v"(a_), "+v"(b_), "+v"(ee0), "+v"(ee1), "+v"(ee2), "+v"(ee3)); PD[J] = a_; PD[J + 1] = b_; SBAR(); } while (0)
#define TILE(KM, KMN, KT, VJ, KTN, VJN, VC0, VC1, VP0, VP1, VADDR, LDK, LDT) do { \
        GRP(sB = MFMA32(KM, qm[1], zero), VC0 = lds128(VADDR), sA, 0, pB1, 2); \
        GRP(O1 = MFMA32(VP0, __builtin_bit_cast(bf16x8, pB0), O1), VC1 = lds128((VADDR) + 32), sA, 4, pA0, 0); \
        GRP(sB = MFMA32(KT, qt[1][VJ], sB), LDK, sA, 8, pA0, 2); \
        GRP(O1 = MFMA32(VP1, __builtin_bit_cast(bf16x8, pB1), O1), LDT, sA, 12, pA1, 0); \
        GRP(sA = MFMA32(KMN, qm[0], zero), (void)0, sB, 0, pA1, 2); \
        GRP(O0 = MFMA32(VC0, __builtin_bit_cast(bf16x8, pA0), O0), (void)0, sB, 4, pB0, 0); \
        GRP(sA = MFMA32(KTN, qt[0][VJN], sA), (void)0, sB, 8, pB0, 2); \
        GRP(O0 = MFMA32(VC1, __builtin_bit_cast(bf16x8, pA1), O0), (void)0, sB, 12, pB1, 0); } while (0)
#pragma unroll 1
    for (int st = 0; st < 4; ++st) {
        const int buf = st == 3 ? 0 : st, nbuf = st == 2 ? 0 : (st == 3 ? 1 : st + 1);
        const lds_cptr kb = kbase + buf * STG_BYTES, vb = vbase + buf * STG_BYTES, kn = kbase + nbuf * STG_BYTES;
        TILE(kmA, kmB, ktA, 0, ktA, 1, vA0, vA1, vB0, vB1, vb + 0 * 64, kmB = lds128(kb + STG_KM + 1 * 1024), (void)0);
        if (st == 1 || st == 2) {
            asm volatile("s_waitcnt vmcnt(0)" ::: "memory"); lds_signal(cnt + (st - 1), lane); SBAR();
        }
        TILE(kmB, kmA, ktA, 1, ktB, 0, vB0, vB1, vA0, vA1, vb + 1 * 64, kmA = lds128(kb + STG_KM + 2 * 1024), ktB = lds128(kb + STG_KT + 1024));
        TILE(kmA, kmB, ktB, 0, ktB, 1, vA0, vA1, vB0, vB1, vb + 2 * 64, kmB = lds128(kb + STG_KM + 3 * 1024), (void)0);
        TILE(kmB, kmA, ktB, 1, ktA, 0, vB0, vB1, vA0, vA1, vb + 3 * 64, kmA = lds128(kb + STG_KM + 4 * 1024), ktA = lds128(kb + STG_KT + 2048));
        TILE(kmA, kmB, ktA, 0, ktA, 1, vA0, vA1, vB0, vB1, vb + 4 * 64, kmB = lds128(kb + STG_KM + 5 * 1024), (void)0);
        TILE(kmB, kmA, ktA, 1, ktB, 0, vB0, vB1, vA0, vA1, vb + 5 * 64, kmA = lds128(kb + STG_KM + 6 * 1024), ktB = lds128(kb + STG_KT + 3072));
        if (st == 1 || st == 2) {
            lds_wait(cnt + (st - 1), 4u);
            if (st == 1) att_stage_load(kv, 4 * kh + 3, lds0, w4, lane);
            SBAR();
        }
        TILE(kmA, kmB, ktB, 0, ktB, 1, vA0, vA1, vB0, vB1, vb + 6 * 64, kmB = lds128(kb + STG_KM + 7 * 1024), (void)0);
        TILE(kmB, kmA, ktB, 1, ktA, 0, vB0, vB1, vA0, vA1, vb + 7 * 64, kmA = lds128(kn + STG_KM + 0 * 1024), ktA = lds128(kn + STG_KT));
    }
    pB1[2] = pk2(ee0, ee1); pB1[3] = pk2(ee2, ee3);
    O1 = MFMA32(vB0, __builtin_bit_cast(bf16x8, pB0), O1);
    O1 = MFMA32(vB1, __builtin_bit_cast(bf16x8, pB1), O1);
#undef GRP
#undef TILE
}

__global__ __launch_bounds__(512, 2) void k_attn(char* ws) {
    extern __shared__ __attribute__((aligned(16))) char lds[];
    const int tid = threadIdx.x, lane = tid & 63, wid = __builtin_amdgcn_readfirstlane(tid >> 6);
    const int w4 = wid & 3, kh = wid >> 2;
    const int b = blockIdx.x, rt = b >> 4, c = b & 15;
    const int s0 = 128 * rt + 32 * w4;
    const int r = lane & 31, hh = lane >> 5;
    const char* kv = ws + WS_KV;
    char* ldsh = lds + kh * 3 * STG_BYTES;
    const unsigned lds0 = (unsigned)(uintptr_t)ldsh;
    LAS unsigned* ctl = (LAS unsigned*)((lds_ptr)lds + 6 * STG_BYTES);
    att_stage_load(kv, 4 * kh, lds0, w4, lane); att_stage_load(kv, 4 * kh + 1, lds0 + STG_BYTES, w4, lane);
    bf16x8 qm[2]; u32x2 qtl[2];
#pragma unroll
    for (int h = 0; h < 2; ++h) {
        qm[h] = *(const bf16x8*)(ws + WS_QM + (((size_t)(2 * c + h) * S + s0 + r) * 16 + hh * 8) * 2);
        qtl[h] = *(const u32x2*)(ws + WS_QT + ((size_t)(2 * c + h) * S + s0 + r) * 8);
    }
    const f32x4 n0 = ((const f32x4*)(ws + WS_NRM))[lane], n1 = ((const f32x4*)(ws + WS_NRM))[64 + lane];
    if (tid < 8) ctl[tid] = 0u;
    float km2 = fmaxf(n0[0], n1[0]), kt2 = fmaxf(n0[1], n1[1]), vmax = fmaxf(fmaxf(n0[2], n1[2]), fmaxf(n0[3], n1[3]));
#pragma unroll
    for (int o = 1; o < 64; o <<= 1) { km2 = fmaxf(km2, __shfl_xor(km2, o)); kt2 = fmaxf(kt2, __shfl_xor(kt2, o)); vmax = fmaxf(vmax, __shfl_xor(vmax, o)); }
    const float kmax2 = km2 + kt2;
    float q2[2];
#pragma unroll
    for (int h = 0; h < 2; ++h) {
        q2[h] = 0.f;
#pragma unroll
        for (int i = 0; i < 8; ++i) { const float v = bf2f((u16)qm[h][i]); q2[h] += v * v; }
        if (hh == 0) { const float a0 = lo2f(qtl[h][0]), a1 = hi2f(qtl[h][0]), a2 = lo2f(qtl[h][1]), a3 = hi2f(qtl[h][1]); q2[h] += a0 * a0 + a1 * a1 + a2 * a2 + a3 * a3; }
        q2[h] += __shfl_xor(q2[h], 32);
    }
    asm volatile("s_waitcnt vmcnt(0) lgkmcnt(0)\n\ts_barrier" ::: "memory");
    att_stage_load(kv, 4 * kh + 2, lds0 + 2 * STG_BYTES, w4, lane);
    const u32x4 z4 = {0u, 0u, 0u, 0u};
    bf16x8 qt[2][2];
#pragma unroll
    for (int h = 0; h < 2; ++h) { const u32x4 a = {qtl[h][0], qtl[h][1], 0u, 0u}; qt[h][0] = __builtin_bit_cast(bf16x8, hh ? z4 : a); qt[h][1] = __builtin_bit_cast(bf16x8, hh ? a : z4); }
    const f32x16 zero = {};
    float m[2]; bool unsafe = false;
    const float lim = 112.f - fmaxf(0.f, __builtin_amdgcn_logf(fmaxf(vmax, 1.f)));
    { const lds_cptr kb = (lds_cptr)ldsh + lane * 16; const bf16x8 k0 = lds128(kb + STG_KM), kt = lds128(kb + STG_KT);
#pragma unroll
      for (int h = 0; h < 2; ++h) {
        f32x16 sc = MFMA32(k0, qm[h], zero); sc = MFMA32(kt, qt[h][0], sc);
        float mm = sc[0];
#pragma unroll
        for (int i = 1; i < 16; ++i) mm = fmaxf(mm, sc[i]);
        mm = fmaxf(mm, __shfl_xor(mm, 32));
        m[h] = bf2f(f2bf(mm));
        unsafe |= !(__builtin_sqrtf(q2[h] * kmax2) * 1.001f + 1.f - m[h] <= lim);
      } }
    if (__any(unsafe) && lane == 0) ctl[0] = 1u;
    LBAR();
    const bool fallback = ctl[0] != 0u;
    if (fallback) {
        att_maxpass(kv, ldsh, kh, w4, lane, qm, qt, m);
#pragma unroll
        for (int h = 0; h < 2; ++h) m[h] = bf2f(f2bf(fmaxf(m[h], __shfl_xor(m[h], 32))));
    }
#pragma unroll
    for (int h = 0; h < 2; ++h) { const unsigned nm = f2bf(-m[h]); const u32x4 a = {qtl[h][0], qtl[h][1], nm, 0u}; qt[h][0] = __builtin_bit_cast(bf16x8, hh ? z4 : a); qt[h][1] = __builtin_bit_cast(bf16x8, hh ? a : z4); }
    f32x16 O[2] = {{}, {}};
    att_main(kv, ldsh, ctl + 2 + 2 * kh, kh, w4, lane, qm, qt, O[0], O[1]);
    LBAR();
    LAS float* xch = (LAS float*)lds + (w4 * 2 + kh) * (13 * 64);
    LAS float* xin = (LAS float*)lds + (w4 * 2 + (1 - kh)) * (13 * 64);
#pragma unroll
    for (int i = 0; i < 12; ++i) xch[i * 64 + lane] = kh ? O[0][i] : O[1][i];
    xch[12 * 64 + lane] = kh ? m[0] : m[1];
    LBAR();
    {
        const float mo = kh ? m[1] : m[0], m1 = xin[12 * 64 + lane];
        const float M = fmaxf(mo, m1), f0 = EXP2(mo - M), f1 = EXP2(m1 - M);
        float o[12];
#pragma unroll
        for (int i = 0; i < 12; ++i) o[i] = (kh ? O[1][i] : O[0][i]) * f0 + xin[i * 64 + lane] * f1;
        const float l = __shfl(o[8], r + 32);
        const float inv = __builtin_amdgcn_rcpf(l);
        u16* Og = (u16*)(ws + WS_O) + (size_t)(s0 + r) * 1280 + 640 + (2 * c + kh) * DH;
        u32x2 w0 = {pk2(o[0] * inv, o[1] * inv), pk2(o[2] * inv, o[3] * inv)}; *(u32x2*)(Og + 4 * hh) = w0;
        u32x2 w1 = {pk2(o[4] * inv, o[5] * inv), pk2(o[6] * inv, o[7] * inv)}; *(u32x2*)(Og + 8 + 4 * hh) = w1;
        if (hh == 0) { u32x2 w2 = {pk2(o[8] * inv, o[9] * inv), pk2(o[10] * inv, o[11] * inv)}; *(u32x2*)(Og + 16) = w2; }
    }
}

constexpr int C_STG = 40960, C_LDS = 3 * C_STG;
__global__ __launch_bounds__(512, 2) void k_out(Inputs in, char* ws, u16* out) {
    extern __shared__ __attribute__((aligned(16))) char lds[];
    const int tid = threadIdx.x, lane = tid & 63, wid = __builtin_amdgcn_readfirstlane(tid >> 6);
    const int b = blockIdx.x, xg = b & 7, li = b >> 3;
    const int rt64 = xg * 4 + (li >> 3), ct = li & 7;
    const int s0 = 64 * rt64, n0 = 80 * ct;
    const unsigned lds0 = (unsigned)(uintptr_t)lds;
    const int rh = wid & 1, kq = wid >> 1, i16 = lane & 15, g = lane >> 4, q4 = i16 >> 2, p4 = i16 & 3;
    const char* src[5];
#pragma unroll
    for (int j = 0; j < 5; ++j) {
        const int p = (wid + 8 * j) * 64 + lane, half = p & 1, rowp = (p >> 1) & 7, sub = p >> 4, qq = sub / 40, s2 = sub - qq * 40, kb = s2 / 5, nb = s2 - kb * 5;
        const int k = 320 * qq + 8 * kb + (rowp ^ (4 * (kb & 1)));
        const u16* w = k < 640 ? in.woA + (size_t)k * 640 : in.woB + (size_t)(k - 640) * 640;
        src[j] = (const char*)(w + n0 + 16 * nb + 8 * half);
    }
#define C_ISSUE(buf) do { _Pragma("unroll") for (int j = 0; j < 5; ++j) { glds16(src[j], rfl(lds0 + (buf) * C_STG + (wid + 8 * j) * 1024)); src[j] += 64 * 640 * 2; } } while (0)
    C_ISSUE(0); C_ISSUE(1);
    bf16x8 of[2][10];
#pragma unroll
    for (int i = 0; i < 2; ++i) { const u16* orow = (const u16*)(ws + WS_O) + (size_t)(s0 + 32 * rh + 16 * i + i16) * 1280 + 320 * kq + 8 * g;
#pragma unroll
        for (int ks = 0; ks < 10; ++ks) of[i][ks] = *(const bf16x8*)(orow + 32 * ks); }
    float gt[2];
#pragma unroll
    for (int i = 0; i < 2; ++i) { const float gg = ((const float*)(ws + WS_G))[s0 + 32 * rh + 16 * i + i16]; gt[i] = kq < 2 ? gg : 1.f - gg; }
    const lds_cptr wb0 = (lds_cptr)lds + kq * 10240 + g * 1280 + (q4 + 4 * (0 ^ (g & 1))) * 32 + p4 * 8;
    const lds_cptr wb1 = (lds_cptr)lds + kq * 10240 + g * 1280 + (q4 + 4 * (1 ^ (g & 1))) * 32 + p4 * 8;
    f32x4 acc[2][5];
#pragma unroll
    for (int i = 0; i < 2; ++i)
#pragma unroll
        for (int j = 0; j < 5; ++j) acc[i][j] = (f32x4){0.f, 0.f, 0.f, 0.f};
#pragma unroll
    for (int t = 0; t < 5; ++t) {
        if (t < 4) WAIT_BAR(5); else WAIT_BAR(0);
        if (t + 2 < 5) C_ISSUE((t + 2) % 3);
        const int bo = (t % 3) * C_STG;
#pragma unroll
        for (int ks = 0; ks < 2; ++ks)
#pragma unroll
            for (int j = 0; j < 5; ++j) {
                const bf16x8 wf = ldstr2(wb0 + bo + ks * 5120 + j * 256, wb1 + bo + ks * 5120 + j * 256);
                acc[0][j] = MFMA16(wf, of[0][2 * t + ks], acc[0][j]); acc[1][j] = MFMA16(wf, of[1][2 * t + ks], acc[1][j]);
            }
    }
    LBAR();
    LAS f32x4* red = (LAS f32x4*)lds;
#pragma unroll
    for (int i = 0; i < 2; ++i)
#pragma unroll
        for (int j = 0; j < 5; ++j) red[((rh * 4 + kq) * 10 + i * 5 + j) * 64 + lane] = acc[i][j] * gt[i];
    LBAR();
#pragma unroll
    for (int u = 0; u < 3; ++u) {
        const int tile = kq + 4 * u;
        if (tile < 10) {
            const int i = tile / 5, j = tile - 5 * i;
            f32x4 a = red[((rh * 4 + 0) * 10 + tile) * 64 + lane];
#pragma unroll
            for (int qq = 1; qq < 4; ++qq) a += red[((rh * 4 + qq) * 10 + tile) * 64 + lane];
            *(u32x2*)(out + (size_t)(s0 + 32 * rh + 16 * i + i16) * 640 + n0 + 16 * j + 4 * g) = (u32x2){pk2(a[0], a[1]), pk2(a[2], a[3])};
        }
    }
}

extern "C" void kernel_launch(void* const* d_in, const int* in_sizes, int n_in, void* d_out, int out_size, void* d_ws, size_t ws_size, hipStream_t stream) {
    Inputs in; in.x = (const u16*)d_in[0]; in.wqkv = (const u16*)d_in[1]; in.woA = (const u16*)d_in[2]; in.wq = (const u16*)d_in[4]; in.wk = (const u16*)d_in[5]; in.wv = (const u16*)d_in[6]; in.woB = (const u16*)d_in[7]; in.gw = (const u16*)d_in[8];
    char* ws = (char*)d_ws;
    (void)hipFuncSetAttribute((const void*)k_proj, hipFuncAttributeMaxDynamicSharedMemorySize, A_LDS);
    (void)hipFuncSetAttribute((const void*)k_attn, hipFuncAttributeMaxDynamicSharedMemorySize, B_LDS);
    (void)hipFuncSetAttribute((const void*)k_out, hipFuncAttributeMaxDynamicSharedMemorySize, C_LDS);
    k_proj<<<256, 512, A_LDS, stream>>>(in, ws);
    k_attn<<<256, 512, B_LDS, stream>>>(ws);
    k_out<<<256, 512, C_LDS, stream>>>(in, ws, (u16*)d_out);
}
#define ATT_LAUNCH(ws, stream) k_attn<<<256, 512, B_LDS, stream>>>(ws)
```

```cpp
#include <hip/hip_runtime.h>
#include <stdint.h>

typedef unsigned short u16;
typedef unsigned long long u64;
typedef short bf16x8 __attribute__((ext_vector_type(8)));
typedef short s16x4 __attribute__((ext_vector_type(4)));
typedef float f32x16 __attribute__((ext_vector_type(16)));
typedef float f32x4 __attribute__((ext_vector_type(4)));
typedef float f32x2 __attribute__((ext_vector_type(2)));
typedef unsigned u32x4 __attribute__((ext_vector_type(4)));
typedef unsigned u32x2 __attribute__((ext_vector_type(2)));
typedef __bf16 bf2_t __attribute__((ext_vector_type(2)));
#define LAS __attribute__((address_space(3)))
#define GAS __attribute__((address_space(1)))
typedef LAS const char* lds_cptr;
typedef LAS char* lds_ptr;
typedef __amdgpu_buffer_rsrc_t rsrc_t;

__device__ __forceinline__ float bf2f(u16 b) { return __uint_as_float(((unsigned)b) << 16); }
__device__ __forceinline__ unsigned pk2(float lo, float hi) { f32x2 v = {lo, hi}; bf2_t r = __builtin_convertvector(v, bf2_t); return __builtin_bit_cast(unsigned, r); }
__device__ __forceinline__ u16 f2bf(float f) { return (u16)(pk2(f, 0.f) & 0xffffu); }
__device__ __forceinline__ float lo2f(unsigned u) { return __uint_as_float(u << 16); }
__device__ __forceinline__ float hi2f(unsigned u) { return __uint_as_float(u & 0xffff0000u); }
__device__ __forceinline__ float dot2(unsigned a, unsigned b, float c) { return __builtin_amdgcn_fdot2_f32_bf16(__builtin_bit_cast(bf2_t, a), __builtin_bit_cast(bf2_t, b), c, false); }

constexpr int S = 2048, D = 640, H = 32, DH = 20;
constexpr float SCALE = 0.22360679774997896f, LOG2E = 1.4426950408889634f;
constexpr float QSCALE = SCALE * LOG2E;

constexpr int STG_KM = 0, STG_KT = 8192, STG_VT = 12288, VT_ROW = 528, STG_BYTES = 24576;
constexpr int WS_EPOCH = 0;
constexpr int WS_FKV = 256;
constexpr int WS_FO = WS_FKV + 2048;
constexpr int WS_NRM = 8192;
constexpr int WS_G = WS_NRM + 2048;
constexpr int WS_KV = 32768;
constexpr int WS_O = WS_KV + 8 * STG_BYTES;
constexpr int WS_END = WS_O + S * 1280 * 2;
constexpr int AUX_SC1 = 16;

#define MFMA32(a, b, c) __builtin_amdgcn_mfma_f32_32x32x16_bf16((a), (b), (c), 0, 0, 0)
#define MFMA16(a, b, c) __builtin_amdgcn_mfma_f32_16x16x32_bf16((a), (b), (c), 0, 0, 0)
#define SBAR() __builtin_amdgcn_sched_barrier(0)
#define EXP2(x) __builtin_amdgcn_exp2f(x)
#define WAIT_BAR(N) asm volatile("s_waitcnt vmcnt(" #N ")\n\ts_barrier" ::: "memory")
#define LBAR() asm volatile("s_waitcnt lgkmcnt(0)\n\ts_barrier" ::: "memory")
#define FULLBAR() asm volatile("s_waitcnt vmcnt(0) lgkmcnt(0)\n\ts_barrier" ::: "memory")
__device__ __forceinline__ int swap23(int x) { return (x & ~12) | ((x & 4) << 1) | ((x & 8) >> 1); }
__device__ __forceinline__ void glds16(const void* g, unsigned lds_base) {
    unsigned sv; asm volatile("s_mov_b32 %0, m0\n\ts_mov_b32 m0, %2\n\ts_nop 0\n\tglobal_load_lds_dwordx4 %1, off\n\ts_mov_b32 m0, %0" : "=&s"(sv) : "v"(g), "s"(lds_base) : "memory"); }
__device__ __forceinline__ bf16x8 lds128(lds_cptr p) { return *(const LAS bf16x8*)p; }
typedef short v4i16_t __attribute__((ext_vector_type(4)));
__device__ __forceinline__ s16x4 ldstr(lds_cptr p) { return __builtin_bit_cast(s16x4, __builtin_amdgcn_ds_read_tr16_b64_v4i16((LAS v4i16_t*)p)); }
__device__ __forceinline__ bf16x8 ldstr2(lds_cptr p0, lds_cptr p1) { const s16x4 a = ldstr(p0), b = ldstr(p1); return __builtin_shufflevector(a, b, 0, 1, 2, 3, 4, 5, 6, 7); }
__device__ __forceinline__ unsigned rfl(unsigned x) { return (unsigned)__builtin_amdgcn_readfirstlane(x); }
__device__ __forceinline__ void st32(rsrc_t r, int off, unsigned v) { __builtin_amdgcn_raw_buffer_store_b32(v, r, off, 0, AUX_SC1); }
__device__ __forceinline__ void st64(rsrc_t r, int off, u32x2 v) { __builtin_amdgcn_raw_buffer_store_b64(v, r, off, 0, AUX_SC1); }
__device__ __forceinline__ void st128(rsrc_t r, int off, u32x4 v) { __builtin_amdgcn_raw_buffer_store_b128(v, r, off, 0, AUX_SC1); }
__device__ __forceinline__ unsigned ld32(rsrc_t r, int off) { return __builtin_amdgcn_raw_buffer_load_b32(r, off, 0, AUX_SC1); }
__device__ __forceinline__ u32x2 ld64(rsrc_t r, int off) { return __builtin_amdgcn_raw_buffer_load_b64(r, off, 0, AUX_SC1); }
__device__ __forceinline__ u32x4 ld128(rsrc_t r, int off) { return __builtin_amdgcn_raw_buffer_load_b128(r, off, 0, AUX_SC1); }

#define PSTAMP(i)
#define PDECL
#define PFLUSH()

struct Params { const u16 *x, *wqkv, *woA, *wq, *wk, *wv, *woB, *gw; char* ws; u16* out; };

constexpr int LDS_TOTAL = 163840, CTL = LDS_TOTAL - 64;
constexpr int F_W = 0, F_WS = 20480, F_X = 81920, F_XS = 10240, F_P0W = 112640, F_P0X = 138304;
constexpr int ST_QB = 49152, ST_QK = 62464, ST_V = 94464, QB_ROW = 208, QK_ROW = 400, V_ROW = 336;
constexpr int C_STG = 40960;
constexpr unsigned SPIN_CAP = 1u << 18;

template <int N>
__device__ __forceinline__ bool poll_tags(rsrc_t r, int off, bool active, u64 tag) {
    for (unsigned spins = 0; spins < SPIN_CAP; ++spins) {
        bool ok = true;
        if (active) {
#pragma unroll
            for (int i = 0; i < N; ++i) { const u32x2 v = ld64(r, off + 8 * i); ok &= (v[0] == (unsigned)tag) & (v[1] == (unsigned)(tag >> 32)); }
        }
        if (__all(ok)) return true;
        __builtin_amdgcn_s_sleep(2);
    }
    return false;
}

__device__ __forceinline__ void phase0(const Params& P, rsrc_t rw, char* lds, int b, int wid, int lane) {
    const int rt16 = b >> 1, part = b & 1, r0 = rt16 * 16;
    const int i16 = lane & 15, g = lane >> 4;
    const int ks0 = r0 & 255; const int img = WS_KV + (r0 >> 8) * STG_BYTES;
    if (wid < 2 || (wid == 2 && part == 0)) {
        const lds_cptr xb = (lds_cptr)lds + F_P0X + i16 * 1280;
        f32x4 acc = {0.f, 0.f, 0.f, 0.f};
#define P0X(ks) lds128(xb + ((((4 * (ks) + g) & ~15) | (((4 * (ks) + g) & 15) ^ i16)) * 16))
        if (wid == 2) {
            const bf16x8 z = {0, 0, 0, 0, 0, 0, 0, 0};
#pragma unroll
            for (int ks = 0; ks < 20; ++ks) { const bf16x8 gv = *(const bf16x8*)(P.gw + 32 * ks + 8 * g); acc = MFMA16(i16 == 0 ? gv : z, P0X(ks), acc); }
            if (g == 0) st32(rw, WS_G + (r0 + i16) * 4, __float_as_uint(1.f / (1.f + __expf(-acc[0]))));
        } else {
            const int q = i16 >> 2, p = i16 & 3;
            const lds_cptr wb = (lds_cptr)lds + F_P0W + (8 * g + q) * 40 + (16 * wid + 4 * p) * 2;
            if (part == 0) {
#pragma unroll
                for (int ks = 0; ks < 20; ++ks) acc = MFMA16(ldstr2(wb + ks * 1280, wb + ks * 1280 + 160), P0X(ks), acc);
                const unsigned w0 = pk2(acc[0], acc[1]), w1 = pk2(acc[2], acc[3]);
                const int key = ks0 + i16, tile = key >> 5, r = swap23(key & 31);
                float n2 = lo2f(w0) * lo2f(w0) + hi2f(w0) * hi2f(w0) + lo2f(w1) * lo2f(w1) + hi2f(w1) * hi2f(w1);
                if (wid == 0) {
                    st64(rw, img + STG_KM + tile * 1024 + (g >> 1) * 512 + r * 16 + (g & 1) * 8, (u32x2){w0, w1});
                    n2 += __shfl_xor(n2, 16); n2 += __shfl_xor(n2, 32);
                } else {
                    if (g == 0) st128(rw, img + STG_KT + (tile >> 1) * 1024 + (tile & 1) * 512 + r * 16, (u32x4){w0, w1, 0x3f80u, 0u});
                    if (g != 0) n2 = 0.f;
                }
#pragma unroll
                for (int o = 1; o < 64; o <<= 1) n2 = fmaxf(n2, __shfl_xor(n2, o));
                if (lane == 0) st32(rw, WS_NRM + rt16 * 16 + wid * 4, __float_as_uint(n2));
            } else {
#pragma unroll
                for (int ks = 0; ks < 20; ++ks) acc = MFMA16(P0X(ks), ldstr2(wb + ks * 1280, wb + ks * 1280 + 160), acc);
                unsigned w0 = pk2(acc[0], acc[1]), w1 = pk2(acc[2], acc[3]);
                const int d = 16 * wid + i16;
                float vm = fmaxf(fmaxf(fabsf(lo2f(w0)), fabsf(hi2f(w0))), fmaxf(fabsf(lo2f(w1)), fabsf(hi2f(w1))));
                if (d >= 20) vm = 0.f;
                if (d == 20) { w0 = 0x3f803f80u; w1 = w0; }
                if (d <= 20) st64(rw, img + STG_VT + d * VT_ROW + (ks0 + 4 * g) * 2, (u32x2){w0, w1});
                if (wid == 1 && ks0 == 0 && lane < 40) st128(rw, img + STG_VT + 21 * VT_ROW + lane * 16, (u32x4){0u, 0u, 0u, 0u});
#pragma unroll
                for (int o = 1; o < 64; o <<= 1) vm = fmaxf(vm, __shfl_xor(vm, o));
                if (lane == 0) st32(rw, WS_NRM + rt16 * 16 + (2 + wid) * 4, __float_as_uint(vm));
            }
        }
#undef P0X
        asm volatile("s_waitcnt vmcnt(0)" ::: "memory");
    }
}

__device__ __forceinline__ void att_stage_load(const char* kv, int st, unsigned ldsbuf, int w4, int lane) {
    const char* src = kv + (size_t)st * STG_BYTES + lane * 16;
#pragma unroll
    for (int i = 0; i < 6; ++i) { const int piece = w4 + 4 * i; glds16(src + piece * 1024, rfl(ldsbuf + piece * 1024)); }
}
__device__ __forceinline__ void lds_signal(LAS unsigned* cnt, int lane) { if (lane == 0) __hip_atomic_fetch_add(cnt, 1u, __ATOMIC_RELAXED, __HIP_MEMORY_SCOPE_WORKGROUP); }
__device__ __forceinline__ void lds_wait(LAS unsigned* cnt, unsigned n) {
    while (rfl(__hip_atomic_load(cnt, __ATOMIC_RELAXED, __HIP_MEMORY_SCOPE_WORKGROUP)) < n) __builtin_amdgcn_s_sleep(1);
    asm volatile("" ::: "memory");
}
constexpr int KVB = 2 * STG_BYTES;

__device__ __forceinline__ void att_maxpass(const char* kv, char* ldsh, int kh, int w4, int lane, const bf16x8 (&qm)[2], const bf16x8 (&qt)[2][2], float (&mx)[2]) {
    const unsigned lds0 = (unsigned)(uintptr_t)ldsh;
    const lds_cptr kbase = (lds_cptr)ldsh + lane * 16;
    const f32x16 zero = {};
    WAIT_BAR(0);
#pragma unroll 1
    for (int st = 0; st < 4; ++st) {
        if (st == 3) { WAIT_BAR(0); att_stage_load(kv, 4 * kh + 3, lds0, w4, lane); WAIT_BAR(0); }
        const lds_cptr kb = kbase + (st == 3 ? 0 : st) * KVB;
#pragma unroll 1
        for (int pr = 0; pr < 4; ++pr) {
            const bf16x8 kt = lds128(kb + STG_KT + pr * 1024), k0 = lds128(kb + STG_KM + (2 * pr) * 1024), k1 = lds128(kb + STG_KM + (2 * pr + 1) * 1024);
#pragma unroll
            for (int h = 0; h < 2; ++h) {
                f32x16 s0 = MFMA32(k0, qm[h], zero); s0 = MFMA32(kt, qt[h][0], s0);
                f32x16 s1 = MFMA32(k1, qm[h], zero); s1 = MFMA32(kt, qt[h][1], s1);
#pragma unroll
                for (int i = 0; i < 16; ++i) mx[h] = fmaxf(mx[h], fmaxf(s0[i], s1[i]));
            }
        }
    }
    WAIT_BAR(0);
    att_stage_load(kv, 4 * kh, lds0, w4, lane);
    WAIT_BAR(0);
}

__device__ __forceinline__ void att_main(const char* kv, char* ldsh, LAS unsigned* cnt, int kh, int w4, int lane, const bf16x8 (&qm)[2], const bf16x8 (&qt)[2][2], f32x16& O0, f32x16& O1) {
    const unsigned lds0 = (unsigned)(uintptr_t)ldsh;
    const int r = lane & 31, hh = lane >> 5;
    const int voff = r <= 20 ? r * VT_ROW : (r < 28 ? 21 * VT_ROW : 21 * VT_ROW + 112);
    const lds_cptr kbase = (lds_cptr)ldsh + lane * 16;
    const lds_cptr vbase = (lds_cptr)ldsh + STG_VT + voff + hh * 16;
    const f32x16 zero = {};
    f32x16 sA, sB; u32x4 pA0, pA1, pB0, pB1; bf16x8 kmA, kmB, ktA, ktB, vA0, vA1, vB0, vB1;
    ktA = lds128(kbase + STG_KT); kmA = lds128(kbase + STG_KM);
    sA = MFMA32(kmA, qm[0], zero); sA = MFMA32(ktA, qt[0][0], sA);
    pB0 = (u32x4){0u, 0u, 0u, 0u}; pB1 = pB0; vB0 = kmA; vB1 = kmA; ktB = ktA; vA0 = kmA; vA1 = kmA; kmB = kmA;
    float ee0 = 0.f, ee1 = 0.f, ee2 = 0.f, ee3 = 0.f;
#define GRP(MF, LD, SC, I, PD, J) do { MF; LD; const float e0_ = EXP2(SC[I]); unsigned a_ = pk2(ee0, ee1); const float e1_ = EXP2(SC[I + 1]); unsigned b_ = pk2(ee2, ee3); const float e2_ = EXP2(SC[I + 2]), e3_ = EXP2(SC[I + 3]); \
        ee0 = e0_; ee1 = e1_; ee2 = e2_; ee3 = e3_; asm volatile("" : "+v"(a_), "+v"(b_), "+v"(ee0), "+v"(ee1), "+v"(ee2), "+v"(ee3)); PD[J] = a_; PD[J + 1] = b_; SBAR(); } while (0)
#define TILE(KM, KMN, KT, VJ, KTN, VJN, VC0, VC1, VP0, VP1, VADDR, LDK, LDT) do { \
        GRP(sB = MFMA32(KM, qm[1], zero), VC0 = lds128(VADDR), sA, 0, pB1, 2); \
        GRP(O1 = MFMA32(VP0, __builtin_bit_cast(bf16x8, pB0), O1), VC1 = lds128((VADDR) + 32), sA, 4, pA0, 0); \
        GRP(sB = MFMA32(KT, qt[1][VJ], sB), LDK, sA, 8, pA0, 2); \
        GRP(O1 = MFMA32(VP1, __builtin_bit_cast(bf16x8, pB1), O1), LDT, sA, 12, pA1, 0); \
        GRP(sA = MFMA32(KMN, qm[0], zero), (void)0, sB, 0, pA1, 2); \
        GRP(O0 = MFMA32(VC0, __builtin_bit_cast(bf16x8, pA0), O0), (void)0, sB, 4, pB0, 0); \
        GRP(sA = MFMA32(KTN, qt[0][VJN], sA), (void)0, sB, 8, pB0, 2); \
        GRP(O0 = MFMA32(VC1, __builtin_bit_cast(bf16x8, pA1), O0), (void)0, sB, 12, pB1, 0); } while (0)
#pragma unroll 1
    for (int st = 0; st < 4; ++st) {
        const int buf = st == 3 ? 0 : st, nbuf = st == 2 ? 0 : (st == 3 ? 1 : st + 1);
        const lds_cptr kb = kbase + buf * KVB, vb = vbase + buf * KVB, kn = kbase + nbuf * KVB;
        TILE(kmA, kmB, ktA, 0, ktA, 1, vA0, vA1, vB0, vB1, vb + 0 * 64, kmB = lds128(kb + STG_KM + 1 * 1024), (void)0);
        if (st < 3) {
            asm volatile("s_waitcnt vmcnt(0)" ::: "memory"); lds_signal(cnt + st, lane); SBAR();
        }
        TILE(kmB, kmA, ktA, 1, ktB, 0, vB0, vB1, vA0, vA1, vb + 1 * 64, kmA = lds128(kb + STG_KM + 2 * 1024), ktB = lds128(kb + STG_KT + 1024));
        TILE(kmA, kmB, ktB, 0, ktB, 1, vA0, vA1, vB0, vB1, vb + 2 * 64, kmB = lds128(kb + STG_KM + 3 * 1024), (void)0);
        TILE(kmB, kmA, ktB, 1, ktA, 0, vB0, vB1, vA0, vA1, vb + 3 * 64, kmA = lds128(kb + STG_KM + 4 * 1024), ktA = lds128(kb + STG_KT + 2048));
        TILE(kmA, kmB, ktA, 0, ktA, 1, vA0, vA1, vB0, vB1, vb + 4 * 64, kmB = lds128(kb + STG_KM + 5 * 1024), (void)0);
        TILE(kmB, kmA, ktA, 1, ktB, 0, vB0, vB1, vA0, vA1, vb + 5 * 64, kmA = lds128(kb + STG_KM + 6 * 1024), ktB = lds128(kb + STG_KT + 3072));
        if (st < 3) {
            lds_wait(cnt + st, 4u);
            if (st == 1) att_stage_load(kv, 4 * kh + 3, lds0, w4, lane);
            SBAR();
        }
        TILE(kmA, kmB, ktB, 0, ktB, 1, vA0, vA1, vB0, vB1, vb + 6 * 64, kmB = lds128(kb + STG_KM + 7 * 1024), (void)0);
        TILE(kmB, kmA, ktB, 1, ktA, 0, vB0, vB1, vA0, vA1, vb + 7 * 64, kmA = lds128(kn + STG_KM + 0 * 1024), ktA = lds128(kn + STG_KT));
    }
    pB1[2] = pk2(ee0, ee1); pB1[3] = pk2(ee2, ee3);
    O1 = MFMA32(vB0, __builtin_bit_cast(bf16x8, pB0), O1);
    O1 = MFMA32(vB1, __builtin_bit_cast(bf16x8, pB1), O1);
#undef GRP
#undef TILE
}

__global__ __launch_bounds__(512, 2) void fused(Params P) {
    extern __shared__ __attribute__((aligned(16))) char lds[];
    PDECL
    PSTAMP(0);
    char* const ws = P.ws;
    const int tid = threadIdx.x, lane = tid & 63, wid = __builtin_amdgcn_readfirstlane(tid >> 6);
    const int b = blockIdx.x, xg = b & 7, li = b >> 3;
    const int rt64 = (xg >> 2) * 16 + (li >> 1), c4 = (xg & 3) * 2 + (li & 1);
    const int s0 = 64 * rt64;
    const unsigned lds0 = (unsigned)(uintptr_t)lds;
    const rsrc_t rw = __builtin_amdgcn_make_buffer_rsrc(ws, 0, WS_END, 0x00020000);
    LAS unsigned* ctl = (LAS unsigned*)((lds_ptr)lds + CTL);
    const int i16 = lane & 15, g = lane >> 4, q4 = i16 >> 2, p4 = i16 & 3;

    {
        const char* wsrc = (const char*)((b & 1) ? P.wv : P.wk) + lane * 16;
#pragma unroll
        for (int i = 0; i < 6; ++i) { const int id = wid + 8 * i;
            if (id < 25) glds16(wsrc + id * 1024, rfl(lds0 + F_P0W + id * 1024));
            else if (id < 45) { const int p = (id - 25) * 64 + lane, row = p / 80, cpos = p - row * 80, ch = (cpos & ~15) | ((cpos & 15) ^ row);
                glds16(P.x + (size_t)((b >> 1) * 16 + row) * D + ch * 8, rfl(lds0 + F_P0X + (id - 25) * 1024)); } }
    }
    const char* wsrcE[3]; const char* wsrcO[3]; unsigned wstrE[3], wstrO[3];
#pragma unroll
    for (int par = 0; par < 2; ++par)
#pragma unroll
        for (int j = 0; j < 3; ++j) {
            const int id = (wid ^ (4 * par)) + 8 * j, p = (id < 20 ? id : 0) * 64 + lane, seg = p / 320, rem = p - seg * 320, pos = rem / 10, j10 = rem - pos * 10;
            const int k = 32 * par + (pos ^ (4 * ((pos >> 3) & 1)));
            const char* sp; unsigned st;
            if (seg == 0) { sp = (const char*)(P.wq + (size_t)k * 640 + 80 * c4 + 8 * j10); st = 64 * 640 * 2; }
            else { sp = (const char*)(P.wqkv + (size_t)k * 1920 + (seg - 1) * 640 + 80 * c4 + 8 * j10); st = 64 * 1920 * 2; }
            if (par == 0) { wsrcE[j] = sp; wstrE[j] = st; } else { wsrcO[j] = sp; wstrO[j] = st; }
        }
    const char* xsrc[2];
#pragma unroll
    for (int j = 0; j < 2; ++j) { const int id = wid + 8 * j, p = (id < 10 ? id : 0) * 64 + lane, row = p >> 3, cpos = p & 7, ch = cpos ^ ((row >> 1) & 7);
        int gr = s0 - 16 + row; gr = gr < 0 ? 0 : gr; xsrc[j] = (const char*)(P.x + (size_t)gr * D + ch * 8); }
#define W_ISSUE_E(slot) do { glds16(wsrcE[0], rfl(lds0 + F_W + (slot) * F_WS + wid * 1024)); wsrcE[0] += wstrE[0]; glds16(wsrcE[1], rfl(lds0 + F_W + (slot) * F_WS + (wid + 8) * 1024)); wsrcE[1] += wstrE[1]; \
        if (wid < 4) { glds16(wsrcE[2], rfl(lds0 + F_W + (slot) * F_WS + (wid + 16) * 1024)); wsrcE[2] += wstrE[2]; } } while (0)
#define W_ISSUE_O(slot) do { glds16(wsrcO[0], rfl(lds0 + F_W + (slot) * F_WS + (wid ^ 4) * 1024)); wsrcO[0] += wstrO[0]; glds16(wsrcO[1], rfl(lds0 + F_W + (slot) * F_WS + ((wid ^ 4) + 8) * 1024)); wsrcO[1] += wstrO[1]; \
        if (wid >= 4) { glds16(wsrcO[2], rfl(lds0 + F_W + (slot) * F_WS + ((wid ^ 4) + 16) * 1024)); wsrcO[2] += wstrO[2]; } } while (0)
#define X_ISSUE(slot) do { glds16(xsrc[0], rfl(lds0 + F_X + (slot) * F_XS + wid * 1024)); xsrc[0] += 128; if (wid < 2) { glds16(xsrc[1], rfl(lds0 + F_X + (slot) * F_XS + (wid + 8) * 1024)); xsrc[1] += 128; } } while (0)
    X_ISSUE(0); W_ISSUE_E(0); W_ISSUE_O(1); X_ISSUE(1); W_ISSUE_E(2);
    const u32x2 ep = ld64(rw, WS_EPOCH);
    const u64 tag = ((u64)ep[0] | ((u64)ep[1] << 32)) + 0x9E3779B97F4A7C15ull;
    if (tid < 16) ctl[tid] = 0u;
    PSTAMP(1);
    asm volatile("s_waitcnt vmcnt(8) lgkmcnt(0)\n\ts_barrier" ::: "memory");
    phase0(P, rw, lds, b, wid, lane);
    LBAR();
    if (tid == 0) st64(rw, WS_FKV + b * 8, (u32x2){(unsigned)tag, (unsigned)(tag >> 32)});
    PSTAMP(2);

    const int seg = wid >> 1, rh = wid & 1;
    const lds_cptr wb0 = (lds_cptr)lds + F_W + seg * 5120 + (8 * g + q4 + 4 * (0 ^ (g & 1))) * 160 + p4 * 8;
    const lds_cptr wb1 = (lds_cptr)lds + F_W + seg * 5120 + (8 * g + q4 + 4 * (1 ^ (g & 1))) * 160 + p4 * 8;
    const lds_cptr xb0 = (lds_cptr)lds + F_X + i16 * 128 + ((g ^ (i16 >> 1)) * 16);
    const lds_cptr xb1 = (lds_cptr)lds + F_X + i16 * 128 + (((4 + g) ^ (i16 >> 1)) * 16);
    f32x4 acc[2][5], hacc[3];
#pragma unroll
    for (int j = 0; j < 5; ++j) { acc[0][j] = (f32x4){0.f, 0.f, 0.f, 0.f}; acc[1][j] = acc[0][j]; }
#pragma unroll
    for (int j = 0; j < 3; ++j) hacc[j] = (f32x4){0.f, 0.f, 0.f, 0.f};
    bool kv_ok = true;
#define F_COMPUTE(xb, wslot, xslot) do { \
        const int wo_ = (wslot) * F_WS, xo_ = (xslot) * F_XS; \
        const bf16x8 x0 = lds128(xb + xo_ + (1 + 2 * rh) * 2048), x1 = lds128(xb + xo_ + (2 + 2 * rh) * 2048); \
        bf16x8 wf[5]; \
        _Pragma("unroll") for (int j = 0; j < 5; ++j) wf[j] = ldstr2(wb0 + wo_ + j * 32, wb1 + wo_ + j * 32); \
        _Pragma("unroll") for (int j = 0; j < 5; ++j) { acc[0][j] = MFMA16(wf[j], x0, acc[0][j]); acc[1][j] = MFMA16(wf[j], x1, acc[1][j]); } \
        if (seg >= 2) { const bf16x8 xh = lds128(xb + xo_); \
            if (rh == 0) { hacc[0] = MFMA16(wf[0], xh, hacc[0]); hacc[1] = MFMA16(wf[1], xh, hacc[1]); hacc[2] = MFMA16(wf[2], xh, hacc[2]); } \
            else { hacc[0] = MFMA16(wf[3], xh, hacc[0]); hacc[1] = MFMA16(wf[4], xh, hacc[1]); } } } while (0)
    {
        int ws4 = 0, xs3 = 0;
#pragma unroll 1
        for (int t = 0; t < 10; ++t) {
            if (t < 9) WAIT_BAR(5); else WAIT_BAR(2);
            if (t + 2 < 10) { const int xs = xs3 == 0 ? 2 : xs3 - 1; X_ISSUE(xs); }
            if (t < 9) W_ISSUE_O((ws4 + 3) & 3);
            if (t == 6 && wid == 7) {
                kv_ok = poll_tags<4>(rw, WS_FKV + lane * 32, true, tag);
                __builtin_amdgcn_fence(__ATOMIC_ACQUIRE, "agent");
            }
            F_COMPUTE(xb0, ws4, xs3);
            if (t < 9) WAIT_BAR(5); else WAIT_BAR(0);
            if (t < 8) W_ISSUE_E((ws4 + 4) & 3);
            F_COMPUTE(xb1, (ws4 + 1) & 3, xs3);
            ws4 = (ws4 + 2) & 3; xs3 = xs3 == 2 ? 0 : xs3 + 1;
        }
    }
    PSTAMP(3);
    FULLBAR();
    const char* kv = ws + WS_KV;
    const int w4 = wid & 3, kh = wid >> 2;
    char* ldsh = lds + kh * STG_BYTES;
    const unsigned ldsh0 = lds0 + kh * STG_BYTES;
    att_stage_load(kv, 4 * kh, ldsh0, w4, lane);
    const u32x4 nr0 = ld128(rw, WS_NRM + lane * 16), nr1 = ld128(rw, WS_NRM + 1024 + lane * 16);
    if (b == 0 && tid == 448) st64(rw, WS_EPOCH, (u32x2){(unsigned)tag, (unsigned)(tag >> 32)});
    {
        lds_ptr st = (lds_ptr)lds;
        auto put = [&](const f32x4& a, int row, int j) {
            const int cq = 4 * j + g, hl = (cq * 13) >> 6, dq = cq - 5 * hl;
            if (seg == 0) *(LAS u32x2*)(st + ST_QB + (row - 16) * QB_ROW + hl * 48 + dq * 8) = (u32x2){pk2(a[0] * QSCALE, a[1] * QSCALE), pk2(a[2] * QSCALE, a[3] * QSCALE)};
            else if (seg < 3) *(LAS u32x2*)(st + ST_QK + row * QK_ROW + (seg - 1) * 192 + hl * 48 + dq * 8) = (u32x2){pk2(a[0], a[1]), pk2(a[2], a[3])};
            else *(LAS f32x4*)(st + ST_V + row * V_ROW + hl * 80 + dq * 16) = a;
        };
#pragma unroll
        for (int i = 0; i < 2; ++i)
#pragma unroll
            for (int j = 0; j < 5; ++j) put(acc[i][j], 16 + 32 * rh + 16 * i + i16, j);
        if (seg >= 2) { if (rh == 0) { put(hacc[0], i16, 0); put(hacc[1], i16, 1); put(hacc[2], i16, 2); } else { put(hacc[0], i16, 3); put(hacc[1], i16, 4); } }
    }
    LBAR();
    PSTAMP(4);
    if (tid < 256) {
        const int hl = tid >> 6, sl = tid & 63, row = 16 + sl, s = s0 + sl;
        const lds_cptr qp = (lds_cptr)lds + ST_QK + row * QK_ROW + hl * 48;
        const u32x4 qa = *(const LAS u32x4*)qp, qb = *(const LAS u32x4*)(qp + 16); const u32x2 qc = *(const LAS u32x2*)(qp + 32);
        float lg[8]; float m = -3.0e38f;
#pragma unroll
        for (int w = 0; w < 8; ++w) {
            const lds_cptr kp = qp + 192 - 2 * w * QK_ROW;
            const u32x4 ka = *(const LAS u32x4*)kp, kb = *(const LAS u32x4*)(kp + 16); const u32x2 kc = *(const LAS u32x2*)(kp + 32);
            float a = dot2(qa[0], ka[0], 0.f), a2 = dot2(qa[1], ka[1], 0.f);
            a = dot2(qa[2], ka[2], a); a2 = dot2(qa[3], ka[3], a2); a = dot2(qb[0], kb[0], a); a2 = dot2(qb[1], kb[1], a2);
            a = dot2(qb[2], kb[2], a); a2 = dot2(qb[3], kb[3], a2); a = dot2(qc[0], kc[0], a); a2 = dot2(qc[1], kc[1], a2);
            a = (a + a2) * QSCALE;
            lg[w] = (s - 2 * w >= 0) ? a : -1.0e9f * LOG2E; m = fmaxf(m, lg[w]);
        }
        float l = 0.f;
#pragma unroll
        for (int w = 0; w < 8; ++w) { lg[w] = EXP2(lg[w] - m); l += lg[w]; }
        const float inv = 1.f / l;
        f32x4 o[5];
#pragma unroll
        for (int j = 0; j < 5; ++j) o[j] = (f32x4){0.f, 0.f, 0.f, 0.f};
        const lds_cptr vp = (lds_cptr)lds + ST_V + row * V_ROW + hl * 80;
#pragma unroll
        for (int w = 0; w < 8; ++w) {
            const float pw = lg[w] * inv;
#pragma unroll
            for (int j = 0; j < 5; ++j) o[j] += pw * *(const LAS f32x4*)(vp - 2 * w * V_ROW + j * 16);
        }
        const int og = WS_O + (s * 1280 + (4 * c4 + hl) * DH) * 2;
#pragma unroll
        for (int j = 0; j < 5; ++j) st64(rw, og + 8 * j, (u32x2){pk2(o[j][0], o[j][1]), pk2(o[j][2], o[j][3])});
    }
    const int r = lane & 31, hh = lane >> 5;
    bf16x8 qm[2]; u32x2 qtl[2];
#pragma unroll
    for (int h = 0; h < 2; ++h) {
        const lds_cptr qp = (lds_cptr)lds + ST_QB + (32 * (w4 & 1) + r) * QB_ROW + (2 * (w4 >> 1) + h) * 48;
        qm[h] = lds128(qp + hh * 16); qtl[h] = *(const LAS u32x2*)(qp + 32);
    }
    float km2 = fmaxf(__uint_as_float(nr0[0]), __uint_as_float(nr1[0])), kt2 = fmaxf(__uint_as_float(nr0[1]), __uint_as_float(nr1[1]));
    float vmax = fmaxf(fmaxf(__uint_as_float(nr0[2]), __uint_as_float(nr1[2])), fmaxf(__uint_as_float(nr0[3]), __uint_as_float(nr1[3])));
#pragma unroll
    for (int o = 1; o < 64; o <<= 1) { km2 = fmaxf(km2, __shfl_xor(km2, o)); kt2 = fmaxf(kt2, __shfl_xor(kt2, o)); vmax = fmaxf(vmax, __shfl_xor(vmax, o)); }
    const float kmax2 = km2 + kt2;
    float q2[2];
#pragma unroll
    for (int h = 0; h < 2; ++h) {
        q2[h] = 0.f;
#pragma unroll
        for (int i = 0; i < 8; ++i) { const float v = bf2f((u16)qm[h][i]); q2[h] += v * v; }
        if (hh == 0) { const float a0 = lo2f(qtl[h][0]), a1 = hi2f(qtl[h][0]), a2 = lo2f(qtl[h][1]), a3 = hi2f(qtl[h][1]); q2[h] += a0 * a0 + a1 * a1 + a2 * a2 + a3 * a3; }
        q2[h] += __shfl_xor(q2[h], 32);
    }
    FULLBAR();
    PSTAMP(5);
    att_stage_load(kv, 4 * kh + 1, ldsh0 + KVB, w4, lane); att_stage_load(kv, 4 * kh + 2, ldsh0 + 2 * KVB, w4, lane);

    const u32x4 z4 = {0u, 0u, 0u, 0u};
    bf16x8 qt[2][2];
#pragma unroll
    for (int h = 0; h < 2; ++h) { const u32x4 a = {qtl[h][0], qtl[h][1], 0u, 0u}; qt[h][0] = __builtin_bit_cast(bf16x8, hh ? z4 : a); qt[h][1] = __builtin_bit_cast(bf16x8, hh ? a : z4); }
    const f32x16 zero = {};
    float m[2]; bool unsafe = false;
    const float lim = 112.f - fmaxf(0.f, __builtin_amdgcn_logf(fmaxf(vmax, 1.f)));
    { const lds_cptr kb = (lds_cptr)ldsh + lane * 16; const bf16x8 k0 = lds128(kb + STG_KM), kt = lds128(kb + STG_KT);
#pragma unroll
      for (int h = 0; h < 2; ++h) {
        f32x16 sc = MFMA32(k0, qm[h], zero); sc = MFMA32(kt, qt[h][0], sc);
        float mm = sc[0];
#pragma unroll
        for (int i = 1; i < 16; ++i) mm = fmaxf(mm, sc[i]);
        mm = fmaxf(mm, __shfl_xor(mm, 32));
        m[h] = bf2f(f2bf(mm));
        unsafe |= !(__builtin_sqrtf(q2[h] * kmax2) * 1.001f + 1.f - m[h] <= lim);
      } }
    if (__any(unsafe) && lane == 0) ctl[0] = 1u;
    LBAR();
    const bool fallback = ctl[0] != 0u;
    if (fallback) {
        att_maxpass(kv, ldsh, kh, w4, lane, qm, qt, m);
#pragma unroll
        for (int h = 0; h < 2; ++h) m[h] = bf2f(f2bf(fmaxf(m[h], __shfl_xor(m[h], 32))));
    }
#pragma unroll
    for (int h = 0; h < 2; ++h) { const unsigned nm = f2bf(-m[h]); const u32x4 a = {qtl[h][0], qtl[h][1], nm, 0u}; qt[h][0] = __builtin_bit_cast(bf16x8, hh ? z4 : a); qt[h][1] = __builtin_bit_cast(bf16x8, hh ? a : z4); }
    f32x16 O[2] = {{}, {}};
    PSTAMP(6);
    att_main(kv, ldsh, ctl + 2 + 4 * kh, kh, w4, lane, qm, qt, O[0], O[1]);
    PSTAMP(7);
    LBAR();
    const int n0 = 80 * c4;
    const char* csrc[5];
#pragma unroll
    for (int j = 0; j < 5; ++j) {
        const int p = (wid + 8 * j) * 64 + lane, half = p & 1, rowp = (p >> 1) & 7, sub = p >> 4, qq = sub / 40, s2 = sub - qq * 40, kb = s2 / 5, nb = s2 - kb * 5;
        const int k = 320 * qq + 8 * kb + (rowp ^ (4 * (kb & 1)));
        const u16* w = k < 640 ? P.woA + (size_t)k * 640 : P.woB + (size_t)(k - 640) * 640;
        csrc[j] = (const char*)(w + n0 + 16 * nb + 8 * half);
    }
#define C_ISSUE(buf) do { _Pragma("unroll") for (int j = 0; j < 5; ++j) { glds16(csrc[j], rfl(lds0 + (buf) * C_STG + (wid + 8 * j) * 1024)); csrc[j] += 64 * 640 * 2; } } while (0)
    C_ISSUE(1); C_ISSUE(2);
    {
        LAS float* xch = (LAS float*)lds + (w4 * 2 + kh) * (13 * 64);
        LAS float* xin = (LAS float*)lds + (w4 * 2 + (1 - kh)) * (13 * 64);
#pragma unroll
        for (int i = 0; i < 12; ++i) xch[i * 64 + lane] = kh ? O[0][i] : O[1][i];
        xch[12 * 64 + lane] = kh ? m[0] : m[1];
        LBAR();
        const float mo = kh ? m[1] : m[0], m1 = xin[12 * 64 + lane];
        const float M = fmaxf(mo, m1), f0 = EXP2(mo - M), f1 = EXP2(m1 - M);
        float o[12];
#pragma unroll
        for (int i = 0; i < 12; ++i) o[i] = (kh ? O[1][i] : O[0][i]) * f0 + xin[i * 64 + lane] * f1;
        const float l = __shfl(o[8], r + 32);
        const float inv = __builtin_amdgcn_rcpf(l);
        const int og = WS_O + ((s0 + 32 * (w4 & 1) + r) * 1280 + 640 + (4 * c4 + 2 * (w4 >> 1) + kh) * DH) * 2;
        st64(rw, og + 8 * hh, (u32x2){pk2(o[0] * inv, o[1] * inv), pk2(o[2] * inv, o[3] * inv)});
        st64(rw, og + 16 + 8 * hh, (u32x2){pk2(o[4] * inv, o[5] * inv), pk2(o[6] * inv, o[7] * inv)});
        if (hh == 0) st64(rw, og + 32, (u32x2){pk2(o[8] * inv, o[9] * inv), pk2(o[10] * inv, o[11] * inv)});
    }
    FULLBAR();
    if (tid == 0) st64(rw, WS_FO + (rt64 * 8 + c4) * 8, (u32x2){(unsigned)tag, (unsigned)(tag >> 32)});
    PSTAMP(8);

    C_ISSUE(0);
    bool o_ok = true;
    if (wid == 0) o_ok = poll_tags<1>(rw, WS_FO + (rt64 * 8 + (lane & 7)) * 8, lane < 8, tag);
    asm volatile("s_barrier" ::: "memory");
    PSTAMP(9);
    const int crh = wid & 1, kq = wid >> 1;
    bf16x8 of[2][10];
#pragma unroll
    for (int i = 0; i < 2; ++i) { const int orow = WS_O + ((s0 + 32 * crh + 16 * i + i16) * 1280 + 320 * kq + 8 * g) * 2;
#pragma unroll
        for (int ks = 0; ks < 10; ++ks) of[i][ks] = __builtin_bit_cast(bf16x8, ld128(rw, orow + 64 * ks)); }
    float gt[2];
#pragma unroll
    for (int i = 0; i < 2; ++i) { const float gg = __uint_as_float(ld32(rw, WS_G + (s0 + 32 * crh + 16 * i + i16) * 4)); gt[i] = kq < 2 ? gg : 1.f - gg; }
    const lds_cptr cb0 = (lds_cptr)lds + kq * 10240 + g * 1280 + (q4 + 4 * (0 ^ (g & 1))) * 32 + p4 * 8;
    const lds_cptr cb1 = (lds_cptr)lds + kq * 10240 + g * 1280 + (q4 + 4 * (1 ^ (g & 1))) * 32 + p4 * 8;
    f32x4 ca[2][5];
#pragma unroll
    for (int i = 0; i < 2; ++i)
#pragma unroll
        for (int j = 0; j < 5; ++j) ca[i][j] = (f32x4){0.f, 0.f, 0.f, 0.f};
#pragma unroll
    for (int t = 0; t < 5; ++t) {
        WAIT_BAR(0);
        if (t >= 1 && t + 2 < 5) C_ISSUE((t + 2 + 1) % 3);
        const int bo = ((t + 1) % 3) * C_STG;
#pragma unroll
        for (int ks = 0; ks < 2; ++ks)
#pragma unroll
            for (int j = 0; j < 5; ++j) {
                const bf16x8 wf = ldstr2(cb0 + bo + ks * 5120 + j * 256, cb1 + bo + ks * 5120 + j * 256);
                ca[0][j] = MFMA16(wf, of[0][2 * t + ks], ca[0][j]); ca[1][j] = MFMA16(wf, of[1][2 * t + ks], ca[1][j]);
            }
    }
    LBAR();
    LAS f32x4* red = (LAS f32x4*)lds;
#pragma unroll
    for (int i = 0; i < 2; ++i)
#pragma unroll
        for (int j = 0; j < 5; ++j) red[((crh * 4 + kq) * 10 + i * 5 + j) * 64 + lane] = ca[i][j] * gt[i];
    LBAR();
    const float poison = (kv_ok && o_ok) ? 0.f : __builtin_nanf("");
#pragma unroll
    for (int u = 0; u < 3; ++u) {
        const int tile = kq + 4 * u;
        if (tile < 10) {
            const int i = tile / 5, j = tile - 5 * i;
            f32x4 a = red[((crh * 4 + 0) * 10 + tile) * 64 + lane];
#pragma unroll
            for (int qq = 1; qq < 4; ++qq) a += red[((crh * 4 + qq) * 10 + tile) * 64 + lane];
            a += poison;
            *(u32x2*)(P.out + (size_t)(s0 + 32 * crh + 16 * i + i16) * 640 + n0 + 16 * j + 4 * g) = (u32x2){pk2(a[0], a[1]), pk2(a[2], a[3])};
        }
    }
    PSTAMP(10); PSTAMP(11);
    PFLUSH();
}

static inline Params mk_params(void* const* d_in, void* d_out, void* d_ws) {
    Params P; P.x = (const u16*)d_in[0]; P.wqkv = (const u16*)d_in[1]; P.woA = (const u16*)d_in[2]; P.wq = (const u16*)d_in[4]; P.wk = (const u16*)d_in[5]; P.wv = (const u16*)d_in[6];
    P.woB = (const u16*)d_in[7]; P.gw = (const u16*)d_in[8]; P.ws = (char*)d_ws; P.out = (u16*)d_out; return P; }
extern "C" void kernel_launch(void* const* d_in, const int* in_sizes, int n_in, void* d_out, int out_size, void* d_ws, size_t ws_size, hipStream_t stream) {
    (void)hipFuncSetAttribute((const void*)fused, hipFuncAttributeMaxDynamicSharedMemorySize, LDS_TOTAL);
    fused<<<256, 512, LDS_TOTAL, stream>>>(mk_params(d_in, d_out, d_ws));
}
#define ST_LAUNCH(d_in, d_out, ws, stream) fused<<<256, 512, LDS_TOTAL, stream>>>(mk_params(d_in, d_out, ws))
```

```cpp
#include <hip/hip_runtime.h>
#include <stdint.h>
#define APRIO 1

typedef unsigned short u16;
typedef unsigned long long u64;
typedef short bf16x8 __attribute__((ext_vector_type(8)));
typedef short s16x4 __attribute__((ext_vector_type(4)));
typedef float f32x16 __attribute__((ext_vector_type(16)));
typedef float f32x4 __attribute__((ext_vector_type(4)));
typedef float f32x2 __attribute__((ext_vector_type(2)));
typedef unsigned u32x4 __attribute__((ext_vector_type(4)));
typedef unsigned u32x2 __attribute__((ext_vector_type(2)));
typedef __bf16 bf2_t __attribute__((ext_vector_type(2)));
#define LAS __attribute__((address_space(3)))
#define GAS __attribute__((address_space(1)))
typedef LAS const char* lds_cptr;
typedef LAS char* lds_ptr;
typedef __amdgpu_buffer_rsrc_t rsrc_t;

__device__ __forceinline__ float bf2f(u16 b) { return __uint_as_float(((unsigned)b) << 16); }
__device__ __forceinline__ unsigned pk2(float lo, float hi) { f32x2 v = {lo, hi}; bf2_t r = __builtin_convertvector(v, bf2_t); return __builtin_bit_cast(unsigned, r); }
__device__ __forceinline__ u16 f2bf(float f) { return (u16)(pk2(f, 0.f) & 0xffffu); }
__device__ __forceinline__ float lo2f(unsigned u) { return __uint_as_float(u << 16); }
__device__ __forceinline__ float hi2f(unsigned u) { return __uint_as_float(u & 0xffff0000u); }
__device__ __forceinline__ float dot2(unsigned a, unsigned b, float c) { return __builtin_amdgcn_fdot2_f32_bf16(__builtin_bit_cast(bf2_t, a), __builtin_bit_cast(bf2_t, b), c, false); }

constexpr int S = 2048, D = 640, H = 32, DH = 20;
constexpr float SCALE = 0.22360679774997896f, LOG2E = 1.4426950408889634f;
constexpr float QSCALE = SCALE * LOG2E;

constexpr int STG_KM = 0, STG_KT = 8192, STG_VT = 12288, VT_ROW = 528, STG_BYTES = 24576;
constexpr int WS_EPOCH = 0;
constexpr int WS_FKV = 256;
constexpr int WS_FO = WS_FKV + 2048;
constexpr int WS_FA = WS_FO + 2048;
constexpr int WS_NRM = 8192;
constexpr int WS_G = WS_NRM + 2048;
constexpr int WS_KV = 32768;
constexpr int WS_O = WS_KV + 8 * STG_BYTES;
constexpr int WS_END = WS_O + S * 1280 * 2;
constexpr int AUX_SC1 = 16;

#define MFMA32(a, b, c) __builtin_amdgcn_mfma_f32_32x32x16_bf16((a), (b), (c), 0, 0, 0)
#define MFMA16(a, b, c) __builtin_amdgcn_mfma_f32_16x16x32_bf16((a), (b), (c), 0, 0, 0)
#define SBAR() __builtin_amdgcn_sched_barrier(0)
#define EXP2(x) __builtin_amdgcn_exp2f(x)
#define WAIT_BAR(N) asm volatile("s_waitcnt vmcnt(" #N ")\n\ts_barrier" ::: "memory")
#define LBAR() asm volatile("s_waitcnt lgkmcnt(0)\n\ts_barrier" ::: "memory")
#define FULLBAR() asm volatile("s_waitcnt vmcnt(0) lgkmcnt(0)\n\ts_barrier" ::: "memory")
__device__ __forceinline__ int swap23(int x) { return (x & ~12) | ((x & 4) << 1) | ((x & 8) >> 1); }
__device__ __forceinline__ void glds16(const void* g, unsigned lds_base) {
    unsigned sv; asm volatile("s_mov_b32 %0, m0\n\ts_mov_b32 m0, %2\n\ts_nop 0\n\tglobal_load_lds_dwordx4 %1, off\n\ts_mov_b32 m0, %0" : "=&s"(sv) : "v"(g), "s"(lds_base) : "memory"); }
__device__ __forceinline__ void gldsb(const void* sbase, unsigned voff, unsigned lds_base) {
    unsigned sv; asm volatile("s_mov_b32 %0, m0\n\ts_mov_b32 m0, %3\n\ts_nop 0\n\tglobal_load_lds_dwordx4 %1, %2\n\ts_mov_b32 m0, %0" : "=&s"(sv) : "v"(voff), "s"(sbase), "s"(lds_base) : "memory"); }
__device__ __forceinline__ void gldsb_sc1(const void* sbase, unsigned voff, unsigned lds_base) {
    unsigned sv; asm volatile("s_mov_b32 %0, m0\n\ts_mov_b32 m0, %3\n\ts_nop 0\n\tglobal_load_lds_dwordx4 %1, %2 sc1\n\ts_mov_b32 m0, %0" : "=&s"(sv) : "v"(voff), "s"(sbase), "s"(lds_base) : "memory"); }
__device__ __forceinline__ bf16x8 lds128(lds_cptr p) { return *(const LAS bf16x8*)p; }
typedef short v4i16_t __attribute__((ext_vector_type(4)));
__device__ __forceinline__ s16x4 ldstr(lds_cptr p) { return __builtin_bit_cast(s16x4, __builtin_amdgcn_ds_read_tr16_b64_v4i16((LAS v4i16_t*)p)); }
__device__ __forceinline__ bf16x8 ldstr2(lds_cptr p0, lds_cptr p1) { const s16x4 a = ldstr(p0), b = ldstr(p1); return __builtin_shufflevector(a, b, 0, 1, 2, 3, 4, 5, 6, 7); }
__device__ __forceinline__ unsigned rfl(unsigned x) { return (unsigned)__builtin_amdgcn_readfirstlane(x); }
__device__ __forceinline__ void st32(rsrc_t r, int off, unsigned v) { __builtin_amdgcn_raw_buffer_store_b32(v, r, off, 0, AUX_SC1); }
__device__ __forceinline__ void st64(rsrc_t r, int off, u32x2 v) { __builtin_amdgcn_raw_buffer_store_b64(v, r, off, 0, AUX_SC1); }
__device__ __forceinline__ void st128(rsrc_t r, int off, u32x4 v) { __builtin_amdgcn_raw_buffer_store_b128(v, r, off, 0, AUX_SC1); }
__device__ __forceinline__ unsigned ld32(rsrc_t r, int off) { return __builtin_amdgcn_raw_buffer_load_b32(r, off, 0, AUX_SC1); }
__device__ __forceinline__ u32x2 ld64(rsrc_t r, int off) { return __builtin_amdgcn_raw_buffer_load_b64(r, off, 0, AUX_SC1); }
__device__ __forceinline__ u32x4 ld128(rsrc_t r, int off) { return __builtin_amdgcn_raw_buffer_load_b128(r, off, 0, AUX_SC1); }

#define PSTAMP(i)
#define PDECL
#define PFLUSH()

struct Params { const u16 *x, *wqkv, *woA, *wq, *wk, *wv, *woB, *gw; char* ws; u16* out; };

constexpr int LDS_TOTAL = 163840, CTL = LDS_TOTAL - 128, DUMMY = CTL - 256;
constexpr int KVB = 2 * STG_BYTES;
constexpr int Q_SLOT = 18432, Q_W = 8192, Q_ST = 73728, QB_ROW = 208;
constexpr int AA = 98304;
constexpr int P0W = AA, P0X = AA + 25664, P0G = P0X + 20480;
constexpr int A_SLOT = 20480, A_X = 15360;
constexpr int ST_QK = AA, ST_V = AA + 32000, QK_ROW = 400, V_ROW = 336;
constexpr int C_S3 = 49152, C_S4 = 69632;
constexpr int C_FALL = 0, C_BB = 1, C_DONE = 2, C_LAND = 4, C_X = 6, C_BD = 7, C_P = 8, C_D = 12, C_AB = 13, C_P0D = 14, C_QL = 15, C_KVOK = 16, C_BAD = 17;
constexpr unsigned CODE_LINES = 232;
constexpr unsigned SPIN_CAP = 1u << 18;

template <int N, int SLEEP = 4>
__device__ __forceinline__ bool poll_tags(rsrc_t r, int off, bool active, u64 tag) {
#pragma unroll 1
    for (unsigned spins = 0; spins < SPIN_CAP; ++spins) {
        bool ok = true;
        if (active) {
#pragma unroll
            for (int i = 0; i < N; ++i) { const u32x2 v = ld64(r, off + 8 * i); ok &= (v[0] == (unsigned)tag) & (v[1] == (unsigned)(tag >> 32)); }
        }
        if (__all(ok)) return true;
        __builtin_amdgcn_s_sleep(SLEEP);
    }
    return false;
}
__device__ __forceinline__ void lds_signal(LAS unsigned* cnt, int lane) {
    (void)lane;
    __builtin_amdgcn_sched_barrier(0);
    asm volatile("s_mov_b64 exec, 1\n\tds_add_u32 %0, %1\n\ts_mov_b64 exec, -1\n\ts_nop 3" :: "v"((unsigned)(uintptr_t)cnt), "v"(1u) : "memory");
    __builtin_amdgcn_sched_barrier(0); }
template <bool SLEEP = true>
__device__ __forceinline__ void lds_wait(LAS unsigned* cnt, unsigned n) {
#pragma unroll 1
    for (unsigned spins = 0; spins < (1u << 20); ++spins) {
        if (rfl(__hip_atomic_load(cnt, __ATOMIC_RELAXED, __HIP_MEMORY_SCOPE_WORKGROUP)) >= n) break;
        if (SLEEP) __builtin_amdgcn_s_sleep(1);
    }
    asm volatile("" ::: "memory");
}
#define VMW(N) asm volatile("s_waitcnt vmcnt(" #N ")" ::: "memory")
#define GBAR(cnt, n, step) do { lds_signal(cnt, lane); n += (step); lds_wait<false>(cnt, n); } while (0)

__device__ __forceinline__ void att_stage_load(const char* kv, int st, unsigned ldsbuf, int w4, int lane) {
    const char* src = kv + (size_t)st * STG_BYTES + w4 * 1024;
#pragma unroll
    for (int i = 0; i < 6; ++i) gldsb_sc1(src + i * 4096, lane * 16, ldsbuf + (w4 + 4 * i) * 1024);
}

__device__ __forceinline__ void att_maxpass(const char* kv, char* ldsh, LAS unsigned* bb, unsigned& bn, int kh, int w4, int lane, const bf16x8 (&qm)[2], const bf16x8 (&qt)[2][2], float (&mx)[2]) {
    const unsigned lds0 = (unsigned)(uintptr_t)ldsh;
    const lds_cptr kbase = (lds_cptr)ldsh + lane * 16;
    const f32x16 zero = {};
#pragma unroll 1
    for (int rnd = 0; rnd < 3; ++rnd) {
        if (rnd) { GBAR(bb, bn, 8); att_stage_load(kv, 4 * kh + 2 * (rnd & 1), lds0, w4, lane); att_stage_load(kv, 4 * kh + 2 * (rnd & 1) + 1, lds0 + KVB, w4, lane); }
        VMW(0); GBAR(bb, bn, 8);
        if (rnd < 2) {
#pragma unroll 1
            for (int pr = 0; pr < 8; ++pr) {
                const lds_cptr kb = kbase + (pr >> 2) * KVB; const int p = pr & 3;
                const bf16x8 kt = lds128(kb + STG_KT + p * 1024), k0 = lds128(kb + STG_KM + (2 * p) * 1024), k1 = lds128(kb + STG_KM + (2 * p + 1) * 1024);
#pragma unroll
                for (int h = 0; h < 2; ++h) {
                    f32x16 s0 = MFMA32(k0, qm[h], zero); s0 = MFMA32(kt, qt[h][0], s0);
                    f32x16 s1 = MFMA32(k1, qm[h], zero); s1 = MFMA32(kt, qt[h][1], s1);
#pragma unroll
                    for (int i = 0; i < 16; ++i) mx[h] = fmaxf(mx[h], fmaxf(s0[i], s1[i]));
                }
            }
        }
    }
}

__device__ __forceinline__ void att_main(const char* kv, char* ldsh, LAS unsigned* ctl, int kh, int w4, int lane, const bf16x8 (&qm)[2], const bf16x8 (&qt)[2][2], f32x16& O0, f32x16& O1) {
    const unsigned lds0 = (unsigned)(uintptr_t)ldsh;
    LAS unsigned* cdone = ctl + C_DONE + kh; LAS unsigned* cland = ctl + C_LAND + kh;
    const int r = lane & 31, hh = lane >> 5;
    const int voff = r <= 20 ? r * VT_ROW : (r < 28 ? 21 * VT_ROW : 21 * VT_ROW + 112);
    const lds_cptr kbase = (lds_cptr)ldsh + lane * 16;
    const lds_cptr vbase = (lds_cptr)ldsh + STG_VT + voff + hh * 16;
    const f32x16 zero = {};
    f32x16 sA, sB; u32x4 pA0, pA1, pB0, pB1; bf16x8 kmA, kmB, ktA, ktB, vA0, vA1, vB0, vB1;
    ktA = lds128(kbase + STG_KT); kmA = lds128(kbase + STG_KM);
    sA = MFMA32(kmA, qm[0], zero); sA = MFMA32(ktA, qt[0][0], sA);
    pB0 = (u32x4){0u, 0u, 0u, 0u}; pB1 = pB0; vB0 = kmA; vB1 = kmA; ktB = ktA; vA0 = kmA; vA1 = kmA; kmB = kmA;
    float ee0 = 0.f, ee1 = 0.f, ee2 = 0.f, ee3 = 0.f;
#define GRP(MF, LD, SC, I, PD, J) do { MF; LD; const float e0_ = EXP2(SC[I]); unsigned a_ = pk2(ee0, ee1); const float e1_ = EXP2(SC[I + 1]); unsigned b_ = pk2(ee2, ee3); const float e2_ = EXP2(SC[I + 2]), e3_ = EXP2(SC[I + 3]); \
        ee0 = e0_; ee1 = e1_; ee2 = e2_; ee3 = e3_; asm volatile("" : "+v"(a_), "+v"(b_), "+v"(ee0), "+v"(ee1), "+v"(ee2), "+v"(ee3)); PD[J] = a_; PD[J + 1] = b_; SBAR(); } while (0)
#define TILE(KM, KMN, KT, VJ, KTN, VJN, VC0, VC1, VP0, VP1, VADDR, LDK, LDT) do { \
        GRP(sB = MFMA32(KM, qm[1], zero), VC0 = lds128(VADDR), sA, 0, pB1, 2); \
        GRP(O1 = MFMA32(VP0, __builtin_bit_cast(bf16x8, pB0), O1), VC1 = lds128((VADDR) + 32), sA, 4, pA0, 0); \
        GRP(sB = MFMA32(KT, qt[1][VJ], sB), LDK, sA, 8, pA0, 2); \
        GRP(O1 = MFMA32(VP1, __builtin_bit_cast(bf16x8, pB1), O1), LDT, sA, 12, pA1, 0); \
        GRP(sA = MFMA32(KMN, qm[0], zero), (void)0, sB, 0, pA1, 2); \
        GRP(O0 = MFMA32(VC0, __builtin_bit_cast(bf16x8, pA0), O0), (void)0, sB, 4, pB0, 0); \
        GRP(sA = MFMA32(KTN, qt[0][VJN], sA), (void)0, sB, 8, pB0, 2); \
        GRP(O0 = MFMA32(VC1, __builtin_bit_cast(bf16x8, pA1), O0), (void)0, sB, 12, pB1, 0); } while (0)
#pragma unroll 1
    for (int st = 0; st < 4; ++st) {
        const int buf = st & 1;
        const lds_cptr kb = kbase + buf * KVB, vb = vbase + buf * KVB, kn = kbase + (buf ^ 1) * KVB;
        TILE(kmA, kmB, ktA, 0, ktA, 1, vA0, vA1, vB0, vB1, vb + 0 * 64, kmB = lds128(kb + STG_KM + 1 * 1024), (void)0);
        if (st) { lds_signal(st == 3 ? ctl + C_X : cdone, lane); SBAR(); }
        TILE(kmB, kmA, ktA, 1, ktB, 0, vB0, vB1, vA0, vA1, vb + 1 * 64, kmA = lds128(kb + STG_KM + 2 * 1024), ktB = lds128(kb + STG_KT + 1024));
        if (st == 1 || st == 2) { lds_wait(cdone, 4u * st); att_stage_load(kv, 4 * kh + st + 1, lds0 + (buf ^ 1) * KVB, w4, lane); SBAR(); }
        TILE(kmA, kmB, ktB, 0, ktB, 1, vA0, vA1, vB0, vB1, vb + 2 * 64, kmB = lds128(kb + STG_KM + 3 * 1024), (void)0);
        TILE(kmB, kmA, ktB, 1, ktA, 0, vB0, vB1, vA0, vA1, vb + 3 * 64, kmA = lds128(kb + STG_KM + 4 * 1024), ktA = lds128(kb + STG_KT + 2048));
        TILE(kmA, kmB, ktA, 0, ktA, 1, vA0, vA1, vB0, vB1, vb + 4 * 64, kmB = lds128(kb + STG_KM + 5 * 1024), (void)0);
        TILE(kmB, kmA, ktA, 1, ktB, 0, vB0, vB1, vA0, vA1, vb + 5 * 64, kmA = lds128(kb + STG_KM + 6 * 1024), ktB = lds128(kb + STG_KT + 3072));
        if (st < 3) { VMW(0); lds_signal(cland, lane); SBAR(); }
        TILE(kmA, kmB, ktB, 0, ktB, 1, vA0, vA1, vB0, vB1, vb + 6 * 64, kmB = lds128(kb + STG_KM + 7 * 1024), (void)0);
        if (st < 3) { lds_wait(cland, 4u * (st + 1)); SBAR(); }
        TILE(kmB, kmA, ktB, 1, ktA, 0, vB0, vB1, vA0, vA1, vb + 7 * 64, kmA = lds128(kn + STG_KM + 0 * 1024), ktA = lds128(kn + STG_KT));
    }
    pB1[2] = pk2(ee0, ee1); pB1[3] = pk2(ee2, ee3);
    O1 = MFMA32(vB0, __builtin_bit_cast(bf16x8, pB0), O1);
    O1 = MFMA32(vB1, __builtin_bit_cast(bf16x8, pB1), O1);
#undef GRP
#undef TILE
}

__device__ __forceinline__ int ofrag_off(int s, int k) { return WS_O + ((((s >> 4) * 40 + (k >> 5)) * 64 + ((k >> 3) & 3) * 16 + (s & 15)) * 16 + (k & 7) * 2); }
__device__ __forceinline__ void touch(const void* g, unsigned lds_dummy) {
    unsigned sv; asm volatile("s_mov_b32 %0, m0\n\ts_mov_b32 m0, %2\n\ts_nop 0\n\tglobal_load_lds_dword %1, off\n\ts_mov_b32 m0, %0" : "=&s"(sv) : "v"(g), "s"(lds_dummy) : "memory"); }

__device__ __forceinline__ void b_waves(const Params& P, char* lds, LAS unsigned* ctl, rsrc_t rw, unsigned wid, unsigned lane, unsigned rt64, unsigned c4, u64* stamps) {
    char* const ws = P.ws;
    const unsigned lds0 = (unsigned)(uintptr_t)lds;
    const int s0 = 64 * rt64;
    const unsigned i16 = lane & 15, g = lane >> 4, q4 = i16 >> 2, p4 = i16 & 3;
    unsigned bn = 0; LAS unsigned* bb = ctl + C_BB;
    unsigned xo, woA_, woB_;
    { const unsigned p = wid * 64 + lane, row = p >> 3, ch = (p & 7) ^ ((row >> 1) & 7); xo = row * 1280 + ch * 16; }
    { const unsigned p = wid * 64 + lane, pos = p / 10, j10 = p - pos * 10, k = pos ^ (4 * ((pos >> 3) & 1)); woA_ = k * 1280 + j10 * 16; }
    { const unsigned p = (8 + (wid & 1)) * 64 + lane, pos = p / 10, j10 = p - pos * 10, k = pos ^ (4 * ((pos >> 3) & 1)); woB_ = k * 1280 + j10 * 16; }
    const char* xs = (const char*)(P.x + (size_t)s0 * D); const char* wsq = (const char*)(P.wq + 80 * c4);
#define Q_ISSUE(slot) do { gldsb(xs, xo, lds0 + (slot) * Q_SLOT + wid * 1024); gldsb(wsq, woA_, lds0 + (slot) * Q_SLOT + Q_W + wid * 1024); \
        if (wid < 2) gldsb(wsq, woB_, lds0 + (slot) * Q_SLOT + Q_W + (8 + wid) * 1024); xs += 128; wsq += 64 * 1280; } while (0)
#pragma unroll 1
    for (int t = 0; t < 5; ++t) Q_ISSUE(t);
    PSTAMP(1);
    const unsigned rti = wid & 3, cgp = wid >> 2;
    const lds_cptr qx0 = (lds_cptr)lds + (16 * rti + i16) * 128 + ((g ^ (i16 >> 1)) * 16), qx1 = (lds_cptr)lds + (16 * rti + i16) * 128 + (((4 + g) ^ (i16 >> 1)) * 16);
    const lds_cptr qw0 = (lds_cptr)lds + Q_W + (8 * g + q4 + 4 * (0 ^ (g & 1))) * 160 + p4 * 8 + cgp * 96, qw1 = (lds_cptr)lds + Q_W + (8 * g + q4 + 4 * (1 ^ (g & 1))) * 160 + p4 * 8 + cgp * 96;
    f32x4 qa[3] = {{0.f, 0.f, 0.f, 0.f}, {0.f, 0.f, 0.f, 0.f}, {0.f, 0.f, 0.f, 0.f}};
    VMW(8); lds_signal(bb, lane); bn += 8;
    { int slot = 0;
#pragma unroll 1
      for (int t = 0; t < 10; ++t) {
        lds_wait<false>(bb, bn);
        if (t >= 2 && t < 7) { const int fs = slot >= 2 ? slot - 2 : slot + 3; Q_ISSUE(fs); }
        if (t == 0) VMW(6); else if (t < 7) VMW(4); else if (t == 7) VMW(2); else VMW(0);
        lds_signal(bb, lane); bn += 8;
        if (t == 8) lds_signal(ctl + C_QL, lane);
        const int so = slot * Q_SLOT;
        const bf16x8 x0 = lds128(qx0 + so), x1 = lds128(qx1 + so);
#pragma unroll
        for (int j = 0; j < 3; ++j) { qa[j] = MFMA16(ldstr2(qw0 + so + j * 32, qw1 + so + j * 32), x0, qa[j]); }
#pragma unroll
        for (int j = 0; j < 3; ++j) { qa[j] = MFMA16(ldstr2(qw0 + so + 5120 + j * 32, qw1 + so + 5120 + j * 32), x1, qa[j]); }
        slot = slot == 4 ? 0 : slot + 1;
      } }
    PSTAMP(2);
    GBAR(bb, bn, 8);
    const char* kv = ws + WS_KV;
    const unsigned w4 = wid & 3, kh = wid >> 2;
    char* ldsh = lds + kh * STG_BYTES;
    const unsigned ldsh0 = lds0 + kh * STG_BYTES;
    lds_wait(ctl + C_KVOK, 1u);
    att_stage_load(kv, 4 * kh, ldsh0, w4, lane);
    const u32x4 nr0 = ld128(rw, WS_NRM + lane * 16), nr1 = ld128(rw, WS_NRM + 1024 + lane * 16);
#pragma unroll
    for (int j = 0; j < 3; ++j) { const unsigned jt = 3 * cgp + j, cq = 4 * jt + g, hl = (cq * 13) >> 6, dq = cq - 5 * hl;
        if (jt < 5) *(LAS u32x2*)((lds_ptr)lds + Q_ST + (16 * rti + i16) * QB_ROW + hl * 48 + dq * 8) = (u32x2){pk2(qa[j][0] * QSCALE, qa[j][1] * QSCALE), pk2(qa[j][2] * QSCALE, qa[j][3] * QSCALE)}; }
    GBAR(bb, bn, 8);
    const int r = lane & 31, hh = lane >> 5;
    bf16x8 qm[2]; u32x2 qtl[2];
#pragma unroll
    for (int h = 0; h < 2; ++h) {
        const lds_cptr qp = (lds_cptr)lds + Q_ST + (32 * (w4 & 1) + r) * QB_ROW + (2 * (w4 >> 1) + h) * 48;
        qm[h] = lds128(qp + hh * 16); qtl[h] = *(const LAS u32x2*)(qp + 32);
    }
    float km2 = fmaxf(__uint_as_float(nr0[0]), __uint_as_float(nr1[0])), kt2 = fmaxf(__uint_as_float(nr0[1]), __uint_as_float(nr1[1]));
    float vmax = fmaxf(fmaxf(__uint_as_float(nr0[2]), __uint_as_float(nr1[2])), fmaxf(__uint_as_float(nr0[3]), __uint_as_float(nr1[3])));
#pragma unroll
    for (int o = 1; o < 64; o <<= 1) { km2 = fmaxf(km2, __shfl_xor(km2, o)); kt2 = fmaxf(kt2, __shfl_xor(kt2, o)); vmax = fmaxf(vmax, __shfl_xor(vmax, o)); }
    const float kmax2 = km2 + kt2;
    float q2[2];
#pragma unroll
    for (int h = 0; h < 2; ++h) {
        q2[h] = 0.f;
#pragma unroll
        for (int i = 0; i < 8; ++i) { const float v = bf2f((u16)qm[h][i]); q2[h] += v * v; }
        if (hh == 0) { const float a0 = lo2f(qtl[h][0]), a1 = hi2f(qtl[h][0]), a2 = lo2f(qtl[h][1]), a3 = hi2f(qtl[h][1]); q2[h] += a0 * a0 + a1 * a1 + a2 * a2 + a3 * a3; }
        q2[h] += __shfl_xor(q2[h], 32);
    }
    asm volatile("s_waitcnt vmcnt(0) lgkmcnt(0)" ::: "memory");
    GBAR(bb, bn, 8);
    PSTAMP(3);
    att_stage_load(kv, 4 * kh + 1, ldsh0 + KVB, w4, lane);
    const u32x4 z4 = {0u, 0u, 0u, 0u};
    bf16x8 qt[2][2];
#pragma unroll
    for (int h = 0; h < 2; ++h) { const u32x4 a = {qtl[h][0], qtl[h][1], 0u, 0u}; qt[h][0] = __builtin_bit_cast(bf16x8, hh ? z4 : a); qt[h][1] = __builtin_bit_cast(bf16x8, hh ? a : z4); }
    const f32x16 zero = {};
    float m[2]; bool unsafe = false;
    const float lim = 112.f - fmaxf(0.f, __builtin_amdgcn_logf(fmaxf(vmax, 1.f)));
    { const lds_cptr kb = (lds_cptr)ldsh + lane * 16; const bf16x8 k0 = lds128(kb + STG_KM), kt = lds128(kb + STG_KT);
#pragma unroll
      for (int h = 0; h < 2; ++h) {
        f32x16 sc = MFMA32(k0, qm[h], zero); sc = MFMA32(kt, qt[h][0], sc);
        float mm = sc[0];
#pragma unroll
        for (int i = 1; i < 16; ++i) mm = fmaxf(mm, sc[i]);
        mm = fmaxf(mm, __shfl_xor(mm, 32));
        m[h] = bf2f(f2bf(mm));
        unsafe |= !(__builtin_sqrtf(q2[h] * kmax2) * 1.001f + 1.f - m[h] <= lim);
      } }
    if (__any(unsafe) && lane == 0) ctl[C_FALL] = 1u;
    GBAR(bb, bn, 8);
    if (rfl(ctl[C_FALL]) != 0u) {
        att_maxpass(kv, ldsh, bb, bn, kh, w4, lane, qm, qt, m);
#pragma unroll
        for (int h = 0; h < 2; ++h) m[h] = bf2f(f2bf(fmaxf(m[h], __shfl_xor(m[h], 32))));
    }
#pragma unroll
    for (int h = 0; h < 2; ++h) { const unsigned nm = f2bf(-m[h]); const u32x4 a = {qtl[h][0], qtl[h][1], nm, 0u}; qt[h][0] = __builtin_bit_cast(bf16x8, hh ? z4 : a); qt[h][1] = __builtin_bit_cast(bf16x8, hh ? a : z4); }
    f32x16 O[2] = {{}, {}};
    PSTAMP(4);
    att_main(kv, ldsh, ctl, kh, w4, lane, qm, qt, O[0], O[1]);
    PSTAMP(5);
    lds_signal(ctl + C_BD, lane);
    LAS float* xch = (LAS float*)((lds_ptr)lds + w4 * 6656);
    if (kh == 0) {
        lds_wait(ctl + C_X, 8u);
#pragma unroll
        for (int h = 0; h < 2; ++h) {
#pragma unroll
            for (int i = 0; i < 12; ++i) xch[(13 * h + i) * 64 + lane] = O[h][i];
            xch[(13 * h + 12) * 64 + lane] = m[h];
        }
        lds_signal(ctl + C_P + w4, lane);
    } else {
        lds_wait(ctl + C_P + w4, 1u);
#pragma unroll
        for (int h = 0; h < 2; ++h) {
            const float m0 = xch[(13 * h + 12) * 64 + lane];
            const float M = fmaxf(m0, m[h]), f0 = EXP2(m0 - M), f1 = EXP2(m[h] - M);
            float o[12];
#pragma unroll
            for (int i = 0; i < 12; ++i) o[i] = O[h][i] * f1 + xch[(13 * h + i) * 64 + lane] * f0;
            const float inv = __builtin_amdgcn_rcpf(__shfl(o[8], r + 32));
            const int s = s0 + 32 * (w4 & 1) + r, k0 = 640 + (4 * c4 + 2 * (w4 >> 1) + h) * DH;
            st64(rw, ofrag_off(s, k0 + 4 * hh), (u32x2){pk2(o[0] * inv, o[1] * inv), pk2(o[2] * inv, o[3] * inv)});
            st64(rw, ofrag_off(s, k0 + 8 + 4 * hh), (u32x2){pk2(o[4] * inv, o[5] * inv), pk2(o[6] * inv, o[7] * inv)});
            if (hh == 0) st64(rw, ofrag_off(s, k0 + 16), (u32x2){pk2(o[8] * inv, o[9] * inv), pk2(o[10] * inv, o[11] * inv)});
        }
        VMW(0);
        lds_signal(ctl + C_D, lane);
    }
    asm volatile("s_waitcnt lgkmcnt(0)" ::: "memory");
    PSTAMP(6);
}

__device__ __forceinline__ void a_waves(const Params& P, char* lds, LAS unsigned* ctl, rsrc_t rw, unsigned a, unsigned lane, unsigned b, unsigned li, unsigned rt64, unsigned c4, u64* stamps) {
    const unsigned lds0 = (unsigned)(uintptr_t)lds;
    const int s0 = 64 * rt64;
    const unsigned i16 = lane & 15, g = lane >> 4, q4 = i16 >> 2, p4 = i16 & 3;
    unsigned an = 0; LAS unsigned* ab = ctl + C_AB;
    const unsigned part = c4 & 1, r0 = s0 + 16 * (c4 >> 1);
    { const char* wsrc = (const char*)(part ? P.wv : P.wk); const char* xsrc = (const char*)(P.x + (size_t)r0 * D);
#pragma unroll 1
      for (unsigned i = 0; i < 12; ++i) { unsigned id = a + 4 * i; if (id == 47) id = 43;
        if (id < 25) gldsb(wsrc, id * 1024 + lane * 16, lds0 + P0W + id * 1024);
        else if (id < 45) { const unsigned p = (id - 25) * 64 + lane, row = p / 80, cpos = p - row * 80, ch = (cpos & ~15u) | ((cpos & 15) ^ row);
            gldsb(xsrc, row * 1280 + ch * 16, lds0 + P0X + (id - 25) * 1024); }
        else { const unsigned off = (id - 45) * 1024 + lane * 16; gldsb(P.gw, off < 1280 ? off : 1264, lds0 + P0G + (id - 45) * 1024); } } }
    const u32x2 ep = ld64(rw, WS_EPOCH);
    const u64 tag = ((u64)ep[0] | ((u64)ep[1] << 32)) + 0x9E3779B97F4A7C15ull;
    const u32x2 tagv = {(unsigned)tag, (unsigned)(tag >> 32)};
    if (a == 3) {
        const unsigned ri = li >> 1, cc = li & 1;
        u64 pc; asm volatile("s_getpc_b64 %0" : "=s"(pc));
        { const unsigned ln = li * 8 + (lane & 7); touch((const char*)(pc & ~127ull) + (ln < CODE_LINES ? ln : 0) * 128, lds0 + DUMMY); }
#pragma unroll 1
        for (unsigned i = 0; i < 5; ++i) { const unsigned k = ri + 16 * (8 * i + (lane >> 3)), seg = (lane >> 1) & 3, ld = seg == 0 ? 640u : 1920u;
            touch((const char*)((seg == 0 ? P.wq : P.wqkv + (seg - 1) * 640) + 80 * c4) + (size_t)(k * ld * 2) + (lane & 1) * 156, lds0 + DUMMY); }
#pragma unroll 1
        for (unsigned i = 0; i < 7; ++i) { const unsigned idx = i * 64 + lane, kl = idx / 40, row = 2 * (idx - kl * 40) + cc; int gr = s0 - 16 + (int)row; gr = gr < 0 ? 0 : gr;
            touch((const char*)(P.x + (size_t)gr * D) + (kl < 10 ? kl : 9) * 128, lds0 + DUMMY); }
    }
    PSTAMP(1);
    if (a == 3) {
        VMW(13);
    } else VMW(0);
    GBAR(ab, an, 4);
    if (a < 2 || (a == 2 && part == 0)) {
        const lds_cptr p0x = (lds_cptr)lds + P0X + i16 * 1280;
        f32x4 pacc = {0.f, 0.f, 0.f, 0.f};
#define P0XF(ks) lds128(p0x + ((((4 * (ks) + g) & ~15u) | (((4 * (ks) + g) & 15) ^ i16)) * 16))
        const int ks0 = r0 & 255, img = WS_KV + (r0 >> 8) * STG_BYTES, nrm = WS_NRM + (r0 >> 4) * 16;
        if (a == 2) {
            const bf16x8 z = {0, 0, 0, 0, 0, 0, 0, 0};
#pragma unroll
            for (int ks = 0; ks < 20; ++ks) { const bf16x8 gv = lds128((lds_cptr)lds + P0G + (32 * ks + 8 * g) * 2); pacc = MFMA16(i16 == 0 ? gv : z, P0XF(ks), pacc); }
            if (g == 0) st32(rw, WS_G + (r0 + i16) * 4, __float_as_uint(1.f / (1.f + __expf(-pacc[0]))));
        } else {
            const lds_cptr p0w = (lds_cptr)lds + P0W + (8 * g + q4) * 40 + (16 * a + 4 * p4) * 2;
            if (part == 0) {
#pragma unroll
                for (int ks = 0; ks < 20; ++ks) pacc = MFMA16(ldstr2(p0w + ks * 1280, p0w + ks * 1280 + 160), P0XF(ks), pacc);
                const unsigned w0 = pk2(pacc[0], pacc[1]), w1 = pk2(pacc[2], pacc[3]);
                const int key = ks0 + i16, tile = key >> 5, r = swap23(key & 31);
                float n2 = lo2f(w0) * lo2f(w0) + hi2f(w0) * hi2f(w0) + lo2f(w1) * lo2f(w1) + hi2f(w1) * hi2f(w1);
                if (a == 0) { st64(rw, img + STG_KM + tile * 1024 + (g >> 1) * 512 + r * 16 + (g & 1) * 8, (u32x2){w0, w1}); n2 += __shfl_xor(n2, 16); n2 += __shfl_xor(n2, 32); }
                else { if (g == 0) st128(rw, img + STG_KT + (tile >> 1) * 1024 + (tile & 1) * 512 + r * 16, (u32x4){w0, w1, 0x3f80u, 0u}); if (g != 0) n2 = 0.f; }
#pragma unroll
                for (int o = 1; o < 64; o <<= 1) n2 = fmaxf(n2, __shfl_xor(n2, o));
                if (lane == 0) st32(rw, nrm + a * 4, __float_as_uint(n2));
            } else {
#pragma unroll
                for (int ks = 0; ks < 20; ++ks) pacc = MFMA16(P0XF(ks), ldstr2(p0w + ks * 1280, p0w + ks * 1280 + 160), pacc);
                const unsigned w0 = pk2(pacc[0], pacc[1]), w1 = pk2(pacc[2], pacc[3]);
                const int d = 16 * a + i16;
                float vm = fmaxf(fmaxf(fabsf(lo2f(w0)), fabsf(hi2f(w0))), fmaxf(fabsf(lo2f(w1)), fabsf(hi2f(w1))));
                if (d >= 20) vm = 0.f;
                if (d <= 20) st64(rw, img + STG_VT + d * VT_ROW + (ks0 + 4 * g) * 2, d == 20 ? (u32x2){0x3f803f80u, 0x3f803f80u} : (u32x2){w0, w1});
                if (a == 1 && ks0 == 0 && lane < 40) st128(rw, img + STG_VT + 21 * VT_ROW + lane * 16, (u32x4){0u, 0u, 0u, 0u});
#pragma unroll
                for (int o = 1; o < 64; o <<= 1) vm = fmaxf(vm, __shfl_xor(vm, o));
                if (lane == 0) st32(rw, nrm + (2 + a) * 4, __float_as_uint(vm));
            }
        }
#undef P0XF
        VMW(0);
    }
    GBAR(ab, an, 4);
    bool ok = true;
    if (a == 3) {
        if (lane == 0) st64(rw, WS_FKV + b * 8, tagv);
        lds_wait(ctl + C_QL, 8u);
        ok = poll_tags<4, 12>(rw, WS_FKV + lane * 32, true, tag);
        lds_signal(ctl + C_KVOK, lane);
        if (b == 0 && lane == 0) st64(rw, WS_EPOCH, tagv);
    }
    PSTAMP(2);
    unsigned aoff[5];
#pragma unroll
    for (unsigned j = 0; j < 5; ++j) { const unsigned id = a + 4 * j;
        if (id < 15) { const unsigned seg = id / 5, p = (id - 5 * seg) * 64 + lane, pos = p / 10, j10 = p - pos * 10, k = pos ^ (4 * ((pos >> 3) & 1)); aoff[j] = k * 3840 + seg * 1280 + j10 * 16; }
        else { const unsigned p = (id - 15) * 64 + lane, row = p >> 2, ch = (p & 3) ^ (3 * ((row >> 2) & 1)); int gr = s0 - 16 + (int)row; gr = gr < 0 ? 0 : gr; aoff[j] = gr * 1280 + ch * 16; } }
    const char* awb = (const char*)(P.wqkv + 80 * c4); const char* axb = (const char*)P.x;
#define A_ISSUE(slot) do { gldsb(awb, aoff[0], lds0 + AA + (slot) * A_SLOT + a * 1024); gldsb(awb, aoff[1], lds0 + AA + (slot) * A_SLOT + (a + 4) * 1024); gldsb(awb, aoff[2], lds0 + AA + (slot) * A_SLOT + (a + 8) * 1024); \
        gldsb(a < 3 ? awb : axb, aoff[3], lds0 + AA + (slot) * A_SLOT + (a + 12) * 1024); gldsb(axb, aoff[4], lds0 + AA + (slot) * A_SLOT + (a + 16) * 1024); awb += 32 * 3840; axb += 64; } while (0)
    lds_wait(ctl + C_QL, 8u);
    A_ISSUE(0); A_ISSUE(1);
    f32x4 acc[5][5];
#pragma unroll
    for (int i = 0; i < 5; ++i)
#pragma unroll
        for (int j = 0; j < 5; ++j) acc[i][j] = (f32x4){0.f, 0.f, 0.f, 0.f};
    const unsigned seg = a < 3 ? a : 0;
    const lds_cptr ax = (lds_cptr)lds + AA + A_X + i16 * 64 + ((g ^ (3 * ((i16 >> 2) & 1))) * 16);
    const lds_cptr aw0 = (lds_cptr)lds + AA + seg * 5120 + (8 * g + q4 + 4 * (0 ^ (g & 1))) * 160 + p4 * 8, aw1 = (lds_cptr)lds + AA + seg * 5120 + (8 * g + q4 + 4 * (1 ^ (g & 1))) * 160 + p4 * 8;
    { int slot = 0;
#pragma unroll 1
      for (int t = 0; t < 20; ++t) {
        if (t < 19) VMW(5); else VMW(0);
        GBAR(ab, an, 4);
        if (t + 2 < 20) { const int fs = slot == 0 ? 2 : slot - 1; A_ISSUE(fs); }
        if (a < 3) {
            const int so = slot * A_SLOT;
            bf16x8 xf[5];
#pragma unroll
            for (int i = 0; i < 5; ++i) xf[i] = lds128(ax + so + i * 1024);
#pragma unroll
            for (int j = 0; j < 5; ++j) { const bf16x8 wf = ldstr2(aw0 + so + j * 32, aw1 + so + j * 32);
#pragma unroll
                for (int i = 0; i < 5; ++i) acc[i][j] = MFMA16(wf, xf[i], acc[i][j]); }
        }
        slot = slot == 2 ? 0 : slot + 1;
      } }
    PSTAMP(3);
    GBAR(ab, an, 4);
    if (a < 3) {
        lds_ptr st = (lds_ptr)lds;
#pragma unroll
        for (int j = 0; j < 5; ++j) { const unsigned cq = 4 * j + g, hl = (cq * 13) >> 6, dq = cq - 5 * hl;
#pragma unroll
            for (int i = 0; i < 5; ++i) { const int row = 16 * i + i16; const f32x4 v = acc[i][j];
                if (a < 2) *(LAS u32x2*)(st + ST_QK + row * QK_ROW + a * 192 + hl * 48 + dq * 8) = (u32x2){pk2(v[0], v[1]), pk2(v[2], v[3])};
                else *(LAS f32x4*)(st + ST_V + row * V_ROW + hl * 80 + dq * 16) = v; } }
    }
    GBAR(ab, an, 4);
    {
        const int sl = lane, row = 16 + sl, s = s0 + sl;
        const lds_cptr qp = (lds_cptr)lds + ST_QK + row * QK_ROW + a * 48;
        const u32x4 qa = *(const LAS u32x4*)qp, qb = *(const LAS u32x4*)(qp + 16); const u32x2 qc = *(const LAS u32x2*)(qp + 32);
        float lg[8]; float m = -3.0e38f;
#pragma unroll
        for (int w = 0; w < 8; ++w) {
            const lds_cptr kp = qp + 192 - 2 * w * QK_ROW;
            const u32x4 ka = *(const LAS u32x4*)kp, kb = *(const LAS u32x4*)(kp + 16); const u32x2 kc = *(const LAS u32x2*)(kp + 32);
            float x = dot2(qa[0], ka[0], 0.f), x2 = dot2(qa[1], ka[1], 0.f);
            x = dot2(qa[2], ka[2], x); x2 = dot2(qa[3], ka[3], x2); x = dot2(qb[0], kb[0], x); x2 = dot2(qb[1], kb[1], x2);
            x = dot2(qb[2], kb[2], x); x2 = dot2(qb[3], kb[3], x2); x = dot2(qc[0], kc[0], x); x2 = dot2(qc[1], kc[1], x2);
            x = (x + x2) * QSCALE;
            lg[w] = (s - 2 * w >= 0) ? x : -1.0e9f * LOG2E; m = fmaxf(m, lg[w]);
        }
        float l = 0.f;
#pragma unroll
        for (int w = 0; w < 8; ++w) { lg[w] = EXP2(lg[w] - m); l += lg[w]; }
        const float inv = 1.f / l;
        f32x4 o[5];
#pragma unroll
        for (int j = 0; j < 5; ++j) o[j] = (f32x4){0.f, 0.f, 0.f, 0.f};
        const lds_cptr vp = (lds_cptr)lds + ST_V + row * V_ROW + a * 80;
#pragma unroll
        for (int w = 0; w < 8; ++w) {
            const float pw = lg[w] * inv;
#pragma unroll
            for (int j = 0; j < 5; ++j) o[j] += pw * *(const LAS f32x4*)(vp - 2 * w * V_ROW + j * 16);
        }
#pragma unroll
        for (int j = 0; j < 5; ++j) st64(rw, ofrag_off(s, (4 * c4 + a) * DH + 4 * j), (u32x2){pk2(o[j][0], o[j][1]), pk2(o[j][2], o[j][3])});
        VMW(0);
    }
    GBAR(ab, an, 4);
    if (a == 0 && lane == 0) st64(rw, WS_FA + (rt64 * 8 + c4) * 8, tagv);
    PSTAMP(4);
    unsigned coff[5];
#pragma unroll
    for (unsigned i = 0; i < 5; ++i) { const unsigned p = 256 * i + a * 64 + lane, pos = p / 10, j10 = p - pos * 10; coff[i] = (pos ^ (4 * ((pos >> 3) & 1))) * 1280 + j10 * 16; }
    auto c_issue = [&](const u16* W, unsigned chunk, unsigned dst) {
        const char* sb = (const char*)(W + 80 * c4) + (size_t)chunk * (128 * 1280);
#pragma unroll
        for (unsigned i = 0; i < 5; ++i) gldsb(sb, coff[i], lds0 + dst + (4 * i + a) * 1024);
    };
    c_issue(P.woA, 0, AA); c_issue(P.woA, 1, AA + A_SLOT);
    ok &= poll_tags<1>(rw, WS_FA + (rt64 * 8 + (lane & 7)) * 8, lane < 8, tag);
    const int fo = WS_O + ((4 * rt64 + a) * 40 * 64 + lane) * 16;
    bf16x8 of[20];
#pragma unroll
    for (int ks = 0; ks < 20; ++ks) of[ks] = __builtin_bit_cast(bf16x8, ld128(rw, fo + ks * 1024));
    const float gg = __uint_as_float(ld32(rw, WS_G + (s0 + 16 * a + i16) * 4));
    const unsigned cbo = (8 * g + q4) * 160 + p4 * 8, ct0 = 4 * (0 ^ (g & 1)) * 160, ct1 = 4 * (1 ^ (g & 1)) * 160;
    f32x4 ca[5], cb[5];
#pragma unroll
    for (int j = 0; j < 5; ++j) { ca[j] = (f32x4){0.f, 0.f, 0.f, 0.f}; cb[j] = ca[j]; }
#define C_COMPUTE(ACC, base, k0) do { const lds_cptr c0_ = (lds_cptr)lds + (base) + cbo + ct0, c1_ = (lds_cptr)lds + (base) + cbo + ct1; \
        _Pragma("unroll") for (int u = 0; u < 4; ++u) { _Pragma("unroll") for (int j = 0; j < 5; ++j) ACC[j] = MFMA16(ldstr2(c0_ + u * 5120 + j * 32, c1_ + u * 5120 + j * 32), of[(k0) + u], ACC[j]); \
            asm volatile("" : "+v"(ACC[0]), "+v"(ACC[1]), "+v"(ACC[2]), "+v"(ACC[3]), "+v"(ACC[4])); SBAR(); } } while (0)
    VMW(0); GBAR(ab, an, 4); c_issue(P.woA, 2, AA + 2 * A_SLOT); C_COMPUTE(ca, AA, 0);
    GBAR(ab, an, 4); c_issue(P.woA, 3, AA); C_COMPUTE(ca, AA + A_SLOT, 4);
    VMW(5); GBAR(ab, an, 4); c_issue(P.woA, 4, AA + A_SLOT); C_COMPUTE(ca, AA + 2 * A_SLOT, 8);
    VMW(5); GBAR(ab, an, 4); c_issue(P.woB, 0, AA + 2 * A_SLOT); C_COMPUTE(ca, AA, 12);
    VMW(5); GBAR(ab, an, 4); c_issue(P.woB, 1, AA); C_COMPUTE(ca, AA + A_SLOT, 16);
    GBAR(ab, an, 4); c_issue(P.woB, 2, AA + A_SLOT);
    PSTAMP(5);
    lds_wait(ctl + C_BD, 8u);
    c_issue(P.woB, 3, C_S3); c_issue(P.woB, 4, C_S4);
    if (a == 0) { lds_wait(ctl + C_D, 4u); if (lane == 0) st64(rw, WS_FO + (rt64 * 8 + c4) * 8, tagv); }
    PSTAMP(6);
    ok &= poll_tags<1>(rw, WS_FO + (rt64 * 8 + (lane & 7)) * 8, lane < 8, tag);
    VMW(0);
    lds_signal(ab, lane); an += 4;
    PSTAMP(7);
#pragma unroll
    for (int ks = 0; ks < 20; ++ks) of[ks] = __builtin_bit_cast(bf16x8, ld128(rw, fo + (20 + ks) * 1024));
    lds_wait(ab, an);
    C_COMPUTE(cb, AA + 2 * A_SLOT, 0); C_COMPUTE(cb, AA, 4); C_COMPUTE(cb, AA + A_SLOT, 8); C_COMPUTE(cb, C_S3, 12); C_COMPUTE(cb, C_S4, 16);
    if (!ok && lane == 0) ctl[C_BAD] = 1u;
    GBAR(ab, an, 4);
    const float poison = rfl(ctl[C_BAD]) ? __builtin_nanf("") : 0.f;
#pragma unroll
    for (int j = 0; j < 5; ++j) { const f32x4 v = ca[j] * gg + cb[j] * (1.f - gg) + poison;
        *(u32x2*)(P.out + (size_t)(s0 + 16 * a + i16) * 640 + 80 * c4 + 16 * j + 4 * g) = (u32x2){pk2(v[0], v[1]), pk2(v[2], v[3])}; }
    PSTAMP(8);
}

__global__ __launch_bounds__(768, 3) void fused(Params P) {
    extern __shared__ __attribute__((aligned(16))) char lds[];
    PDECL
    PSTAMP(0);
    char* const ws = P.ws;
    const unsigned tid = threadIdx.x, lane = tid & 63, wid = rfl(tid >> 6);
    const unsigned b = blockIdx.x & 255, xg = b & 7, li = b >> 3;
    const unsigned rt64 = (xg >> 2) * 16 + (li >> 1), c4 = (xg & 3) * 2 + (li & 1);
    const rsrc_t rw = __builtin_amdgcn_make_buffer_rsrc(ws, 0, WS_END, 0x00020000);
    LAS unsigned* ctl = (LAS unsigned*)((lds_ptr)lds + CTL);
    if (tid < 32) ctl[tid] = 0u;
    LBAR();
    u64* stp = nullptr;
    if (wid < 8) b_waves(P, lds, ctl, rw, wid & 7, lane, rt64, c4, stp);
    else { if (APRIO) __builtin_amdgcn_s_setprio(APRIO); a_waves(P, lds, ctl, rw, wid & 3, lane, b, li, rt64, c4, stp); }
    PFLUSH();
}

static inline Params mk_params(void* const* d_in, void* d_out, void* d_ws) {
    Params P; P.x = (const u16*)d_in[0]; P.wqkv = (const u16*)d_in[1]; P.woA = (const u16*)d_in[2]; P.wq = (const u16*)d_in[4]; P.wk = (const u16*)d_in[5]; P.wv = (const u16*)d_in[6];
    P.woB = (const u16*)d_in[7]; P.gw = (const u16*)d_in[8]; P.ws = (char*)d_ws; P.out = (u16*)d_out; return P; }
extern "C" void kernel_launch(void* const* d_in, const int* in_sizes, int n_in, void* d_out, int out_size, void* d_ws, size_t ws_size, hipStream_t stream) {
    (void)hipFuncSetAttribute((const void*)fused, hipFuncAttributeMaxDynamicSharedMemorySize, LDS_TOTAL);
    fused<<<256, 768, LDS_TOTAL, stream>>>(mk_params(d_in, d_out, d_ws));
}
#define ST_LAUNCH(d_in, d_out, ws, stream) fused<<<256, 768, LDS_TOTAL, stream>>>(mk_params(d_in, d_out, ws))
```

```cpp
#include <hip/hip_runtime.h>
#include <stdint.h>
#define POLL_SLEEP 4
#define POLL_DELAY lds_wait4<true>(ctl + C_QL, 8u)
#define A_PACE (void)0
#define APRIO 1

typedef unsigned short u16;
typedef unsigned long long u64;
typedef short bf16x8 __attribute__((ext_vector_type(8)));
typedef short s16x4 __attribute__((ext_vector_type(4)));
typedef float f32x16 __attribute__((ext_vector_type(16)));
typedef float f32x4 __attribute__((ext_vector_type(4)));
typedef float f32x2 __attribute__((ext_vector_type(2)));
typedef unsigned u32x4 __attribute__((ext_vector_type(4)));
typedef unsigned u32x2 __attribute__((ext_vector_type(2)));
typedef __bf16 bf2_t __attribute__((ext_vector_type(2)));
#define LAS __attribute__((address_space(3)))
#define GAS __attribute__((address_space(1)))
typedef LAS const char* lds_cptr;
typedef LAS char* lds_ptr;
typedef __amdgpu_buffer_rsrc_t rsrc_t;

__device__ __forceinline__ float bf2f(u16 b) { return __uint_as_float(((unsigned)b) << 16); }
__device__ __forceinline__ unsigned pk2(float lo, float hi) { f32x2 v = {lo, hi}; bf2_t r = __builtin_convertvector(v, bf2_t); return __builtin_bit_cast(unsigned, r); }
__device__ __forceinline__ u16 f2bf(float f) { return (u16)(pk2(f, 0.f) & 0xffffu); }
__device__ __forceinline__ float lo2f(unsigned u) { return __uint_as_float(u << 16); }
__device__ __forceinline__ float hi2f(unsigned u) { return __uint_as_float(u & 0xffff0000u); }
__device__ __forceinline__ float dot2(unsigned a, unsigned b, float c) { return __builtin_amdgcn_fdot2_f32_bf16(__builtin_bit_cast(bf2_t, a), __builtin_bit_cast(bf2_t, b), c, false); }

constexpr int S = 2048, D = 640, H = 32, DH = 20;
constexpr float SCALE = 0.22360679774997896f, LOG2E = 1.4426950408889634f;
constexpr float QSCALE = SCALE * LOG2E;

constexpr int STG_KM = 0, STG_KT = 8192, STG_VT = 12288, VT_ROW = 528, STG_BYTES = 24576;
constexpr int WS_FKV_AT = 262144;
constexpr int WS_EPOCH = 0;
constexpr int WS_FKV = WS_FKV_AT;
constexpr int WS_FO = 256;
constexpr int WS_FA = WS_FO + 2048;
constexpr int WS_NRM = 8192;
constexpr int WS_G = WS_NRM + 2048;
constexpr int WS_KV = 32768;
static_assert(WS_KV + 8 * STG_BYTES <= WS_FKV_AT, "map");
constexpr int WS_O = WS_FKV_AT + 32768;
constexpr int WS_END = WS_O + S * 1280 * 2;
constexpr int AUX_SC1 = 16;

#define MFMA32(a, b, c) __builtin_amdgcn_mfma_f32_32x32x16_bf16((a), (b), (c), 0, 0, 0)
#define MFMA16(a, b, c) __builtin_amdgcn_mfma_f32_16x16x32_bf16((a), (b), (c), 0, 0, 0)
#define SBAR() __builtin_amdgcn_sched_barrier(0)
#define EXP2(x) __builtin_amdgcn_exp2f(x)
#define WAIT_BAR(N) asm volatile("s_waitcnt vmcnt(" #N ")\n\ts_barrier" ::: "memory")
#define LBAR() asm volatile("s_waitcnt lgkmcnt(0)\n\ts_barrier" ::: "memory")
#define FULLBAR() asm volatile("s_waitcnt vmcnt(0) lgkmcnt(0)\n\ts_barrier" ::: "memory")
__device__ __forceinline__ float wave_max_pos(float f) {
    unsigned x = __float_as_uint(f);
    x = max(x, (unsigned)__builtin_amdgcn_update_dpp(0, (int)x, 0x111, 0xf, 0xf, false));
    x = max(x, (unsigned)__builtin_amdgcn_update_dpp(0, (int)x, 0x112, 0xf, 0xf, false));
    x = max(x, (unsigned)__builtin_amdgcn_update_dpp(0, (int)x, 0x114, 0xf, 0xf, false));
    x = max(x, (unsigned)__builtin_amdgcn_update_dpp(0, (int)x, 0x118, 0xf, 0xf, false));
    x = max(x, (unsigned)__builtin_amdgcn_update_dpp(0, (int)x, 0x142, 0xa, 0xf, false));
    x = max(x, (unsigned)__builtin_amdgcn_update_dpp(0, (int)x, 0x143, 0xc, 0xf, false));
    return __uint_as_float((unsigned)__builtin_amdgcn_readlane((int)x, 63));
}
__device__ __forceinline__ float sum16(float f) { auto r = __builtin_amdgcn_permlane16_swap(__float_as_uint(f), __float_as_uint(f), false, false); return __uint_as_float(r[0]) + __uint_as_float(r[1]); }
__device__ __forceinline__ float sum32(float f) { auto r = __builtin_amdgcn_permlane32_swap(__float_as_uint(f), __float_as_uint(f), false, false); return __uint_as_float(r[0]) + __uint_as_float(r[1]); }
__device__ __forceinline__ float max32(float f) { auto r = __builtin_amdgcn_permlane32_swap(__float_as_uint(f), __float_as_uint(f), false, false); return fmaxf(__uint_as_float(r[0]), __uint_as_float(r[1])); }
__device__ __forceinline__ int swap23(int x) { return (x & ~12) | ((x & 4) << 1) | ((x & 8) >> 1); }
__device__ __forceinline__ void glds16(const void* g, unsigned lds_base) {
    unsigned sv; asm volatile("s_mov_b32 %0, m0\n\ts_mov_b32 m0, %2\n\ts_nop 0\n\tglobal_load_lds_dwordx4 %1, off\n\ts_mov_b32 m0, %0" : "=&s"(sv) : "v"(g), "s"(lds_base) : "memory"); }
__device__ __forceinline__ void gldsb(const void* sbase, unsigned voff, unsigned lds_base) {
    unsigned sv; asm volatile("s_mov_b32 %0, m0\n\ts_mov_b32 m0, %3\n\ts_nop 0\n\tglobal_load_lds_dwordx4 %1, %2\n\ts_mov_b32 m0, %0" : "=&s"(sv) : "v"(voff), "s"(sbase), "s"(lds_base) : "memory"); }
__device__ __forceinline__ void gldsb_sc1(const void* sbase, unsigned voff, unsigned lds_base) {
    unsigned sv; asm volatile("s_mov_b32 %0, m0\n\ts_mov_b32 m0, %3\n\ts_nop 0\n\tglobal_load_lds_dwordx4 %1, %2 sc1\n\ts_mov_b32 m0, %0" : "=&s"(sv) : "v"(voff), "s"(sbase), "s"(lds_base) : "memory"); }
__device__ __forceinline__ bf16x8 lds128(lds_cptr p) { return *(const LAS bf16x8*)p; }
typedef short v4i16_t __attribute__((ext_vector_type(4)));
__device__ __forceinline__ s16x4 ldstr(lds_cptr p) { return __builtin_bit_cast(s16x4, __builtin_amdgcn_ds_read_tr16_b64_v4i16((LAS v4i16_t*)p)); }
__device__ __forceinline__ bf16x8 ldstr2(lds_cptr p0, lds_cptr p1) { const s16x4 a = ldstr(p0), b = ldstr(p1); return __builtin_shufflevector(a, b, 0, 1, 2, 3, 4, 5, 6, 7); }
__device__ __forceinline__ unsigned rfl(unsigned x) { return (unsigned)__builtin_amdgcn_readfirstlane(x); }
__device__ __forceinline__ void st32(rsrc_t r, int off, unsigned v) { __builtin_amdgcn_raw_buffer_store_b32(v, r, off, 0, AUX_SC1); }
__device__ __forceinline__ void st64(rsrc_t r, int off, u32x2 v) { __builtin_amdgcn_raw_buffer_store_b64(v, r, off, 0, AUX_SC1); }
__device__ __forceinline__ void st128(rsrc_t r, int off, u32x4 v) { __builtin_amdgcn_raw_buffer_store_b128(v, r, off, 0, AUX_SC1); }
__device__ __forceinline__ unsigned ld32(rsrc_t r, int off) { return __builtin_amdgcn_raw_buffer_load_b32(r, off, 0, AUX_SC1); }
__device__ __forceinline__ u32x2 ld64(rsrc_t r, int off) { return __builtin_amdgcn_raw_buffer_load_b64(r, off, 0, AUX_SC1); }
__device__ __forceinline__ u32x4 ld128(rsrc_t r, int off) { return __builtin_amdgcn_raw_buffer_load_b128(r, off, 0, AUX_SC1); }

#define PSTAMP(i)
#define PDECL
#define PFLUSH()

#define QT(i)
#define QFLUSH()
struct Params { const u16 *x, *wqkv, *woA, *wq, *wk, *wv, *woB, *gw; char* ws; u16* out; };

constexpr int LDS_TOTAL = 163840, CTL = LDS_TOTAL - 128, DUMMY = CTL - 256;
constexpr int KVB = 2 * STG_BYTES;
constexpr int Q_SLOT = 18432, Q_W = 8192, Q_ST = 73728, QB_ROW = 208;
constexpr int AA = 98304;
constexpr int P0W = AA, P0X = AA + 25664, P0G = P0X + 20480;
constexpr int A_SLOT = 20480, A_X = 15360;
constexpr int ST_QK = AA, ST_V = AA + 32000, QK_ROW = 400, V_ROW = 336;
constexpr int C_S3 = 49152, C_S4 = 69632;
constexpr int C_FALL = 0, C_BB = 1, C_DONE = 2, C_LAND = 4, C_X = 6, C_BD = 7, C_P = 8, C_D = 12, C_AB = 13, C_P0D = 14, C_KVOK = 16, C_BAD = 17, C_P0I = 18, C_QL = 20, C_QD = 24, C_TAG = 28, C_QS = 19;
constexpr unsigned CODE_LINES = 232;
constexpr unsigned SPIN_CAP = 1u << 18;

template <int N, int SLEEP = 4>
__device__ __forceinline__ bool poll_tags(rsrc_t r, int off, bool active, u64 tag) {
#pragma unroll 1
    for (unsigned spins = 0; spins < SPIN_CAP; ++spins) {
        bool ok = true;
        if (active) {
#pragma unroll
            for (int i = 0; i < N; ++i) { const u32x2 v = ld64(r, off + 8 * i); ok &= (v[0] == (unsigned)tag) & (v[1] == (unsigned)(tag >> 32)); }
        }
        if (__all(ok)) return true;
        __builtin_amdgcn_s_sleep(SLEEP);
    }
    return false;
}
__device__ __forceinline__ void lds_signal(LAS unsigned* cnt, int lane) {
    (void)lane;
    __builtin_amdgcn_sched_barrier(0);
    asm volatile("s_mov_b64 exec, 1\n\tds_add_u32 %0, %1\n\ts_mov_b64 exec, -1\n\ts_nop 3" :: "v"((unsigned)(uintptr_t)cnt), "v"(1u) : "memory");
    __builtin_amdgcn_sched_barrier(0); }
template <bool SLEEP = true>
__device__ __forceinline__ void lds_wait(LAS unsigned* cnt, unsigned n) {
    unsigned v, sv, left;
    if (SLEEP)
        asm volatile("s_mov_b32 %2, 0x100000\n1:\n\tds_read_b32 %0, %3\n\ts_waitcnt lgkmcnt(0)\n\tv_readfirstlane_b32 %1, %0\n\ts_cmp_ge_u32 %1, %4\n\ts_cbranch_scc1 2f\n\ts_sleep 1\n\ts_sub_u32 %2, %2, 1\n\ts_cbranch_scc0 1b\n2:"
                     : "=&v"(v), "=&s"(sv), "=&s"(left) : "v"((unsigned)(uintptr_t)cnt), "s"(n) : "memory", "scc");
    else
        asm volatile("s_mov_b32 %2, 0x100000\n1:\n\tds_read_b32 %0, %3\n\ts_waitcnt lgkmcnt(0)\n\tv_readfirstlane_b32 %1, %0\n\ts_cmp_ge_u32 %1, %4\n\ts_cbranch_scc1 2f\n\ts_sub_u32 %2, %2, 1\n\ts_cbranch_scc0 1b\n2:"
                     : "=&v"(v), "=&s"(sv), "=&s"(left) : "v"((unsigned)(uintptr_t)cnt), "s"(n) : "memory", "scc");
}
template <bool SLEEP = false>
__device__ __forceinline__ void lds_wait4(LAS unsigned* cnt, unsigned n) {
#pragma unroll 1
    for (unsigned spins = 0; spins < (1u << 20); ++spins) {
        const u32x4 v = *(volatile LAS u32x4*)cnt;
        const unsigned lo = min(min(v[0], v[1]), min(v[2], v[3]));
        if (rfl(lo) >= n) break;
        if (SLEEP) __builtin_amdgcn_s_sleep(1);
    }
    asm volatile("" ::: "memory");
}
#define VMW(N) asm volatile("s_waitcnt vmcnt(" #N ")" ::: "memory")
#define GBAR(cnt, n, step) do { lds_signal(cnt, lane); n += (step); lds_wait<false>(cnt, n); } while (0)
#define GBARS(cnt, n, step) do { lds_signal(cnt, lane); n += (step); lds_wait<true>(cnt, n); } while (0)

#define KV_GLDS gldsb_sc1
__device__ __forceinline__ void att_stage_load(const char* kv, int st, unsigned ldsbuf, int w4, int lane) {
    const char* src = kv + (size_t)st * STG_BYTES + w4 * 1024;
#pragma unroll
    for (int i = 0; i < 6; ++i) KV_GLDS(src + i * 4096, lane * 16, ldsbuf + (w4 + 4 * i) * 1024);
}

__device__ __forceinline__ void att_maxpass(const char* kv, char* ldsh, LAS unsigned* bb, unsigned& bn, int kh, int w4, int lane, const bf16x8 (&qm)[2], const bf16x8 (&qt)[2][2], float (&mx)[2]) {
    const unsigned lds0 = (unsigned)(uintptr_t)ldsh;
    const lds_cptr kbase = (lds_cptr)ldsh + lane * 16;
    const f32x16 zero = {};
#pragma unroll 1
    for (int rnd = 0; rnd < 3; ++rnd) {
        if (rnd) { GBAR(bb, bn, 8); att_stage_load(kv, 4 * kh + 2 * (rnd & 1), lds0, w4, lane); att_stage_load(kv, 4 * kh + 2 * (rnd & 1) + 1, lds0 + KVB, w4, lane); }
        VMW(0); GBAR(bb, bn, 8);
        if (rnd < 2) {
#pragma unroll 1
            for (int pr = 0; pr < 8; ++pr) {
                const lds_cptr kb = kbase + (pr >> 2) * KVB; const int p = pr & 3;
                const bf16x8 kt = lds128(kb + STG_KT + p * 1024), k0 = lds128(kb + STG_KM + (2 * p) * 1024), k1 = lds128(kb + STG_KM + (2 * p + 1) * 1024);
#pragma unroll
                for (int h = 0; h < 2; ++h) {
                    f32x16 s0 = MFMA32(k0, qm[h], zero); s0 = MFMA32(kt, qt[h][0], s0);
                    f32x16 s1 = MFMA32(k1, qm[h], zero); s1 = MFMA32(kt, qt[h][1], s1);
#pragma unroll
                    for (int i = 0; i < 16; ++i) mx[h] = fmaxf(mx[h], fmaxf(s0[i], s1[i]));
                }
            }
        }
    }
}

__device__ __forceinline__ void att_main(const char* kv, char* ldsh, LAS unsigned* ctl, int kh, int w4, int lane, const bf16x8 (&qm)[2], const bf16x8 (&qt)[2][2], f32x16& O0, f32x16& O1) {
    const unsigned lds0 = (unsigned)(uintptr_t)ldsh;
    LAS unsigned* cdone = ctl + C_DONE + kh; LAS unsigned* cland = ctl + C_LAND + kh;
    const int r = lane & 31, hh = lane >> 5;
    const int voff = r <= 20 ? r * VT_ROW : (r < 28 ? 21 * VT_ROW : 21 * VT_ROW + 112);
    const lds_cptr kbase = (lds_cptr)ldsh + lane * 16;
    const lds_cptr vbase = (lds_cptr)ldsh + STG_VT + voff + hh * 16;
    const f32x16 zero = {};
    f32x16 sA, sB; u32x4 pA0, pA1, pB0, pB1; bf16x8 kmA, kmB, ktA, ktB, vA0, vA1, vB0, vB1;
    ktA = lds128(kbase + STG_KT); kmA = lds128(kbase + STG_KM);
    sA = MFMA32(kmA, qm[0], zero); sA = MFMA32(ktA, qt[0][0], sA);
    pB0 = (u32x4){0u, 0u, 0u, 0u}; pB1 = pB0; vB0 = kmA; vB1 = kmA; ktB = ktA; vA0 = kmA; vA1 = kmA; kmB = kmA;
    float ee0 = 0.f, ee1 = 0.f, ee2 = 0.f, ee3 = 0.f;
#define GRP(MF, LD, SC, I, PD, J) do { MF; LD; const float e0_ = EXP2(SC[I]); unsigned a_ = pk2(ee0, ee1); const float e1_ = EXP2(SC[I + 1]); unsigned b_ = pk2(ee2, ee3); const float e2_ = EXP2(SC[I + 2]), e3_ = EXP2(SC[I + 3]); \
        ee0 = e0_; ee1 = e1_; ee2 = e2_; ee3 = e3_; asm volatile("" : "+v"(a_), "+v"(b_), "+v"(ee0), "+v"(ee1), "+v"(ee2), "+v"(ee3)); PD[J] = a_; PD[J + 1] = b_; SBAR(); } while (0)
#define TILE(KM, KMN, KT, VJ, KTN, VJN, VC0, VC1, VP0, VP1, VADDR, LDK, LDT) do { \
        GRP(sB = MFMA32(KM, qm[1], zero), VC0 = lds128(VADDR), sA, 0, pB1, 2); \
        GRP(O1 = MFMA32(VP0, __builtin_bit_cast(bf16x8, pB0), O1), VC1 = lds128((VADDR) + 32), sA, 4, pA0, 0); \
        GRP(sB = MFMA32(KT, qt[1][VJ], sB), LDK, sA, 8, pA0, 2); \
        GRP(O1 = MFMA32(VP1, __builtin_bit_cast(bf16x8, pB1), O1), LDT, sA, 12, pA1, 0); \
        GRP(sA = MFMA32(KMN, qm[0], zero), (void)0, sB, 0, pA1, 2); \
        GRP(O0 = MFMA32(VC0, __builtin_bit_cast(bf16x8, pA0), O0), (void)0, sB, 4, pB0, 0); \
        GRP(sA = MFMA32(KTN, qt[0][VJN], sA), (void)0, sB, 8, pB0, 2); \
        GRP(O0 = MFMA32(VC1, __builtin_bit_cast(bf16x8, pA1), O0), (void)0, sB, 12, pB1, 0); } while (0)
#pragma unroll 1
    for (int st = 0; st < 4; ++st) {
        const int buf = st & 1;
        const lds_cptr kb = kbase + buf * KVB, vb = vbase + buf * KVB, kn = kbase + (buf ^ 1) * KVB;
        TILE(kmA, kmB, ktA, 0, ktA, 1, vA0, vA1, vB0, vB1, vb + 0 * 64, kmB = lds128(kb + STG_KM + 1 * 1024), (void)0);
        if (st) { lds_signal(st == 3 ? ctl + C_X : cdone, lane); SBAR(); }
        TILE(kmB, kmA, ktA, 1, ktB, 0, vB0, vB1, vA0, vA1, vb + 1 * 64, kmA = lds128(kb + STG_KM + 2 * 1024), ktB = lds128(kb + STG_KT + 1024));
        if (st == 1 || st == 2) { lds_wait(cdone, 4u * st); att_stage_load(kv, 4 * kh + st + 1, lds0 + (buf ^ 1) * KVB, w4, lane); SBAR(); }
        TILE(kmA, kmB, ktB, 0, ktB, 1, vA0, vA1, vB0, vB1, vb + 2 * 64, kmB = lds128(kb + STG_KM + 3 * 1024), (void)0);
        TILE(kmB, kmA, ktB, 1, ktA, 0, vB0, vB1, vA0, vA1, vb + 3 * 64, kmA = lds128(kb + STG_KM + 4 * 1024), ktA = lds128(kb + STG_KT + 2048));
        TILE(kmA, kmB, ktA, 0, ktA, 1, vA0, vA1, vB0, vB1, vb + 4 * 64, kmB = lds128(kb + STG_KM + 5 * 1024), (void)0);
        TILE(kmB, kmA, ktA, 1, ktB, 0, vB0, vB1, vA0, vA1, vb + 5 * 64, kmA = lds128(kb + STG_KM + 6 * 1024), ktB = lds128(kb + STG_KT + 3072));
        if (st < 3) { VMW(0); lds_signal(cland, lane); SBAR(); }
        TILE(kmA, kmB, ktB, 0, ktB, 1, vA0, vA1, vB0, vB1, vb + 6 * 64, kmB = lds128(kb + STG_KM + 7 * 1024), (void)0);
        if (st < 3) { lds_wait(cland, 4u * (st + 1)); SBAR(); }
        TILE(kmB, kmA, ktB, 1, ktA, 0, vB0, vB1, vA0, vA1, vb + 7 * 64, kmA = lds128(kn + STG_KM + 0 * 1024), ktA = lds128(kn + STG_KT));
    }
    pB1[2] = pk2(ee0, ee1); pB1[3] = pk2(ee2, ee3);
    O1 = MFMA32(vB0, __builtin_bit_cast(bf16x8, pB0), O1);
    O1 = MFMA32(vB1, __builtin_bit_cast(bf16x8, pB1), O1);
#undef GRP
#undef TILE
}

__device__ __forceinline__ int ofrag_off(int s, int k) { return WS_O + ((((s >> 4) * 40 + (k >> 5)) * 64 + ((k >> 3) & 3) * 16 + (s & 15)) * 16 + (k & 7) * 2); }
__device__ __forceinline__ void touch(const void* g, unsigned lds_dummy) {
    unsigned sv; asm volatile("s_mov_b32 %0, m0\n\ts_mov_b32 m0, %2\n\ts_nop 0\n\tglobal_load_lds_dword %1, off\n\ts_mov_b32 m0, %0" : "=&s"(sv) : "v"(g), "s"(lds_dummy) : "memory"); }

__device__ __forceinline__ void b_waves(const Params& P, char* lds, LAS unsigned* ctl, rsrc_t rw, unsigned wid, unsigned lane, unsigned b, unsigned li, unsigned rt64, unsigned c4, u64* stamps) {
    char* const ws = P.ws;
    const unsigned lds0 = (unsigned)(uintptr_t)lds;
    const int s0 = 64 * rt64;
    const unsigned i16 = lane & 15, g = lane >> 4, q4 = i16 >> 2, p4 = i16 & 3;
    unsigned bn = 0; LAS unsigned* bb = ctl + C_BB;
    f32x4 qa[5];
#pragma unroll
    for (int j = 0; j < 5; ++j) qa[j] = (f32x4){0.f, 0.f, 0.f, 0.f};
    if (wid >= 4) {
        const unsigned l = wid - 4;
        unsigned off[5];
#pragma unroll
        for (unsigned j = 0; j < 5; ++j) { const unsigned id = l + 4 * j;
            if (j < 2) { const unsigned p = id * 64 + lane, row = p >> 3, ch = (p & 7) ^ ((row >> 1) & 7); off[j] = row * 1280 + ch * 16; }
            else { const unsigned p = ((id < 18 ? id : 8) - 8) * 64 + lane, pos = p / 10, j10 = p - pos * 10, k = pos ^ (4 * ((pos >> 3) & 1)); off[j] = k * 1280 + j10 * 16; } }
        const char* xs = (const char*)(P.x + (size_t)s0 * D); const char* wsq = (const char*)(P.wq + 80 * c4);
        int slot = 0;
        lds_wait<false>(ctl + C_P0I, 4u);
#pragma unroll 1
        for (int t = 0; t < 10; ++t) {
            if (t >= 5) lds_wait4(ctl + C_QD, t - 4);
            const unsigned so = lds0 + slot * Q_SLOT + l * 1024;
            gldsb(xs, off[0], so); gldsb(xs, off[1], so + 4096); gldsb(wsq, off[2], so + 8192); gldsb(wsq, off[3], so + 12288);
            if (l < 2) gldsb(wsq, off[4], so + 16384);
            xs += 128; wsq += 64 * 1280;
            if (t >= 3) { VMW(12); lds_signal(ctl + C_QL + l, lane); }
            slot = slot == 4 ? 0 : slot + 1;
        }
        VMW(8); lds_signal(ctl + C_QL + l, lane); VMW(4); lds_signal(ctl + C_QL + l, lane); VMW(0); lds_signal(ctl + C_QL + l, lane);
        PSTAMP(1);
    } else {
        const lds_cptr qx0 = (lds_cptr)lds + (16 * wid + i16) * 128 + ((g ^ (i16 >> 1)) * 16), qx1 = (lds_cptr)lds + (16 * wid + i16) * 128 + (((4 + g) ^ (i16 >> 1)) * 16);
        const lds_cptr qw0 = (lds_cptr)lds + Q_W + (8 * g + q4 + 4 * (0 ^ (g & 1))) * 160 + p4 * 8, qw1 = (lds_cptr)lds + Q_W + (8 * g + q4 + 4 * (1 ^ (g & 1))) * 160 + p4 * 8;
        {
            const unsigned ri = li >> 1, cc = li & 1, l2 = lane - 32;
#pragma unroll 1
            for (unsigned j = 5 + wid; j < 10; j += 4) {
                const char* p = lane < 32 ? (const char*)(P.x + (size_t)(s0 + 2 * lane + cc) * D) + j * 128 : (const char*)(P.wq + 80 * c4) + (size_t)((64 * j + ri + 16 * ((l2 >> 1) & 3)) * 1280) + (l2 & 1) * 156;
                touch(p, lds0 + DUMMY); } }
        PSTAMP(1);
        bf16x8 xa0, xa1, wa[10], xb0, xb1, wb_[10];
#define Q_READ(X0, X1, W, t_, so_) do { lds_wait4(ctl + C_QL, (t_) + 1); X0 = lds128(qx0 + (so_)); X1 = lds128(qx1 + (so_)); \
            _Pragma("unroll") for (int j = 0; j < 10; ++j) W[j] = ldstr2(qw0 + (so_) + (j / 5) * 5120 + (j % 5) * 32, qw1 + (so_) + (j / 5) * 5120 + (j % 5) * 32); SBAR(); } while (0)
#define Q_MUL(X0, X1, W) do { _Pragma("unroll") for (int j = 0; j < 5; ++j) qa[j] = MFMA16(W[j], X0, qa[j]); _Pragma("unroll") for (int j = 0; j < 5; ++j) qa[j] = MFMA16(W[5 + j], X1, qa[j]); \
            asm volatile("" : "+v"(qa[0]), "+v"(qa[1]), "+v"(qa[2]), "+v"(qa[3]), "+v"(qa[4])); SBAR(); } while (0)
#define Q_DONE() do { asm volatile("s_waitcnt lgkmcnt(0)" ::: "memory"); lds_signal(ctl + C_QD + wid, lane); } while (0)
        Q_READ(xa0, xa1, wa, 0, 0); Q_DONE();
        int slot = 1;
#pragma unroll 1
        for (int t = 0; t < 10; t += 2) {
            Q_READ(xb0, xb1, wb_, t + 1, slot * Q_SLOT); slot = slot == 4 ? 0 : slot + 1;
            Q_MUL(xa0, xa1, wa); Q_DONE();
            if (t + 2 < 10) { Q_READ(xa0, xa1, wa, t + 2, slot * Q_SLOT); slot = slot == 4 ? 0 : slot + 1; }
            Q_MUL(xb0, xb1, wb_);
            if (t + 2 < 10) Q_DONE();
        }
#undef Q_READ
#undef Q_MUL
#undef Q_DONE
    }
    PSTAMP(2);
    if (wid == 4) {
        lds_wait<true>(ctl + C_TAG, 1u);
        const u64 tag = (u64)ctl[C_TAG + 1] | ((u64)ctl[C_TAG + 2] << 32);
        bool ok = false;
#pragma unroll 1
        for (unsigned spins = 0; spins < SPIN_CAP; ++spins) {
            const u32x4 v = ld128(rw, WS_FKV + li * 1024 + lane * 16);
            if (__all((v[0] == (unsigned)tag) & (v[1] == (unsigned)tag) & (v[2] == (unsigned)tag) & (v[3] == (unsigned)tag))) { ok = true; break; }
            __builtin_amdgcn_s_sleep(POLL_SLEEP);
        }
        if (!ok && lane == 0) ctl[C_BAD] = 1u;
        lds_signal(ctl + C_KVOK, lane);
        if (b == 0 && lane == 0) st64(rw, WS_EPOCH, (u32x2){(unsigned)tag, (unsigned)(tag >> 32)});
    }
    const char* kv = ws + WS_KV;
    const unsigned w4 = wid & 3, kh = wid >> 2;
    char* ldsh = lds + kh * STG_BYTES;
    const unsigned ldsh0 = lds0 + kh * STG_BYTES;
    u32x4 nr0, nr1;
    if (wid < 4) {
        lds_wait4(ctl + C_QD, 10u);
#pragma unroll
        for (int j = 0; j < 5; ++j) { const unsigned cq = 4 * j + g, hl = (cq * 13) >> 6, dq = cq - 5 * hl;
            *(LAS u32x2*)((lds_ptr)lds + Q_ST + (16 * wid + i16) * QB_ROW + hl * 48 + dq * 8) = (u32x2){pk2(qa[j][0] * QSCALE, qa[j][1] * QSCALE), pk2(qa[j][2] * QSCALE, qa[j][3] * QSCALE)}; }
        lds_signal(ctl + C_QS, lane);
    } else {
        lds_wait<false>(ctl + C_KVOK, 1u);
        lds_wait4(ctl + C_QD, 8u);
        att_stage_load(kv, 0, lds0, w4, lane); att_stage_load(kv, 4, lds0 + STG_BYTES, w4, lane);
    }
    lds_wait<false>(ctl + C_QS, 4u);
    const int r = lane & 31, hh = lane >> 5;
    bf16x8 qm[2]; u32x2 qtl[2];
#pragma unroll
    for (int h = 0; h < 2; ++h) {
        const lds_cptr qp = (lds_cptr)lds + Q_ST + (32 * (w4 & 1) + r) * QB_ROW + (2 * (w4 >> 1) + h) * 48;
        qm[h] = lds128(qp + hh * 16); qtl[h] = *(const LAS u32x2*)(qp + 32);
    }
    float q2[2];
#pragma unroll
    for (int h = 0; h < 2; ++h) {
        q2[h] = 0.f;
#pragma unroll
        for (int i = 0; i < 8; ++i) { const float v = bf2f((u16)qm[h][i]); q2[h] += v * v; }
        if (hh == 0) { const float a0 = lo2f(qtl[h][0]), a1 = hi2f(qtl[h][0]), a2 = lo2f(qtl[h][1]), a3 = hi2f(qtl[h][1]); q2[h] += a0 * a0 + a1 * a1 + a2 * a2 + a3 * a3; }
        q2[h] = sum32(q2[h]);
    }
    lds_wait<false>(ctl + C_KVOK, 1u);
    PSTAMP(3);
    asm volatile("global_load_dwordx4 %0, %2, %3 sc1\n\tglobal_load_dwordx4 %1, %2, %3 offset:1024 sc1\n\ts_waitcnt vmcnt(0)" : "=&v"(nr0), "=&v"(nr1) : "v"(lane * 16), "s"(ws + WS_NRM) : "memory");
    const float kmax2 = wave_max_pos(fmaxf(__uint_as_float(nr0[0]), __uint_as_float(nr1[0]))) + wave_max_pos(fmaxf(__uint_as_float(nr0[1]), __uint_as_float(nr1[1])));
    const float vmax = wave_max_pos(fmaxf(fmaxf(__uint_as_float(nr0[2]), __uint_as_float(nr1[2])), fmaxf(__uint_as_float(nr0[3]), __uint_as_float(nr1[3]))));
    const float lim = 112.f - fmaxf(0.f, __builtin_amdgcn_logf(fmaxf(vmax, 1.f)));
    const float bnd0 = __builtin_sqrtf(q2[0] * kmax2) * 1.001f + 1.f, bnd1 = __builtin_sqrtf(q2[1] * kmax2) * 1.001f + 1.f;
    GBAR(bb, bn, 8);
    const u32x4 z4 = {0u, 0u, 0u, 0u};
    bf16x8 qt[2][2];
#pragma unroll
    for (int h = 0; h < 2; ++h) { const u32x4 a = {qtl[h][0], qtl[h][1], 0u, 0u}; qt[h][0] = __builtin_bit_cast(bf16x8, hh ? z4 : a); qt[h][1] = __builtin_bit_cast(bf16x8, hh ? a : z4); }
    const f32x16 zero = {};
    float m[2]; bool unsafe = false;
    { const lds_cptr kb = (lds_cptr)ldsh + lane * 16; const bf16x8 k0 = lds128(kb + STG_KM), kt = lds128(kb + STG_KT);
#pragma unroll
      for (int h = 0; h < 2; ++h) {
        f32x16 sc = MFMA32(k0, qm[h], zero); sc = MFMA32(kt, qt[h][0], sc);
        float mm = sc[0];
#pragma unroll
        for (int i = 1; i < 16; ++i) mm = fmaxf(mm, sc[i]);
        mm = max32(mm);
        m[h] = bf2f(f2bf(mm));
        unsafe |= !((h ? bnd1 : bnd0) - m[h] <= lim);
      } }
    if (__any(unsafe) && lane == 0) ctl[C_FALL] = 1u;
    att_stage_load(kv, 4 * kh + 1, ldsh0 + KVB, w4, lane);
    GBAR(bb, bn, 8);
    if (rfl(ctl[C_FALL]) != 0u) {
        att_maxpass(kv, ldsh, bb, bn, kh, w4, lane, qm, qt, m);
#pragma unroll
        for (int h = 0; h < 2; ++h) m[h] = bf2f(f2bf(max32(m[h])));
    }
#pragma unroll
    for (int h = 0; h < 2; ++h) { const unsigned nm = f2bf(-m[h]); const u32x4 a = {qtl[h][0], qtl[h][1], nm, 0u}; qt[h][0] = __builtin_bit_cast(bf16x8, hh ? z4 : a); qt[h][1] = __builtin_bit_cast(bf16x8, hh ? a : z4); }
    f32x16 O[2] = {{}, {}};
    PSTAMP(4);
    att_main(kv, ldsh, ctl, kh, w4, lane, qm, qt, O[0], O[1]);
    PSTAMP(5);
    lds_signal(ctl + C_BD, lane);
    LAS float* xch = (LAS float*)((lds_ptr)lds + w4 * 6656);
    if (kh == 0) {
        lds_wait(ctl + C_X, 8u);
#pragma unroll
        for (int h = 0; h < 2; ++h) {
#pragma unroll
            for (int i = 0; i < 12; ++i) xch[(13 * h + i) * 64 + lane] = O[h][i];
            xch[(13 * h + 12) * 64 + lane] = m[h];
        }
        lds_signal(ctl + C_P + w4, lane);
    } else {
        lds_wait(ctl + C_P + w4, 1u);
#pragma unroll
        for (int h = 0; h < 2; ++h) {
            const float m0 = xch[(13 * h + 12) * 64 + lane];
            const float M = fmaxf(m0, m[h]), f0 = EXP2(m0 - M), f1 = EXP2(m[h] - M);
            float o[12];
#pragma unroll
            for (int i = 0; i < 12; ++i) o[i] = O[h][i] * f1 + xch[(13 * h + i) * 64 + lane] * f0;
            const float inv = __builtin_amdgcn_rcpf(__shfl(o[8], r + 32));
            const int s = s0 + 32 * (w4 & 1) + r, k0 = 640 + (4 * c4 + 2 * (w4 >> 1) + h) * DH;
            st64(rw, ofrag_off(s, k0 + 4 * hh), (u32x2){pk2(o[0] * inv, o[1] * inv), pk2(o[2] * inv, o[3] * inv)});
            st64(rw, ofrag_off(s, k0 + 8 + 4 * hh), (u32x2){pk2(o[4] * inv, o[5] * inv), pk2(o[6] * inv, o[7] * inv)});
            if (hh == 0) st64(rw, ofrag_off(s, k0 + 16), (u32x2){pk2(o[8] * inv, o[9] * inv), pk2(o[10] * inv, o[11] * inv)});
        }
        VMW(0);
        lds_signal(ctl + C_D, lane);
    }
    asm volatile("s_waitcnt lgkmcnt(0)" ::: "memory");
    PSTAMP(6);
}

__device__ __forceinline__ void a_waves(const Params& P, char* lds, LAS unsigned* ctl, rsrc_t rw, unsigned a, unsigned lane, unsigned b, unsigned li, unsigned rt64, unsigned c4, u64* stamps) {
    const unsigned lds0 = (unsigned)(uintptr_t)lds;
    const int s0 = 64 * rt64;
    const unsigned i16 = lane & 15, g = lane >> 4, q4 = i16 >> 2, p4 = i16 & 3;
    unsigned an = 0; LAS unsigned* ab = ctl + C_AB;
    const unsigned part = c4 & 1, r0 = s0 + 16 * (c4 >> 1);
    { const char* wsrc = (const char*)(part ? P.wv : P.wk); const char* xsrc = (const char*)(P.x + (size_t)r0 * D);
#pragma unroll 1
      for (unsigned i = 0; i < 12; ++i) { unsigned id = a + 4 * i; if (id == 47) id = 43;
        if (id < 25) gldsb(wsrc, id * 1024 + lane * 16, lds0 + P0W + id * 1024);
        else if (id < 45) { const unsigned p = (id - 25) * 64 + lane, row = p / 80, cpos = p - row * 80, ch = (cpos & ~15u) | ((cpos & 15) ^ row);
            gldsb(xsrc, row * 1280 + ch * 16, lds0 + P0X + (id - 25) * 1024); }
        else { const unsigned off = (id - 45) * 1024 + lane * 16; gldsb(P.gw, off < 1280 ? off : 1264, lds0 + P0G + (id - 45) * 1024); } } }
    lds_signal(ctl + C_P0I, lane);
    const u32x2 ep = ld64(rw, WS_EPOCH);
    const u64 tag = ((u64)ep[0] | ((u64)ep[1] << 32)) + 0x9E3779B97F4A7C15ull;
    const u32x2 tagv = {(unsigned)tag, (unsigned)(tag >> 32)};
    PSTAMP(1);
    VMW(0);
    GBARS(ab, an, 4);
    PSTAMP(9);
    if (a < 2 || (a == 2 && part == 0)) {
        const lds_cptr p0x = (lds_cptr)lds + P0X + i16 * 1280;
        f32x4 pacc = {0.f, 0.f, 0.f, 0.f};
#define P0XF(ks) lds128(p0x + ((((4 * (ks) + g) & ~15u) | (((4 * (ks) + g) & 15) ^ i16)) * 16))
        const int ks0 = r0 & 255, img = WS_KV + (r0 >> 8) * STG_BYTES, nrm = WS_NRM + (r0 >> 4) * 16;
        if (a == 2) {
            const bf16x8 z = {0, 0, 0, 0, 0, 0, 0, 0};
#pragma unroll
            for (int ks = 0; ks < 20; ++ks) { const bf16x8 gv = lds128((lds_cptr)lds + P0G + (32 * ks + 8 * g) * 2); pacc = MFMA16(i16 == 0 ? gv : z, P0XF(ks), pacc); }
            if (g == 0) st32(rw, WS_G + (r0 + i16) * 4, __float_as_uint(1.f / (1.f + __expf(-pacc[0]))));
        } else {
            const lds_cptr p0w = (lds_cptr)lds + P0W + (8 * g + q4) * 40 + (16 * a + 4 * p4) * 2;
            if (part == 0) {
#pragma unroll
                for (int ks = 0; ks < 20; ++ks) pacc = MFMA16(ldstr2(p0w + ks * 1280, p0w + ks * 1280 + 160), P0XF(ks), pacc);
                const unsigned w0 = pk2(pacc[0], pacc[1]), w1 = pk2(pacc[2], pacc[3]);
                const int key = ks0 + i16, tile = key >> 5, r = swap23(key & 31);
                float n2 = lo2f(w0) * lo2f(w0) + hi2f(w0) * hi2f(w0) + lo2f(w1) * lo2f(w1) + hi2f(w1) * hi2f(w1);
                if (a == 0) { st64(rw, img + STG_KM + tile * 1024 + (g >> 1) * 512 + r * 16 + (g & 1) * 8, (u32x2){w0, w1});
                    n2 = sum16(n2); n2 = sum32(n2); }
                else { if (g == 0) st128(rw, img + STG_KT + (tile >> 1) * 1024 + (tile & 1) * 512 + r * 16, (u32x4){w0, w1, 0x3f80u, 0u}); if (g != 0) n2 = 0.f; }
                n2 = wave_max_pos(n2);
                if (lane == 0) st32(rw, nrm + a * 4, __float_as_uint(n2));
            } else {
#pragma unroll
                for (int ks = 0; ks < 20; ++ks) pacc = MFMA16(P0XF(ks), ldstr2(p0w + ks * 1280, p0w + ks * 1280 + 160), pacc);
                const unsigned w0 = pk2(pacc[0], pacc[1]), w1 = pk2(pacc[2], pacc[3]);
                const int d = 16 * a + i16;
                float vm = fmaxf(fmaxf(fabsf(lo2f(w0)), fabsf(hi2f(w0))), fmaxf(fabsf(lo2f(w1)), fabsf(hi2f(w1))));
                if (d >= 20) vm = 0.f;
                if (d <= 20) st64(rw, img + STG_VT + d * VT_ROW + (ks0 + 4 * g) * 2, d == 20 ? (u32x2){0x3f803f80u, 0x3f803f80u} : (u32x2){w0, w1});
                if (a == 1 && ks0 == 0 && lane < 40) st128(rw, img + STG_VT + 21 * VT_ROW + lane * 16, (u32x4){0u, 0u, 0u, 0u});
                vm = wave_max_pos(vm);
                if (lane == 0) st32(rw, nrm + (2 + a) * 4, __float_as_uint(vm));
            }
        }
#undef P0XF
        PSTAMP(10);
        VMW(0);
    }
    GBARS(ab, an, 4);
    PSTAMP(11);
    bool ok = true;
    if (a == 3) { if (lane < 32) st32(rw, WS_FKV + lane * 1024 + b * 4, tagv[0]);
        if (lane == 0) { ctl[C_TAG + 1] = tagv[0]; ctl[C_TAG + 2] = tagv[1]; }
        lds_signal(ctl + C_TAG, lane); }
    PSTAMP(2);
    unsigned aoff[5];
#pragma unroll
    for (unsigned j = 0; j < 5; ++j) { const unsigned id = a + 4 * j;
        if (id < 15) { const unsigned seg = id / 5, p = (id - 5 * seg) * 64 + lane, pos = p / 10, j10 = p - pos * 10, k = pos ^ (4 * ((pos >> 3) & 1)); aoff[j] = k * 3840 + seg * 1280 + j10 * 16; }
        else { const unsigned p = (id - 15) * 64 + lane, row = p >> 2, ch = (p & 3) ^ (3 * ((row >> 2) & 1)); int gr = s0 - 16 + (int)row; gr = gr < 0 ? 0 : gr; aoff[j] = gr * 1280 + ch * 16; } }
    const char* awb = (const char*)(P.wqkv + 80 * c4); const char* axb = (const char*)P.x;
#define A_ISSUE(slot) do { gldsb(awb, aoff[0], lds0 + AA + (slot) * A_SLOT + a * 1024); gldsb(awb, aoff[1], lds0 + AA + (slot) * A_SLOT + (a + 4) * 1024); gldsb(awb, aoff[2], lds0 + AA + (slot) * A_SLOT + (a + 8) * 1024); \
        gldsb(a < 3 ? awb : axb, aoff[3], lds0 + AA + (slot) * A_SLOT + (a + 12) * 1024); gldsb(axb, aoff[4], lds0 + AA + (slot) * A_SLOT + (a + 16) * 1024); awb += 32 * 3840; axb += 64; } while (0)
    lds_wait4<true>(ctl + C_QL, 10u);
    A_ISSUE(0); A_ISSUE(1);
    f32x4 acc[5][4];
#pragma unroll
    for (int i = 0; i < 5; ++i)
#pragma unroll
        for (int j = 0; j < 4; ++j) acc[i][j] = (f32x4){0.f, 0.f, 0.f, 0.f};
    const lds_cptr ax = (lds_cptr)lds + AA + A_X + i16 * 64 + ((g ^ (3 * ((i16 >> 2) & 1))) * 16);
    lds_cptr aw0[4], aw1[4];
#pragma unroll
    for (unsigned j = 0; j < 4; ++j) { unsigned jt = 4 * a + j; jt = jt < 15 ? jt : 14; const unsigned sg = jt / 5, wi = jt - 5 * sg;
        aw0[j] = (lds_cptr)lds + AA + sg * 5120 + wi * 32 + (8 * g + q4 + 4 * (0 ^ (g & 1))) * 160 + p4 * 8; aw1[j] = (lds_cptr)lds + AA + sg * 5120 + wi * 32 + (8 * g + q4 + 4 * (1 ^ (g & 1))) * 160 + p4 * 8; }
    VMW(5); lds_signal(ab, lane); an += 4;
    { int slot = 0;
#pragma unroll 1
      for (int t = 0; t < 20; ++t) {
        lds_wait<true>(ab, an);
        const int so = slot * A_SLOT, fs = slot == 0 ? 2 : slot - 1;
        bf16x8 xf[5], w0 = ldstr2(aw0[0] + so, aw1[0] + so), w1;
#pragma unroll
        for (int i = 0; i < 5; ++i) xf[i] = lds128(ax + so + i * 1024);
        w1 = ldstr2(aw0[1] + so, aw1[1] + so);
        SBAR();
#define A_MF(i, j, w) acc[i][j] = MFMA16(w, xf[i], acc[i][j])
#define A_LD(j) do { if (t + 2 < 20) gldsb((j) < 3 || ((j) == 3 && a < 3) ? awb : axb, aoff[j], lds0 + AA + fs * A_SLOT + (a + 4 * (j)) * 1024); } while (0)
        A_MF(0, 0, w0); A_LD(0); A_MF(1, 0, w0); A_PACE; A_MF(2, 0, w0); A_LD(1); A_MF(3, 0, w0); A_PACE; A_MF(4, 0, w0); SBAR();
        w0 = ldstr2(aw0[2] + so, aw1[2] + so);
        A_MF(0, 1, w1); A_LD(2); A_MF(1, 1, w1); A_PACE; A_MF(2, 1, w1); A_LD(3); A_MF(3, 1, w1); A_PACE; A_MF(4, 1, w1); SBAR();
        w1 = ldstr2(aw0[3] + so, aw1[3] + so);
        A_MF(0, 2, w0); A_LD(4); A_MF(1, 2, w0); A_PACE; A_MF(2, 2, w0); A_PACE; SBAR();
        awb += 32 * 3840; axb += 64;
        if (t < 18) VMW(5); else VMW(0);
        lds_signal(ab, lane); an += 4;
        A_MF(3, 2, w0); A_PACE; A_MF(4, 2, w0); A_PACE;
        A_MF(0, 3, w1); A_PACE; A_MF(1, 3, w1); A_PACE; A_MF(2, 3, w1); A_PACE; A_MF(3, 3, w1); A_PACE; A_MF(4, 3, w1); SBAR();
        slot = slot == 2 ? 0 : slot + 1;
      } }
#undef A_MF
#undef A_LD
    PSTAMP(3);
    lds_wait<true>(ab, an);
    { lds_ptr st = (lds_ptr)lds;
#pragma unroll
      for (unsigned j = 0; j < 4; ++j) { const unsigned jt = 4 * a + j, sg = jt / 5, wi = jt - 5 * sg, cq = 4 * wi + g, hl = (cq * 13) >> 6, dq = cq - 5 * hl;
        if (jt < 15) {
#pragma unroll
            for (int i = 0; i < 5; ++i) { const int row = 16 * i + i16; const f32x4 v = acc[i][j];
                if (sg < 2) *(LAS u32x2*)(st + ST_QK + row * QK_ROW + sg * 192 + hl * 48 + dq * 8) = (u32x2){pk2(v[0], v[1]), pk2(v[2], v[3])};
                else *(LAS f32x4*)(st + ST_V + row * V_ROW + hl * 80 + dq * 16) = v; } } } }
    GBARS(ab, an, 4);
    {
        const int sl = lane, row = 16 + sl, s = s0 + sl;
        const lds_cptr qp = (lds_cptr)lds + ST_QK + row * QK_ROW + a * 48;
        const u32x4 qa = *(const LAS u32x4*)qp, qb = *(const LAS u32x4*)(qp + 16); const u32x2 qc = *(const LAS u32x2*)(qp + 32);
        float lg[8]; float m = -3.0e38f;
#pragma unroll
        for (int w = 0; w < 8; ++w) {
            const lds_cptr kp = qp + 192 - 2 * w * QK_ROW;
            const u32x4 ka = *(const LAS u32x4*)kp, kb = *(const LAS u32x4*)(kp + 16); const u32x2 kc = *(const LAS u32x2*)(kp + 32);
            float x = dot2(qa[0], ka[0], 0.f), x2 = dot2(qa[1], ka[1], 0.f);
            x = dot2(qa[2], ka[2], x); x2 = dot2(qa[3], ka[3], x2); x = dot2(qb[0], kb[0], x); x2 = dot2(qb[1], kb[1], x2);
            x = dot2(qb[2], kb[2], x); x2 = dot2(qb[3], kb[3], x2); x = dot2(qc[0], kc[0], x); x2 = dot2(qc[1], kc[1], x2);
            x = (x + x2) * QSCALE;
            lg[w] = (s - 2 * w >= 0) ? x : -1.0e9f * LOG2E; m = fmaxf(m, lg[w]);
        }
        float l = 0.f;
#pragma unroll
        for (int w = 0; w < 8; ++w) { lg[w] = EXP2(lg[w] - m); l += lg[w]; }
        const float inv = 1.f / l;
        f32x4 o[5];
#pragma unroll
        for (int j = 0; j < 5; ++j) o[j] = (f32x4){0.f, 0.f, 0.f, 0.f};
        const lds_cptr vp = (lds_cptr)lds + ST_V + row * V_ROW + a * 80;
#pragma unroll
        for (int w = 0; w < 8; ++w) {
            const float pw = lg[w] * inv;
#pragma unroll
            for (int j = 0; j < 5; ++j) o[j] += pw * *(const LAS f32x4*)(vp - 2 * w * V_ROW + j * 16);
        }
#pragma unroll
        for (int j = 0; j < 5; ++j) st64(rw, ofrag_off(s, (4 * c4 + a) * DH + 4 * j), (u32x2){pk2(o[j][0], o[j][1]), pk2(o[j][2], o[j][3])});
        VMW(0);
    }
    GBARS(ab, an, 4);
    if (a == 0 && lane == 0) st64(rw, WS_FA + (rt64 * 8 + c4) * 8, tagv);
    PSTAMP(4);
    unsigned coff[5];
#pragma unroll
    for (unsigned i = 0; i < 5; ++i) { const unsigned p = 256 * i + a * 64 + lane, pos = p / 10, j10 = p - pos * 10; coff[i] = (pos ^ (4 * ((pos >> 3) & 1))) * 1280 + j10 * 16; }
    auto c_issue = [&](const u16* W, unsigned chunk, unsigned dst) {
        const char* sb = (const char*)(W + 80 * c4) + (size_t)chunk * (128 * 1280);
#pragma unroll
        for (unsigned i = 0; i < 5; ++i) gldsb(sb, coff[i], lds0 + dst + (4 * i + a) * 1024);
    };
    c_issue(P.woA, 0, AA); c_issue(P.woA, 1, AA + A_SLOT);
    ok &= poll_tags<1>(rw, WS_FA + (rt64 * 8 + (lane & 7)) * 8, lane < 8, tag);
    const int fo = WS_O + ((4 * rt64 + a) * 40 * 64 + lane) * 16;
    bf16x8 of[20];
#pragma unroll
    for (int ks = 0; ks < 20; ++ks) of[ks] = __builtin_bit_cast(bf16x8, ld128(rw, fo + ks * 1024));
    const float gg = __uint_as_float(ld32(rw, WS_G + (s0 + 16 * a + i16) * 4));
    const unsigned cbo = (8 * g + q4) * 160 + p4 * 8, ct0 = 4 * (0 ^ (g & 1)) * 160, ct1 = 4 * (1 ^ (g & 1)) * 160;
    f32x4 ca[5], cb[5];
#pragma unroll
    for (int j = 0; j < 5; ++j) { ca[j] = (f32x4){0.f, 0.f, 0.f, 0.f}; cb[j] = ca[j]; }
#define C_COMPUTE(ACC, base, k0) do { const lds_cptr c0_ = (lds_cptr)lds + (base) + cbo + ct0, c1_ = (lds_cptr)lds + (base) + cbo + ct1; \
        _Pragma("unroll") for (int u = 0; u < 4; ++u) { _Pragma("unroll") for (int j = 0; j < 5; ++j) ACC[j] = MFMA16(ldstr2(c0_ + u * 5120 + j * 32, c1_ + u * 5120 + j * 32), of[(k0) + u], ACC[j]); \
            asm volatile("" : "+v"(ACC[0]), "+v"(ACC[1]), "+v"(ACC[2]), "+v"(ACC[3]), "+v"(ACC[4])); SBAR(); } } while (0)
    VMW(0); GBARS(ab, an, 4); c_issue(P.woA, 2, AA + 2 * A_SLOT); C_COMPUTE(ca, AA, 0);
    GBARS(ab, an, 4); c_issue(P.woA, 3, AA); C_COMPUTE(ca, AA + A_SLOT, 4);
    VMW(5); GBARS(ab, an, 4); c_issue(P.woA, 4, AA + A_SLOT); C_COMPUTE(ca, AA + 2 * A_SLOT, 8);
    VMW(5); GBARS(ab, an, 4); c_issue(P.woB, 0, AA + 2 * A_SLOT); C_COMPUTE(ca, AA, 12);
    VMW(5); GBARS(ab, an, 4); c_issue(P.woB, 1, AA); C_COMPUTE(ca, AA + A_SLOT, 16);
    GBARS(ab, an, 4); c_issue(P.woB, 2, AA + A_SLOT);
    PSTAMP(5);
    lds_wait(ctl + C_BD, 8u);
    c_issue(P.woB, 3, C_S3); c_issue(P.woB, 4, C_S4);
    if (a == 0) { lds_wait(ctl + C_D, 4u); if (lane == 0) st64(rw, WS_FO + (rt64 * 8 + c4) * 8, tagv); }
    PSTAMP(6);
    ok &= poll_tags<1>(rw, WS_FO + (rt64 * 8 + (lane & 7)) * 8, lane < 8, tag);
    VMW(0);
    lds_signal(ab, lane); an += 4;
    PSTAMP(7);
#pragma unroll
    for (int ks = 0; ks < 20; ++ks) of[ks] = __builtin_bit_cast(bf16x8, ld128(rw, fo + (20 + ks) * 1024));
    lds_wait(ab, an);
    C_COMPUTE(cb, AA + 2 * A_SLOT, 0); C_COMPUTE(cb, AA, 4); C_COMPUTE(cb, AA + A_SLOT, 8); C_COMPUTE(cb, C_S3, 12); C_COMPUTE(cb, C_S4, 16);
    if (!ok && lane == 0) ctl[C_BAD] = 1u;
    GBARS(ab, an, 4);
    const float poison = rfl(ctl[C_BAD]) ? __builtin_nanf("") : 0.f;
#pragma unroll
    for (int j = 0; j < 5; ++j) { const f32x4 v = ca[j] * gg + cb[j] * (1.f - gg) + poison;
        *(u32x2*)(P.out + (size_t)(s0 + 16 * a + i16) * 640 + 80 * c4 + 16 * j + 4 * g) = (u32x2){pk2(v[0], v[1]), pk2(v[2], v[3])}; }
    PSTAMP(8);
}

__global__ __launch_bounds__(768, 3) void fused(Params P) {
    extern __shared__ __attribute__((aligned(16))) char lds[];
    PDECL
    PSTAMP(0);
    char* const ws = P.ws;
    const unsigned tid = threadIdx.x, lane = tid & 63, wid = rfl(tid >> 6);
    const unsigned b = blockIdx.x & 255, xg = b & 7, li = b >> 3;
    const unsigned rt64 = (xg >> 2) * 16 + (li >> 1), c4 = (xg & 3) * 2 + (li & 1);
    const rsrc_t rw = __builtin_amdgcn_make_buffer_rsrc(ws, 0, WS_END, 0x00020000);
    LAS unsigned* ctl = (LAS unsigned*)((lds_ptr)lds + CTL);
    if (tid < 32) ctl[tid] = 0u;
    LBAR();
    u64* stp = nullptr;
    if (wid < 8) b_waves(P, lds, ctl, rw, wid & 7, lane, b, li, rt64, c4, stp);
    else { if (APRIO) __builtin_amdgcn_s_setprio(APRIO); a_waves(P, lds, ctl, rw, wid & 3, lane, b, li, rt64, c4, stp); }
    PFLUSH();
}

static inline Params mk_params(void* const* d_in, void* d_out, void* d_ws) {
    Params P; P.x = (const u16*)d_in[0]; P.wqkv = (const u16*)d_in[1]; P.woA = (const u16*)d_in[2]; P.wq = (const u16*)d_in[4]; P.wk = (const u16*)d_in[5]; P.wv = (const u16*)d_in[6];
    P.woB = (const u16*)d_in[7]; P.gw = (const u16*)d_in[8]; P.ws = (char*)d_ws; P.out = (u16*)d_out; return P; }
extern "C" void kernel_launch(void* const* d_in, const int* in_sizes, int n_in, void* d_out, int out_size, void* d_ws, size_t ws_size, hipStream_t stream) {
    (void)hipFuncSetAttribute((const void*)fused, hipFuncAttributeMaxDynamicSharedMemorySize, LDS_TOTAL);
    fused<<<256, 768, LDS_TOTAL, stream>>>(mk_params(d_in, d_out, d_ws));
}
#define ST_LAUNCH(d_in, d_out, ws, stream) fused<<<256, 768, LDS_TOTAL, stream>>>(mk_params(d_in, d_out, ws))
```

```cpp
#include <hip/hip_runtime.h>
#include <stdint.h>
#define OB_DELAY 10
#define POLL_SLEEP 4
#define POLL_DELAY lds_wait4<true>(ctl + C_QL, 8u)
#define A_PACE (void)0
#define APRIO 2

typedef unsigned short u16;
typedef unsigned long long u64;
typedef short bf16x8 __attribute__((ext_vector_type(8)));
typedef short s16x4 __attribute__((ext_vector_type(4)));
typedef float f32x16 __attribute__((ext_vector_type(16)));
typedef float f32x4 __attribute__((ext_vector_type(4)));
typedef float f32x2 __attribute__((ext_vector_type(2)));
typedef unsigned u32x4 __attribute__((ext_vector_type(4)));
typedef unsigned u32x2 __attribute__((ext_vector_type(2)));
typedef __bf16 bf2_t __attribute__((ext_vector_type(2)));
#define LAS __attribute__((address_space(3)))
#define GAS __attribute__((address_space(1)))
typedef LAS const char* lds_cptr;
typedef LAS char* lds_ptr;
typedef __amdgpu_buffer_rsrc_t rsrc_t;

__device__ __forceinline__ float bf2f(u16 b) { return __uint_as_float(((unsigned)b) << 16); }
__device__ __forceinline__ unsigned pk2(float lo, float hi) { f32x2 v = {lo, hi}; bf2_t r = __builtin_convertvector(v, bf2_t); return __builtin_bit_cast(unsigned, r); }
__device__ __forceinline__ u16 f2bf(float f) { return (u16)(pk2(f, 0.f) & 0xffffu); }
__device__ __forceinline__ float lo2f(unsigned u) { return __uint_as_float(u << 16); }
__device__ __forceinline__ float hi2f(unsigned u) { return __uint_as_float(u & 0xffff0000u); }
__device__ __forceinline__ float dot2(unsigned a, unsigned b, float c) { return __builtin_amdgcn_fdot2_f32_bf16(__builtin_bit_cast(bf2_t, a), __builtin_bit_cast(bf2_t, b), c, false); }

constexpr int S = 2048, D = 640, H = 32, DH = 20;
constexpr float SCALE = 0.22360679774997896f, LOG2E = 1.4426950408889634f;
constexpr float QSCALE = SCALE * LOG2E;

constexpr int STG_KM = 0, STG_KT = 8192, STG_VT = 12288, VT_ROW = 528, STG_BYTES = 24576;
constexpr int WS_FKV_AT = 262144;
constexpr int WS_EPOCH = 0;
constexpr int WS_FKV = WS_FKV_AT;
constexpr int WS_FO = 256;
constexpr int WS_FA = WS_FO + 2048;
constexpr int WS_NRM = 8192;
constexpr int WS_G = WS_NRM + 2048;
constexpr int WS_KV = 32768;
static_assert(WS_KV + 8 * STG_BYTES <= WS_FKV_AT, "map");
constexpr int WS_O = WS_FKV_AT + 32768;
constexpr int WS_END = WS_O + S * 1280 * 2;
constexpr int AUX_SC1 = 16;

#define MFMA32(a, b, c) __builtin_amdgcn_mfma_f32_32x32x16_bf16((a), (b), (c), 0, 0, 0)
#define MFMA16(a, b, c) __builtin_amdgcn_mfma_f32_16x16x32_bf16((a), (b), (c), 0, 0, 0)
#define SBAR() __builtin_amdgcn_sched_barrier(0)
#define EXP2(x) __builtin_amdgcn_exp2f(x)
#define WAIT_BAR(N) asm volatile("s_waitcnt vmcnt(" #N ")\n\ts_barrier" ::: "memory")
#define LBAR() asm volatile("s_waitcnt lgkmcnt(0)\n\ts_barrier" ::: "memory")
#define FULLBAR() asm volatile("s_waitcnt vmcnt(0) lgkmcnt(0)\n\ts_barrier" ::: "memory")
__device__ __forceinline__ float wave_max_pos(float f) {
    unsigned x = __float_as_uint(f);
    x = max(x, (unsigned)__builtin_amdgcn_update_dpp(0, (int)x, 0x111, 0xf, 0xf, false));
    x = max(x, (unsigned)__builtin_amdgcn_update_dpp(0, (int)x, 0x112, 0xf, 0xf, false));
    x = max(x, (unsigned)__builtin_amdgcn_update_dpp(0, (int)x, 0x114, 0xf, 0xf, false));
    x = max(x, (unsigned)__builtin_amdgcn_update_dpp(0, (int)x, 0x118, 0xf, 0xf, false));
    x = max(x, (unsigned)__builtin_amdgcn_update_dpp(0, (int)x, 0x142, 0xa, 0xf, false));
    x = max(x, (unsigned)__builtin_amdgcn_update_dpp(0, (int)x, 0x143, 0xc, 0xf, false));
    return __uint_as_float((unsigned)__builtin_amdgcn_readlane((int)x, 63));
}
__device__ __forceinline__ float sum16(float f) { auto r = __builtin_amdgcn_permlane16_swap(__float_as_uint(f), __float_as_uint(f), false, false); return __uint_as_float(r[0]) + __uint_as_float(r[1]); }
__device__ __forceinline__ float sum32(float f) { auto r = __builtin_amdgcn_permlane32_swap(__float_as_uint(f), __float_as_uint(f), false, false); return __uint_as_float(r[0]) + __uint_as_float(r[1]); }
__device__ __forceinline__ float max32(float f) { auto r = __builtin_amdgcn_permlane32_swap(__float_as_uint(f), __float_as_uint(f), false, false); return fmaxf(__uint_as_float(r[0]), __uint_as_float(r[1])); }
__device__ __forceinline__ int swap23(int x) { return (x & ~12) | ((x & 4) << 1) | ((x & 8) >> 1); }
__device__ __forceinline__ void glds16(const void* g, unsigned lds_base) {
    unsigned sv; asm volatile("s_mov_b32 %0, m0\n\ts_mov_b32 m0, %2\n\ts_nop 0\n\tglobal_load_lds_dwordx4 %1, off\n\ts_mov_b32 m0, %0" : "=&s"(sv) : "v"(g), "s"(lds_base) : "memory"); }
__device__ __forceinline__ void gldsb(const void* sbase, unsigned voff, unsigned lds_base) {
    unsigned sv; asm volatile("s_mov_b32 %0, m0\n\ts_mov_b32 m0, %3\n\ts_nop 0\n\tglobal_load_lds_dwordx4 %1, %2\n\ts_mov_b32 m0, %0" : "=&s"(sv) : "v"(voff), "s"(sbase), "s"(lds_base) : "memory"); }
__device__ __forceinline__ void gldsb_sc1(const void* sbase, unsigned voff, unsigned lds_base) {
    unsigned sv; asm volatile("s_mov_b32 %0, m0\n\ts_mov_b32 m0, %3\n\ts_nop 0\n\tglobal_load_lds_dwordx4 %1, %2 sc1\n\ts_mov_b32 m0, %0" : "=&s"(sv) : "v"(voff), "s"(sbase), "s"(lds_base) : "memory"); }
__device__ __forceinline__ bf16x8 lds128(lds_cptr p) { return *(const LAS bf16x8*)p; }
typedef short v4i16_t __attribute__((ext_vector_type(4)));
__device__ __forceinline__ s16x4 ldstr(lds_cptr p) { return __builtin_bit_cast(s16x4, __builtin_amdgcn_ds_read_tr16_b64_v4i16((LAS v4i16_t*)p)); }
__device__ __forceinline__ bf16x8 ldstr2(lds_cptr p0, lds_cptr p1) { const s16x4 a = ldstr(p0), b = ldstr(p1); return __builtin_shufflevector(a, b, 0, 1, 2, 3, 4, 5, 6, 7); }
__device__ __forceinline__ unsigned rfl(unsigned x) { return (unsigned)__builtin_amdgcn_readfirstlane(x); }
__device__ __forceinline__ void st32(rsrc_t r, int off, unsigned v) { __builtin_amdgcn_raw_buffer_store_b32(v, r, off, 0, AUX_SC1); }
__device__ __forceinline__ void st64(rsrc_t r, int off, u32x2 v) { __builtin_amdgcn_raw_buffer_store_b64(v, r, off, 0, AUX_SC1); }
__device__ __forceinline__ void st128(rsrc_t r, int off, u32x4 v) { __builtin_amdgcn_raw_buffer_store_b128(v, r, off, 0, AUX_SC1); }
__device__ __forceinline__ unsigned ld32(rsrc_t r, int off) { return __builtin_amdgcn_raw_buffer_load_b32(r, off, 0, AUX_SC1); }
__device__ __forceinline__ u32x2 ld64(rsrc_t r, int off) { return __builtin_amdgcn_raw_buffer_load_b64(r, off, 0, AUX_SC1); }
__device__ __forceinline__ u32x4 ld128(rsrc_t r, int off) { return __builtin_amdgcn_raw_buffer_load_b128(r, off, 0, AUX_SC1); }

#define PSTAMP(i)
#define PDECL
#define PFLUSH()

#define QT(i)
#define QFLUSH()
struct Params { const u16 *x, *wqkv, *woA, *wq, *wk, *wv, *woB, *gw; char* ws; u16* out; };

constexpr int LDS_TOTAL = 163840, CTL = LDS_TOTAL - 256, DUMMY = CTL - 256;
constexpr int KVB = 2 * STG_BYTES;
constexpr int Q_SLOT = 18432, Q_W = 8192, Q_ST = 73728, QB_ROW = 208;
constexpr int AA = 98304;
constexpr int P0W = AA, P0X = AA + 25664, P0G = P0X + 20480;
constexpr int A_SLOT = 20480, A_X = 15360;
constexpr int ST_QK = AA, ST_V = AA + 32000, QK_ROW = 400, V_ROW = 336;
constexpr int C_S3 = 49152, C_S4 = 69632;
constexpr int C_FALL = 0, C_BB = 1, C_DONE = 2, C_LAND = 4, C_X = 6, C_BD = 7, C_D = 12, C_AB = 13, C_KVOK = 16, C_BAD = 17, C_P0I = 18, C_QL = 20, C_QD = 24, C_TAG = 28, C_QS = 19, C_MKD = 15, C_P = 32;
constexpr unsigned CODE_LINES = 232;
constexpr unsigned EMPTY = 0xFFFFFFFFu;
constexpr unsigned SPIN_CAP = 1u << 18;

template <int N, int SLEEP = 4>
__device__ __forceinline__ bool poll_tags(rsrc_t r, int off, bool active, u64 tag) {
#pragma unroll 1
    for (unsigned spins = 0; spins < SPIN_CAP; ++spins) {
        bool ok = true;
        if (active) {
#pragma unroll
            for (int i = 0; i < N; ++i) { const u32x2 v = ld64(r, off + 8 * i); ok &= (v[0] == (unsigned)tag) & (v[1] == (unsigned)(tag >> 32)); }
        }
        if (__all(ok)) return true;
        __builtin_amdgcn_s_sleep(SLEEP);
    }
    return false;
}
__device__ __forceinline__ void lds_signal(LAS unsigned* cnt, int lane) {
    (void)lane;
    __builtin_amdgcn_sched_barrier(0);
    asm volatile("s_mov_b64 exec, 1\n\tds_add_u32 %0, %1\n\ts_mov_b64 exec, -1\n\ts_nop 3" :: "v"((unsigned)(uintptr_t)cnt), "v"(1u) : "memory");
    __builtin_amdgcn_sched_barrier(0); }
template <bool SLEEP = true>
__device__ __forceinline__ void lds_wait(LAS unsigned* cnt, unsigned n) {
    unsigned v, sv, left;
    if (SLEEP)
        asm volatile("s_mov_b32 %2, 0x100000\n1:\n\tds_read_b32 %0, %3\n\ts_waitcnt lgkmcnt(0)\n\tv_readfirstlane_b32 %1, %0\n\ts_cmp_ge_u32 %1, %4\n\ts_cbranch_scc1 2f\n\ts_sleep 1\n\ts_sub_u32 %2, %2, 1\n\ts_cbranch_scc0 1b\n2:"
                     : "=&v"(v), "=&s"(sv), "=&s"(left) : "v"((unsigned)(uintptr_t)cnt), "s"(n) : "memory", "scc");
    else
        asm volatile("s_mov_b32 %2, 0x100000\n1:\n\tds_read_b32 %0, %3\n\ts_waitcnt lgkmcnt(0)\n\tv_readfirstlane_b32 %1, %0\n\ts_cmp_ge_u32 %1, %4\n\ts_cbranch_scc1 2f\n\ts_sub_u32 %2, %2, 1\n\ts_cbranch_scc0 1b\n2:"
                     : "=&v"(v), "=&s"(sv), "=&s"(left) : "v"((unsigned)(uintptr_t)cnt), "s"(n) : "memory", "scc");
}
template <bool SLEEP = false>
__device__ __forceinline__ void lds_wait4(LAS unsigned* cnt, unsigned n) {
#pragma unroll 1
    for (unsigned spins = 0; spins < (1u << 20); ++spins) {
        const u32x4 v = *(volatile LAS u32x4*)cnt;
        const unsigned lo = min(min(v[0], v[1]), min(v[2], v[3]));
        if (rfl(lo) >= n) break;
        if (SLEEP) __builtin_amdgcn_s_sleep(1);
    }
    asm volatile("" ::: "memory");
}
#define VMW(N) asm volatile("s_waitcnt vmcnt(" #N ")" ::: "memory")
#define GBAR(cnt, n, step) do { lds_signal(cnt, lane); n += (step); lds_wait<false>(cnt, n); } while (0)
#define GBARS(cnt, n, step) do { lds_signal(cnt, lane); n += (step); lds_wait<true>(cnt, n); } while (0)

#define KV_GLDS gldsb_sc1
__device__ __forceinline__ void att_stage_load(const char* kv, int st, unsigned ldsbuf, int w4, int lane) {
    const char* src = kv + (size_t)st * STG_BYTES + w4 * 1024;
#pragma unroll
    for (int i = 0; i < 6; ++i) KV_GLDS(src + i * 4096, lane * 16, ldsbuf + (w4 + 4 * i) * 1024);
}

__device__ __forceinline__ void att_maxpass(const char* kv, char* ldsh, LAS unsigned* bb, unsigned& bn, int kh, int w4, int lane, const bf16x8 (&qm)[2], const bf16x8 (&qt)[2][2], float (&mx)[2]) {
    const unsigned lds0 = (unsigned)(uintptr_t)ldsh;
    const lds_cptr kbase = (lds_cptr)ldsh + lane * 16;
    const f32x16 zero = {};
#pragma unroll 1
    for (int rnd = 0; rnd < 3; ++rnd) {
        if (rnd) { GBAR(bb, bn, 8); att_stage_load(kv, 4 * kh + 2 * (rnd & 1), lds0, w4, lane); att_stage_load(kv, 4 * kh + 2 * (rnd & 1) + 1, lds0 + KVB, w4, lane); }
        VMW(0); GBAR(bb, bn, 8);
        if (rnd < 2) {
#pragma unroll 1
            for (int pr = 0; pr < 8; ++pr) {
                const lds_cptr kb = kbase + (pr >> 2) * KVB; const int p = pr & 3;
                const bf16x8 kt = lds128(kb + STG_KT + p * 1024), k0 = lds128(kb + STG_KM + (2 * p) * 1024), k1 = lds128(kb + STG_KM + (2 * p + 1) * 1024);
#pragma unroll
                for (int h = 0; h < 2; ++h) {
                    f32x16 s0 = MFMA32(k0, qm[h], zero); s0 = MFMA32(kt, qt[h][0], s0);
                    f32x16 s1 = MFMA32(k1, qm[h], zero); s1 = MFMA32(kt, qt[h][1], s1);
#pragma unroll
                    for (int i = 0; i < 16; ++i) mx[h] = fmaxf(mx[h], fmaxf(s0[i], s1[i]));
                }
            }
        }
    }
}

__device__ __forceinline__ void att_main(const char* kv, char* ldsh, LAS unsigned* ctl, rsrc_t rw, int mark_off, int kbase0, int fm_off, int kh, int w4, int lane, const bf16x8 (&qm)[2], const bf16x8 (&qt)[2][2], f32x16& O0, f32x16& O1) {
    const unsigned lds0 = (unsigned)(uintptr_t)ldsh;
    LAS unsigned* cdone = ctl + C_DONE + kh; LAS unsigned* cland = ctl + C_LAND + kh;
    const int r = lane & 31, hh = lane >> 5;
    const int voff = r <= 20 ? r * VT_ROW : (r < 28 ? 21 * VT_ROW : 21 * VT_ROW + 112);
    const lds_cptr kbase = (lds_cptr)ldsh + lane * 16;
    const lds_cptr vbase = (lds_cptr)ldsh + STG_VT + voff + hh * 16;
    const f32x16 zero = {};
    f32x16 sA, sB; u32x4 pA0, pA1, pB0, pB1; bf16x8 kmA, kmB, ktA, ktB, vA0, vA1, vB0, vB1;
    ktA = lds128(kbase + STG_KT); kmA = lds128(kbase + STG_KM);
    sA = MFMA32(kmA, qm[0], zero); sA = MFMA32(ktA, qt[0][0], sA);
    pB0 = (u32x4){0u, 0u, 0u, 0u}; pB1 = pB0; vB0 = kmA; vB1 = kmA; ktB = ktA; vA0 = kmA; vA1 = kmA; kmB = kmA;
    float ee0 = 0.f, ee1 = 0.f, ee2 = 0.f, ee3 = 0.f;
#define GRP(MF, LD, SC, I, PD, J) do { MF; LD; const float e0_ = EXP2(SC[I]); unsigned a_ = pk2(ee0, ee1); const float e1_ = EXP2(SC[I + 1]); unsigned b_ = pk2(ee2, ee3); const float e2_ = EXP2(SC[I + 2]), e3_ = EXP2(SC[I + 3]); \
        ee0 = e0_; ee1 = e1_; ee2 = e2_; ee3 = e3_; asm volatile("" : "+v"(a_), "+v"(b_), "+v"(ee0), "+v"(ee1), "+v"(ee2), "+v"(ee3)); PD[J] = a_; PD[J + 1] = b_; SBAR(); } while (0)
#define TILE(KM, KMN, KT, VJ, KTN, VJN, VC0, VC1, VP0, VP1, VADDR, LDK, LDT) do { \
        GRP(sB = MFMA32(KM, qm[1], zero), VC0 = lds128(VADDR), sA, 0, pB1, 2); \
        GRP(O1 = MFMA32(VP0, __builtin_bit_cast(bf16x8, pB0), O1), VC1 = lds128((VADDR) + 32), sA, 4, pA0, 0); \
        GRP(sB = MFMA32(KT, qt[1][VJ], sB), LDK, sA, 8, pA0, 2); \
        GRP(O1 = MFMA32(VP1, __builtin_bit_cast(bf16x8, pB1), O1), LDT, sA, 12, pA1, 0); \
        GRP(sA = MFMA32(KMN, qm[0], zero), (void)0, sB, 0, pA1, 2); \
        GRP(O0 = MFMA32(VC0, __builtin_bit_cast(bf16x8, pA0), O0), (void)0, sB, 4, pB0, 0); \
        GRP(sA = MFMA32(KTN, qt[0][VJN], sA), (void)0, sB, 8, pB0, 2); \
        GRP(O0 = MFMA32(VC1, __builtin_bit_cast(bf16x8, pA1), O0), (void)0, sB, 12, pB1, 0); } while (0)
#pragma unroll 1
    for (int st = 0; st < 4; ++st) {
        if (kh == 1) { if (st == 0) __builtin_amdgcn_s_setprio(1); if (st == 3) __builtin_amdgcn_s_setprio(0); }
        const int buf = st & 1;
        const lds_cptr kb = kbase + buf * KVB, vb = vbase + buf * KVB, kn = kbase + (buf ^ 1) * KVB;
        TILE(kmA, kmB, ktA, 0, ktA, 1, vA0, vA1, vB0, vB1, vb + 0 * 64, kmB = lds128(kb + STG_KM + 1 * 1024), (void)0);
        if (st) { lds_signal(st == 3 ? ctl + C_X : cdone, lane); SBAR(); }
        TILE(kmB, kmA, ktA, 1, ktB, 0, vB0, vB1, vA0, vA1, vb + 1 * 64, kmA = lds128(kb + STG_KM + 2 * 1024), ktB = lds128(kb + STG_KT + 1024));
        if (st == 0 && kh == 0) {
#pragma unroll 1
            for (int pc = w4; pc < 20; pc += 4) { const int k = kbase0 + 8 * pc + (pc < 10 ? 0 : 560); st128(rw, mark_off + ((k >> 5) * 64 + ((k >> 3) & 3) * 16) * 16, (u32x4){EMPTY, EMPTY, EMPTY, EMPTY}); }
            SBAR(); }
        if (st == 1 || st == 2) { lds_wait(cdone, 4u * st); att_stage_load(kv, 4 * kh + st + 1, lds0 + (buf ^ 1) * KVB, w4, lane); SBAR(); }
        TILE(kmA, kmB, ktB, 0, ktB, 1, vA0, vA1, vB0, vB1, vb + 2 * 64, kmB = lds128(kb + STG_KM + 3 * 1024), (void)0);
        TILE(kmB, kmA, ktB, 1, ktA, 0, vB0, vB1, vA0, vA1, vb + 3 * 64, kmA = lds128(kb + STG_KM + 4 * 1024), ktA = lds128(kb + STG_KT + 2048));
        TILE(kmA, kmB, ktA, 0, ktA, 1, vA0, vA1, vB0, vB1, vb + 4 * 64, kmB = lds128(kb + STG_KM + 5 * 1024), (void)0);
        TILE(kmB, kmA, ktA, 1, ktB, 0, vB0, vB1, vA0, vA1, vb + 5 * 64, kmA = lds128(kb + STG_KM + 6 * 1024), ktB = lds128(kb + STG_KT + 3072));
        if (st < 3) { VMW(0); lds_signal(cland, lane); SBAR(); }
        TILE(kmA, kmB, ktB, 0, ktB, 1, vA0, vA1, vB0, vB1, vb + 6 * 64, kmB = lds128(kb + STG_KM + 7 * 1024), (void)0);
        if (st < 3) { lds_wait(cland, 4u * (st + 1));
            if (st == 0 && kh == 0 && w4 == 0) { if (lane == 0) st64(rw, fm_off, (u32x2){ctl[C_TAG + 1], ctl[C_TAG + 2]}); lds_signal(ctl + C_MKD, lane); }
            SBAR(); }
        TILE(kmB, kmA, ktB, 1, ktA, 0, vB0, vB1, vA0, vA1, vb + 7 * 64, kmA = lds128(kn + STG_KM + 0 * 1024), ktA = lds128(kn + STG_KT));
    }
    pB1[2] = pk2(ee0, ee1); pB1[3] = pk2(ee2, ee3);
    O1 = MFMA32(vB0, __builtin_bit_cast(bf16x8, pB0), O1);
    O1 = MFMA32(vB1, __builtin_bit_cast(bf16x8, pB1), O1);
#undef GRP
#undef TILE
}

__device__ __forceinline__ int ofrag_off(int s, int k) { return WS_O + ((((s >> 4) * 40 + (k >> 5)) * 64 + ((k >> 3) & 3) * 16 + (s & 15)) * 16 + (k & 7) * 2); }
__device__ __forceinline__ void touch(const void* g, unsigned lds_dummy) {
    unsigned sv; asm volatile("s_mov_b32 %0, m0\n\ts_mov_b32 m0, %2\n\ts_nop 0\n\tglobal_load_lds_dword %1, off\n\ts_mov_b32 m0, %0" : "=&s"(sv) : "v"(g), "s"(lds_dummy) : "memory"); }

__device__ __forceinline__ void b_waves(const Params& P, char* lds, LAS unsigned* ctl, rsrc_t rw, unsigned wid, unsigned lane, unsigned b, unsigned li, unsigned rt64, unsigned c4, u64* stamps) {
    char* const ws = P.ws;
    const unsigned lds0 = (unsigned)(uintptr_t)lds;
    const int s0 = 64 * rt64;
    const unsigned i16 = lane & 15, g = lane >> 4, q4 = i16 >> 2, p4 = i16 & 3;
    unsigned bn = 0; LAS unsigned* bb = ctl + C_BB;
    f32x4 qa[5];
#pragma unroll
    for (int j = 0; j < 5; ++j) qa[j] = (f32x4){0.f, 0.f, 0.f, 0.f};
    if (wid >= 4) {
        const unsigned l = wid - 4;
        unsigned off[5];
#pragma unroll
        for (unsigned j = 0; j < 5; ++j) { const unsigned id = l + 4 * j;
            if (j < 2) { const unsigned p = id * 64 + lane, row = p >> 3, ch = (p & 7) ^ ((row >> 1) & 7); off[j] = row * 1280 + ch * 16; }
            else { const unsigned p = ((id < 18 ? id : 8) - 8) * 64 + lane, pos = p / 10, j10 = p - pos * 10, k = pos ^ (4 * ((pos >> 3) & 1)); off[j] = k * 1280 + j10 * 16; } }
        const char* xs = (const char*)(P.x + (size_t)s0 * D); const char* wsq = (const char*)(P.wq + 80 * c4);
        int slot = 0;
        lds_wait<false>(ctl + C_P0I, 4u);
#pragma unroll 1
        for (int t = 0; t < 10; ++t) {
            if (t >= 5) lds_wait4(ctl + C_QD, t - 4);
            const unsigned so = lds0 + slot * Q_SLOT + l * 1024;
            gldsb(xs, off[0], so); gldsb(xs, off[1], so + 4096); gldsb(wsq, off[2], so + 8192); gldsb(wsq, off[3], so + 12288);
            if (l < 2) gldsb(wsq, off[4], so + 16384);
            xs += 128; wsq += 64 * 1280;
            if (t >= 3) { VMW(12); lds_signal(ctl + C_QL + l, lane); }
            slot = slot == 4 ? 0 : slot + 1;
        }
        VMW(8); lds_signal(ctl + C_QL + l, lane); VMW(4); lds_signal(ctl + C_QL + l, lane); VMW(0); lds_signal(ctl + C_QL + l, lane);
        PSTAMP(1);
    } else {
        const lds_cptr qx0 = (lds_cptr)lds + (16 * wid + i16) * 128 + ((g ^ (i16 >> 1)) * 16), qx1 = (lds_cptr)lds + (16 * wid + i16) * 128 + (((4 + g) ^ (i16 >> 1)) * 16);
        const lds_cptr qw0 = (lds_cptr)lds + Q_W + (8 * g + q4 + 4 * (0 ^ (g & 1))) * 160 + p4 * 8, qw1 = (lds_cptr)lds + Q_W + (8 * g + q4 + 4 * (1 ^ (g & 1))) * 160 + p4 * 8;
        {
            const unsigned ri = li >> 1, cc = li & 1, l2 = lane - 32;
#pragma unroll 1
            for (unsigned j = 5 + wid; j < 10; j += 4) {
                const char* p = lane < 32 ? (const char*)(P.x + (size_t)(s0 + 2 * lane + cc) * D) + j * 128 : (const char*)(P.wq + 80 * c4) + (size_t)((64 * j + ri + 16 * ((l2 >> 1) & 3)) * 1280) + (l2 & 1) * 156;
                touch(p, lds0 + DUMMY); } }
        PSTAMP(1);
        bf16x8 xa0, xa1, wa[10], xb0, xb1, wb_[10];
#define Q_READ(X0, X1, W, t_, so_) do { lds_wait4(ctl + C_QL, (t_) + 1); X0 = lds128(qx0 + (so_)); X1 = lds128(qx1 + (so_)); \
            _Pragma("unroll") for (int j = 0; j < 10; ++j) W[j] = ldstr2(qw0 + (so_) + (j / 5) * 5120 + (j % 5) * 32, qw1 + (so_) + (j / 5) * 5120 + (j % 5) * 32); SBAR(); } while (0)
#define Q_MUL(X0, X1, W) do { _Pragma("unroll") for (int j = 0; j < 5; ++j) qa[j] = MFMA16(W[j], X0, qa[j]); _Pragma("unroll") for (int j = 0; j < 5; ++j) qa[j] = MFMA16(W[5 + j], X1, qa[j]); \
            asm volatile("" : "+v"(qa[0]), "+v"(qa[1]), "+v"(qa[2]), "+v"(qa[3]), "+v"(qa[4])); SBAR(); } while (0)
#define Q_DONE() do { asm volatile("s_waitcnt lgkmcnt(0)" ::: "memory"); lds_signal(ctl + C_QD + wid, lane); } while (0)
        Q_READ(xa0, xa1, wa, 0, 0); Q_DONE();
        int slot = 1;
#pragma unroll 1
        for (int t = 0; t < 10; t += 2) {
            Q_READ(xb0, xb1, wb_, t + 1, slot * Q_SLOT); slot = slot == 4 ? 0 : slot + 1;
            Q_MUL(xa0, xa1, wa); Q_DONE();
            if (t + 2 < 10) { Q_READ(xa0, xa1, wa, t + 2, slot * Q_SLOT); slot = slot == 4 ? 0 : slot + 1; }
            Q_MUL(xb0, xb1, wb_);
            if (t + 2 < 10) Q_DONE();
        }
#undef Q_READ
#undef Q_MUL
#undef Q_DONE
    }
    PSTAMP(2);
    if (wid == 4) {
        lds_wait<true>(ctl + C_TAG, 1u);
        const u64 tag = (u64)ctl[C_TAG + 1] | ((u64)ctl[C_TAG + 2] << 32);
        bool ok = false;
#pragma unroll 1
        for (unsigned spins = 0; spins < SPIN_CAP; ++spins) {
            const u32x4 v = ld128(rw, WS_FKV + li * 1024 + lane * 16);
            if (__all((v[0] == (unsigned)tag) & (v[1] == (unsigned)tag) & (v[2] == (unsigned)tag) & (v[3] == (unsigned)tag))) { ok = true; break; }
            __builtin_amdgcn_s_sleep(POLL_SLEEP);
        }
        if (!ok && lane == 0) ctl[C_BAD] = 1u;
        lds_signal(ctl + C_KVOK, lane);
        if (b == 0 && lane == 0) st64(rw, WS_EPOCH, (u32x2){(unsigned)tag, (unsigned)(tag >> 32)});
    }
    const char* kv = ws + WS_KV;
    const unsigned w4 = wid & 3, kh = wid >> 2;
    char* ldsh = lds + kh * STG_BYTES;
    const unsigned ldsh0 = lds0 + kh * STG_BYTES;
    u32x4 nr0, nr1;
    if (wid < 4) {
        lds_wait4(ctl + C_QD, 10u);
#pragma unroll
        for (int j = 0; j < 5; ++j) { const unsigned cq = 4 * j + g, hl = (cq * 13) >> 6, dq = cq - 5 * hl;
            *(LAS u32x2*)((lds_ptr)lds + Q_ST + (16 * wid + i16) * QB_ROW + hl * 48 + dq * 8) = (u32x2){pk2(qa[j][0] * QSCALE, qa[j][1] * QSCALE), pk2(qa[j][2] * QSCALE, qa[j][3] * QSCALE)}; }
        lds_signal(ctl + C_QS, lane);
    } else {
        lds_wait<false>(ctl + C_KVOK, 1u);
        lds_wait4(ctl + C_QD, 8u);
        att_stage_load(kv, 0, lds0, w4, lane); att_stage_load(kv, 4, lds0 + STG_BYTES, w4, lane);
    }
    lds_wait<false>(ctl + C_QS, 4u);
    const int r = lane & 31, hh = lane >> 5;
    bf16x8 qm[2]; u32x2 qtl[2];
#pragma unroll
    for (int h = 0; h < 2; ++h) {
        const lds_cptr qp = (lds_cptr)lds + Q_ST + (32 * (w4 & 1) + r) * QB_ROW + (2 * (w4 >> 1) + h) * 48;
        qm[h] = lds128(qp + hh * 16); qtl[h] = *(const LAS u32x2*)(qp + 32);
    }
    float q2[2];
#pragma unroll
    for (int h = 0; h < 2; ++h) {
        q2[h] = 0.f;
#pragma unroll
        for (int i = 0; i < 8; ++i) { const float v = bf2f((u16)qm[h][i]); q2[h] += v * v; }
        if (hh == 0) { const float a0 = lo2f(qtl[h][0]), a1 = hi2f(qtl[h][0]), a2 = lo2f(qtl[h][1]), a3 = hi2f(qtl[h][1]); q2[h] += a0 * a0 + a1 * a1 + a2 * a2 + a3 * a3; }
        q2[h] = sum32(q2[h]);
    }
    lds_wait<false>(ctl + C_KVOK, 1u);
    PSTAMP(3);
    asm volatile("global_load_dwordx4 %0, %2, %3 sc1\n\tglobal_load_dwordx4 %1, %2, %3 offset:1024 sc1\n\ts_waitcnt vmcnt(0)" : "=&v"(nr0), "=&v"(nr1) : "v"(lane * 16), "s"(ws + WS_NRM) : "memory");
    const float kmax2 = wave_max_pos(fmaxf(__uint_as_float(nr0[0]), __uint_as_float(nr1[0]))) + wave_max_pos(fmaxf(__uint_as_float(nr0[1]), __uint_as_float(nr1[1])));
    const float vmax = wave_max_pos(fmaxf(fmaxf(__uint_as_float(nr0[2]), __uint_as_float(nr1[2])), fmaxf(__uint_as_float(nr0[3]), __uint_as_float(nr1[3]))));
    const float lim = 112.f - fmaxf(0.f, __builtin_amdgcn_logf(fmaxf(vmax, 1.f)));
    const float bnd0 = __builtin_sqrtf(q2[0] * kmax2) * 1.001f + 1.f, bnd1 = __builtin_sqrtf(q2[1] * kmax2) * 1.001f + 1.f;
    GBAR(bb, bn, 8);
    const u32x4 z4 = {0u, 0u, 0u, 0u};
    bf16x8 qt[2][2];
#pragma unroll
    for (int h = 0; h < 2; ++h) { const u32x4 a = {qtl[h][0], qtl[h][1], 0u, 0u}; qt[h][0] = __builtin_bit_cast(bf16x8, hh ? z4 : a); qt[h][1] = __builtin_bit_cast(bf16x8, hh ? a : z4); }
    const f32x16 zero = {};
    float m[2]; bool unsafe = false;
    { const lds_cptr kb = (lds_cptr)ldsh + lane * 16; const bf16x8 k0 = lds128(kb + STG_KM), kt = lds128(kb + STG_KT);
#pragma unroll
      for (int h = 0; h < 2; ++h) {
        f32x16 sc = MFMA32(k0, qm[h], zero); sc = MFMA32(kt, qt[h][0], sc);
        float mm = sc[0];
#pragma unroll
        for (int i = 1; i < 16; ++i) mm = fmaxf(mm, sc[i]);
        mm = max32(mm);
        m[h] = bf2f(f2bf(mm));
        unsafe |= !((h ? bnd1 : bnd0) - m[h] <= lim);
      } }
    if (__any(unsafe) && lane == 0) ctl[C_FALL] = 1u;
    att_stage_load(kv, 4 * kh + 1, ldsh0 + KVB, w4, lane);
    GBAR(bb, bn, 8);
    if (rfl(ctl[C_FALL]) != 0u) {
        att_maxpass(kv, ldsh, bb, bn, kh, w4, lane, qm, qt, m);
#pragma unroll
        for (int h = 0; h < 2; ++h) m[h] = bf2f(f2bf(max32(m[h])));
    }
#pragma unroll
    for (int h = 0; h < 2; ++h) { const unsigned nm = f2bf(-m[h]); const u32x4 a = {qtl[h][0], qtl[h][1], nm, 0u}; qt[h][0] = __builtin_bit_cast(bf16x8, hh ? z4 : a); qt[h][1] = __builtin_bit_cast(bf16x8, hh ? a : z4); }
    f32x16 O[2] = {{}, {}};
    PSTAMP(4);
    att_main(kv, ldsh, ctl, rw, ofrag_off(s0 + lane, 0), 80 * c4, WS_FA + (rt64 * 8 + c4) * 8, kh, w4, lane, qm, qt, O[0], O[1]);
    PSTAMP(5);
    lds_signal(ctl + C_BD, lane);
    {
        LAS float* xo = (LAS float*)((lds_ptr)lds + (2 * w4 + kh) * 3328);
        LAS float* xi = (LAS float*)((lds_ptr)lds + (2 * w4 + (kh ^ 1)) * 3328);
        lds_wait(ctl + C_X, 8u);
#pragma unroll
        for (int i = 0; i < 12; ++i) xo[i * 64 + lane] = kh ? O[0][i] : O[1][i];
        xo[12 * 64 + lane] = kh ? m[0] : m[1];
        lds_signal(ctl + C_P + 2 * w4 + kh, lane);
        lds_wait<false>(ctl + C_P + 2 * w4 + (kh ^ 1), 1u);
        const float mo = kh ? m[1] : m[0], m1 = xi[12 * 64 + lane];
        const float M = fmaxf(mo, m1), f0 = EXP2(mo - M), f1 = EXP2(m1 - M);
        float o[12];
#pragma unroll
        for (int i = 0; i < 12; ++i) o[i] = (kh ? O[1][i] : O[0][i]) * f0 + xi[i * 64 + lane] * f1;
        auto lr = __builtin_amdgcn_permlane32_swap(__float_as_uint(o[8]), __float_as_uint(o[8]), false, false);
        const float inv = __builtin_amdgcn_rcpf(__uint_as_float(lr[1]));
        const int s = s0 + 32 * (w4 & 1) + r, k0 = 640 + (4 * c4 + 2 * (w4 >> 1) + kh) * DH;
#define NE(x) ((x) == EMPTY ? 0x7fc07fc0u : (x))
        const unsigned w0 = pk2(o[0] * inv, o[1] * inv), w1 = pk2(o[2] * inv, o[3] * inv), w2 = pk2(o[4] * inv, o[5] * inv), w3 = pk2(o[6] * inv, o[7] * inv), w4_ = pk2(o[8] * inv, o[9] * inv), w5 = pk2(o[10] * inv, o[11] * inv);
        st64(rw, ofrag_off(s, k0 + 4 * hh), (u32x2){NE(w0), NE(w1)});
        st64(rw, ofrag_off(s, k0 + 8 + 4 * hh), (u32x2){NE(w2), NE(w3)});
        if (hh == 0) st64(rw, ofrag_off(s, k0 + 16), (u32x2){NE(w4_), NE(w5)});
#undef NE
        lds_signal(ctl + C_D, lane);
    }
    asm volatile("s_waitcnt lgkmcnt(0)" ::: "memory");
    PSTAMP(6);
}

__device__ __forceinline__ void a_waves(const Params& P, char* lds, LAS unsigned* ctl, rsrc_t rw, unsigned a, unsigned lane, unsigned b, unsigned li, unsigned rt64, unsigned c4, u64* stamps) {
    const unsigned lds0 = (unsigned)(uintptr_t)lds;
    const int s0 = 64 * rt64;
    const unsigned i16 = lane & 15, g = lane >> 4, q4 = i16 >> 2, p4 = i16 & 3;
    unsigned an = 0; LAS unsigned* ab = ctl + C_AB;
    const unsigned part = c4 & 1, r0 = s0 + 16 * (c4 >> 1);
    {
        if (a == 3) { u64 pc; asm volatile("s_getpc_b64 %0" : "=s"(pc)); const unsigned ln = li * 8 + (lane & 7); touch((const char*)(pc & ~127ull) + (ln < CODE_LINES ? ln : 0) * 128, lds0 + DUMMY); }
    }
    { const char* wsrc = (const char*)(part ? P.wv : P.wk); const char* xsrc = (const char*)(P.x + (size_t)r0 * D);
#pragma unroll 1
      for (unsigned i = 0; i < 12; ++i) { unsigned id = a + 4 * i; if (id == 47) id = 43;
        if (id < 25) gldsb(wsrc, id * 1024 + lane * 16, lds0 + P0W + id * 1024);
        else if (id < 45) { const unsigned p = (id - 25) * 64 + lane, row = p / 80, cpos = p - row * 80, ch = (cpos & ~15u) | ((cpos & 15) ^ row);
            gldsb(xsrc, row * 1280 + ch * 16, lds0 + P0X + (id - 25) * 1024); }
        else { const unsigned off = (id - 45) * 1024 + lane * 16; gldsb(P.gw, off < 1280 ? off : 1264, lds0 + P0G + (id - 45) * 1024); } } }
    lds_signal(ctl + C_P0I, lane);
    const u32x2 ep = ld64(rw, WS_EPOCH);
    const u64 tag = ((u64)ep[0] | ((u64)ep[1] << 32)) + 0x9E3779B97F4A7C15ull;
    const u32x2 tagv = {(unsigned)tag, (unsigned)(tag >> 32)};
    {
        const unsigned idx = a * 60 + (lane < 60 ? lane : 0), k16 = idx / 6, sub = idx - 6 * k16;
        touch((const char*)(P.wqkv + (sub >> 1) * 640 + 80 * c4) + (size_t)(((li >> 1) + 16 * k16) * 3840) + (sub & 1) * 156, lds0 + DUMMY); }
    PSTAMP(1);
    VMW(1);
    GBARS(ab, an, 4);
    if (a < 2 || (a == 2 && part == 0)) {
        const lds_cptr p0x = (lds_cptr)lds + P0X + i16 * 1280;
        f32x4 pacc = {0.f, 0.f, 0.f, 0.f};
#define P0XF(ks) lds128(p0x + ((((4 * (ks) + g) & ~15u) | (((4 * (ks) + g) & 15) ^ i16)) * 16))
        const int ks0 = r0 & 255, img = WS_KV + (r0 >> 8) * STG_BYTES, nrm = WS_NRM + (r0 >> 4) * 16;
        if (a == 2) {
            const bf16x8 z = {0, 0, 0, 0, 0, 0, 0, 0};
#pragma unroll
            for (int ks = 0; ks < 20; ++ks) { const bf16x8 gv = lds128((lds_cptr)lds + P0G + (32 * ks + 8 * g) * 2); pacc = MFMA16(i16 == 0 ? gv : z, P0XF(ks), pacc); }
            if (g == 0) st32(rw, WS_G + (r0 + i16) * 4, __float_as_uint(1.f / (1.f + __expf(-pacc[0]))));
        } else {
            const lds_cptr p0w = (lds_cptr)lds + P0W + (8 * g + q4) * 40 + (16 * a + 4 * p4) * 2;
            if (part == 0) {
#pragma unroll
                for (int ks = 0; ks < 20; ++ks) pacc = MFMA16(ldstr2(p0w + ks * 1280, p0w + ks * 1280 + 160), P0XF(ks), pacc);
                const unsigned w0 = pk2(pacc[0], pacc[1]), w1 = pk2(pacc[2], pacc[3]);
                const int key = ks0 + i16, tile = key >> 5, r = swap23(key & 31);
                float n2 = lo2f(w0) * lo2f(w0) + hi2f(w0) * hi2f(w0) + lo2f(w1) * lo2f(w1) + hi2f(w1) * hi2f(w1);
                if (a == 0) { st64(rw, img + STG_KM + tile * 1024 + (g >> 1) * 512 + r * 16 + (g & 1) * 8, (u32x2){w0, w1});
                    n2 = sum16(n2); n2 = sum32(n2); }
                else { if (g == 0) st128(rw, img + STG_KT + (tile >> 1) * 1024 + (tile & 1) * 512 + r * 16, (u32x4){w0, w1, 0x3f80u, 0u}); if (g != 0) n2 = 0.f; }
                n2 = wave_max_pos(n2);
                if (lane == 0) st32(rw, nrm + a * 4, __float_as_uint(n2));
            } else {
#pragma unroll
                for (int ks = 0; ks < 20; ++ks) pacc = MFMA16(P0XF(ks), ldstr2(p0w + ks * 1280, p0w + ks * 1280 + 160), pacc);
                const unsigned w0 = pk2(pacc[0], pacc[1]), w1 = pk2(pacc[2], pacc[3]);
                const int d = 16 * a + i16;
                float vm = fmaxf(fmaxf(fabsf(lo2f(w0)), fabsf(hi2f(w0))), fmaxf(fabsf(lo2f(w1)), fabsf(hi2f(w1))));
                if (d >= 20) vm = 0.f;
                if (d <= 20) st64(rw, img + STG_VT + d * VT_ROW + (ks0 + 4 * g) * 2, d == 20 ? (u32x2){0x3f803f80u, 0x3f803f80u} : (u32x2){w0, w1});
                if (a == 1 && ks0 == 0 && lane < 40) st128(rw, img + STG_VT + 21 * VT_ROW + lane * 16, (u32x4){0u, 0u, 0u, 0u});
                vm = wave_max_pos(vm);
                if (lane == 0) st32(rw, nrm + (2 + a) * 4, __float_as_uint(vm));
            }
        }
#undef P0XF
        VMW(0);
    }
    GBARS(ab, an, 4);
    bool ok = true;
    if (a == 3) { if (lane < 32) st32(rw, WS_FKV + lane * 1024 + b * 4, tagv[0]);
        if (lane == 0) { ctl[C_TAG + 1] = tagv[0]; ctl[C_TAG + 2] = tagv[1]; }
        lds_signal(ctl + C_TAG, lane); }
    PSTAMP(2);
    unsigned aoff[5];
#pragma unroll
    for (unsigned j = 0; j < 5; ++j) { const unsigned id = a + 4 * j;
        if (id < 15) { const unsigned seg = id / 5, p = (id - 5 * seg) * 64 + lane, pos = p / 10, j10 = p - pos * 10, k = pos ^ (4 * ((pos >> 3) & 1)); aoff[j] = k * 3840 + seg * 1280 + j10 * 16; }
        else { const unsigned p = (id - 15) * 64 + lane, row = p >> 2, ch = (p & 3) ^ (3 * ((row >> 2) & 1)); int gr = s0 - 16 + (int)row; gr = gr < 0 ? 0 : gr; aoff[j] = gr * 1280 + ch * 16; } }
    const char* awb = (const char*)(P.wqkv + 80 * c4); const char* axb = (const char*)P.x;
#define A_ISSUE(slot) do { gldsb(awb, aoff[0], lds0 + AA + (slot) * A_SLOT + a * 1024); gldsb(awb, aoff[1], lds0 + AA + (slot) * A_SLOT + (a + 4) * 1024); gldsb(awb, aoff[2], lds0 + AA + (slot) * A_SLOT + (a + 8) * 1024); \
        gldsb(a < 3 ? awb : axb, aoff[3], lds0 + AA + (slot) * A_SLOT + (a + 12) * 1024); gldsb(axb, aoff[4], lds0 + AA + (slot) * A_SLOT + (a + 16) * 1024); awb += 32 * 3840; axb += 64; } while (0)
    lds_wait4<true>(ctl + C_QL, 10u);
    A_ISSUE(0); A_ISSUE(1);
    f32x4 acc[5][4];
#pragma unroll
    for (int i = 0; i < 5; ++i)
#pragma unroll
        for (int j = 0; j < 4; ++j) acc[i][j] = (f32x4){0.f, 0.f, 0.f, 0.f};
    const lds_cptr ax = (lds_cptr)lds + AA + A_X + i16 * 64 + ((g ^ (3 * ((i16 >> 2) & 1))) * 16);
    lds_cptr aw0[4], aw1[4];
#pragma unroll
    for (unsigned j = 0; j < 4; ++j) { unsigned jt = 4 * a + j; jt = jt < 15 ? jt : 14; const unsigned sg = jt / 5, wi = jt - 5 * sg;
        aw0[j] = (lds_cptr)lds + AA + sg * 5120 + wi * 32 + (8 * g + q4 + 4 * (0 ^ (g & 1))) * 160 + p4 * 8; aw1[j] = (lds_cptr)lds + AA + sg * 5120 + wi * 32 + (8 * g + q4 + 4 * (1 ^ (g & 1))) * 160 + p4 * 8; }
    VMW(5); lds_signal(ab, lane); an += 4;
    { int slot = 0;
#pragma unroll 1
      for (int t = 0; t < 20; ++t) {
        lds_wait<true>(ab, an);
        const int so = slot * A_SLOT, fs = slot == 0 ? 2 : slot - 1;
        bf16x8 xf[5], w0 = ldstr2(aw0[0] + so, aw1[0] + so), w1;
#pragma unroll
        for (int i = 0; i < 5; ++i) xf[i] = lds128(ax + so + i * 1024);
        w1 = ldstr2(aw0[1] + so, aw1[1] + so);
        SBAR();
#define A_MF(i, j, w) acc[i][j] = MFMA16(w, xf[i], acc[i][j])
#define A_LD(j) do { if (t + 2 < 20) gldsb((j) < 3 || ((j) == 3 && a < 3) ? awb : axb, aoff[j], lds0 + AA + fs * A_SLOT + (a + 4 * (j)) * 1024); } while (0)
        A_MF(0, 0, w0); A_LD(0); A_MF(1, 0, w0); A_PACE; A_MF(2, 0, w0); A_LD(1); A_MF(3, 0, w0); A_PACE; A_MF(4, 0, w0); SBAR();
        w0 = ldstr2(aw0[2] + so, aw1[2] + so);
        A_MF(0, 1, w1); A_LD(2); A_MF(1, 1, w1); A_PACE; A_MF(2, 1, w1); A_LD(3); A_MF(3, 1, w1); A_PACE; A_MF(4, 1, w1); SBAR();
        w1 = ldstr2(aw0[3] + so, aw1[3] + so);
        A_MF(0, 2, w0); A_LD(4); A_MF(1, 2, w0); A_PACE; A_MF(2, 2, w0); A_PACE; SBAR();
        awb += 32 * 3840; axb += 64;
        if (t < 18) VMW(5); else VMW(0);
        lds_signal(ab, lane); an += 4;
        A_MF(3, 2, w0); A_PACE; A_MF(4, 2, w0); A_PACE;
        A_MF(0, 3, w1); A_PACE; A_MF(1, 3, w1); A_PACE; A_MF(2, 3, w1); A_PACE; A_MF(3, 3, w1); A_PACE; A_MF(4, 3, w1); SBAR();
        slot = slot == 2 ? 0 : slot + 1;
      } }
#undef A_MF
#undef A_LD
    PSTAMP(3);
    lds_wait<true>(ab, an);
    { lds_ptr st = (lds_ptr)lds;
#pragma unroll
      for (unsigned j = 0; j < 4; ++j) { const unsigned jt = 4 * a + j, sg = jt / 5, wi = jt - 5 * sg, cq = 4 * wi + g, hl = (cq * 13) >> 6, dq = cq - 5 * hl;
        if (jt < 15) {
#pragma unroll
            for (int i = 0; i < 5; ++i) { const int row = 16 * i + i16; const f32x4 v = acc[i][j];
                if (sg < 2) *(LAS u32x2*)(st + ST_QK + row * QK_ROW + sg * 192 + hl * 48 + dq * 8) = (u32x2){pk2(v[0], v[1]), pk2(v[2], v[3])};
                else *(LAS f32x4*)(st + ST_V + row * V_ROW + hl * 80 + dq * 16) = v; } } } }
    PSTAMP(9);
    GBARS(ab, an, 4);
    PSTAMP(10);
    {
        const int sl = lane, row = 16 + sl, s = s0 + sl;
        const lds_cptr qp = (lds_cptr)lds + ST_QK + row * QK_ROW + a * 48;
        const lds_cptr kp = qp + 192 - 14 * QK_ROW;
        const u32x4 qa = *(const LAS u32x4*)qp, qb = *(const LAS u32x4*)(qp + 16); const u32x2 qc = *(const LAS u32x2*)(qp + 32);
        u32x4 ka[8], kb[8]; u32x2 kc[8];
#pragma unroll
        for (int w = 0; w < 8; ++w) { ka[w] = *(const LAS u32x4*)(kp + (14 - 2 * w) * QK_ROW); kb[w] = *(const LAS u32x4*)(kp + (14 - 2 * w) * QK_ROW + 16); kc[w] = *(const LAS u32x2*)(kp + (14 - 2 * w) * QK_ROW + 32); }
        SBAR();
        const lds_cptr vp = (lds_cptr)lds + ST_V + (row - 14) * V_ROW + a * 80;
        float lg[8]; float m = -3.0e38f;
#pragma unroll
        for (int w = 0; w < 8; ++w) {
            float x = dot2(qa[0], ka[w][0], 0.f), x2 = dot2(qa[1], ka[w][1], 0.f);
            x = dot2(qa[2], ka[w][2], x); x2 = dot2(qa[3], ka[w][3], x2); x = dot2(qb[0], kb[w][0], x); x2 = dot2(qb[1], kb[w][1], x2);
            x = dot2(qb[2], kb[w][2], x); x2 = dot2(qb[3], kb[w][3], x2); x = dot2(qc[0], kc[w][0], x); x2 = dot2(qc[1], kc[w][1], x2);
            x = (x + x2) * QSCALE;
            lg[w] = (s - 2 * w >= 0) ? x : -1.0e9f * LOG2E; m = fmaxf(m, lg[w]);
        }
        f32x4 va[3][5], vb[3][5];
#define LV(dst, w0, n) do { _Pragma("unroll") for (int w = 0; w < (n); ++w) _Pragma("unroll") for (int j = 0; j < 5; ++j) dst[w][j] = *(const LAS f32x4*)(vp + (14 - 2 * ((w0) + w)) * V_ROW + j * 16); SBAR(); } while (0)
#define PIN5(o) asm volatile("" : "+v"(o[0]), "+v"(o[1]), "+v"(o[2]), "+v"(o[3]), "+v"(o[4]))
        LV(va, 0, 3);
        float l = 0.f;
#pragma unroll
        for (int w = 0; w < 8; ++w) { lg[w] = EXP2(lg[w] - m); l += lg[w]; }
        const float inv = __builtin_amdgcn_rcpf(l);
        LV(vb, 3, 3);
        f32x4 o[5];
#pragma unroll
        for (int j = 0; j < 5; ++j) o[j] = va[0][j] * lg[0] + va[1][j] * lg[1] + va[2][j] * lg[2];
        PIN5(o); SBAR();
        LV(va, 6, 2);
#pragma unroll
        for (int j = 0; j < 5; ++j) o[j] += vb[0][j] * lg[3] + vb[1][j] * lg[4] + vb[2][j] * lg[5];
        PIN5(o); SBAR();
#pragma unroll
        for (int j = 0; j < 5; ++j) o[j] += va[0][j] * lg[6] + va[1][j] * lg[7];
#undef LV
#undef PIN5
        lds_wait(ctl + C_MKD, 1u);
#pragma unroll
        for (int j = 0; j < 5; ++j) { const unsigned w0 = pk2(o[j][0] * inv, o[j][1] * inv), w1 = pk2(o[j][2] * inv, o[j][3] * inv);
            st64(rw, ofrag_off(s, (4 * c4 + a) * DH + 4 * j), (u32x2){w0 == EMPTY ? 0x7fc07fc0u : w0, w1 == EMPTY ? 0x7fc07fc0u : w1}); }
        PSTAMP(11);
    }
    GBARS(ab, an, 4);

    PSTAMP(4);
    unsigned coff[5];
#pragma unroll
    for (unsigned i = 0; i < 5; ++i) { const unsigned p = 256 * i + a * 64 + lane, pos = p / 10, j10 = p - pos * 10; coff[i] = (pos ^ (4 * ((pos >> 3) & 1))) * 1280 + j10 * 16; }
    auto c_issue = [&](const u16* W, unsigned chunk, unsigned dst) {
        const char* sb = (const char*)(W + 80 * c4) + (size_t)chunk * (128 * 1280);
#pragma unroll
        for (unsigned i = 0; i < 5; ++i) gldsb(sb, coff[i], lds0 + dst + (4 * i + a) * 1024);
    };
    c_issue(P.woA, 0, AA); c_issue(P.woA, 1, AA + A_SLOT); c_issue(P.woA, 2, AA + 2 * A_SLOT);
    ok &= poll_tags<1>(rw, WS_FA + (rt64 * 8 + (lane & 7)) * 8, lane < 8, tag);
    __builtin_amdgcn_s_sleep(8);
    const int fo = WS_O + ((4 * rt64 + a) * 40 * 64 + lane) * 16;
    bf16x8 of[20];
#pragma unroll
    for (int ks = 0; ks < 20; ++ks) of[ks] = __builtin_bit_cast(bf16x8, ld128(rw, fo + ks * 1024));
    const float gg = __uint_as_float(ld32(rw, WS_G + (s0 + 16 * a + i16) * 4));
    const unsigned cbo = (8 * g + q4) * 160 + p4 * 8, ct0 = 4 * (0 ^ (g & 1)) * 160, ct1 = 4 * (1 ^ (g & 1)) * 160;
    f32x4 ca[5], cb[5];
#pragma unroll
    for (int j = 0; j < 5; ++j) { ca[j] = (f32x4){0.f, 0.f, 0.f, 0.f}; cb[j] = ca[j]; }
#define C_READY(F, n0, N) do { \
        bool got = false; \
        _Pragma("unroll 1") for (unsigned spins = 0; spins < SPIN_CAP; ++spins) { \
            unsigned mx = 0; \
            _Pragma("unroll") for (int u = 0; u < (N); ++u) { const u32x4 v = __builtin_bit_cast(u32x4, of[(n0) + u]); mx = max(max(mx, v[0]), max(max(v[1], v[2]), v[3])); } \
            if (!__any(mx == EMPTY)) { got = true; break; } \
            __builtin_amdgcn_s_sleep(2); \
            _Pragma("unroll") for (int u = 0; u < (N); ++u) of[(n0) + u] = __builtin_bit_cast(bf16x8, ld128(rw, fo + ((F) + (n0) + u) * 1024)); \
        } \
        ok &= got; } while (0)
#define C_COMPUTE(ACC, base, k0) do { const lds_cptr c0_ = (lds_cptr)lds + (base) + cbo + ct0, c1_ = (lds_cptr)lds + (base) + cbo + ct1; \
        _Pragma("unroll") for (int u = 0; u < 4; ++u) { _Pragma("unroll") for (int j = 0; j < 5; ++j) ACC[j] = MFMA16(ldstr2(c0_ + u * 5120 + j * 32, c1_ + u * 5120 + j * 32), of[(k0) + u], ACC[j]); \
            asm volatile("" : "+v"(ACC[0]), "+v"(ACC[1]), "+v"(ACC[2]), "+v"(ACC[3]), "+v"(ACC[4])); SBAR(); } } while (0)
    C_READY(0, 0, 20);
    VMW(0); GBARS(ab, an, 4); C_COMPUTE(ca, AA, 0);
    GBARS(ab, an, 4); c_issue(P.woA, 3, AA); C_COMPUTE(ca, AA + A_SLOT, 4);
    GBARS(ab, an, 4); c_issue(P.woA, 4, AA + A_SLOT); C_COMPUTE(ca, AA + 2 * A_SLOT, 8);
    VMW(5); GBARS(ab, an, 4); c_issue(P.woB, 0, AA + 2 * A_SLOT); C_COMPUTE(ca, AA, 12);
    VMW(5); GBARS(ab, an, 4); c_issue(P.woB, 1, AA); C_COMPUTE(ca, AA + A_SLOT, 16);
    GBARS(ab, an, 4); c_issue(P.woB, 2, AA + A_SLOT);
    PSTAMP(5);
    lds_wait(ctl + C_BD, 8u);
    c_issue(P.woB, 3, C_S3); c_issue(P.woB, 4, C_S4);
    lds_wait(ctl + C_D, 8u);
    __builtin_amdgcn_s_sleep(OB_DELAY);
    PSTAMP(6);
#pragma unroll
    for (int ks = 0; ks < 20; ++ks) of[ks] = __builtin_bit_cast(bf16x8, ld128(rw, fo + (20 + ks) * 1024));
    VMW(20);
    GBARS(ab, an, 4);
    PSTAMP(7);
#define C_STEP(ks) do { constexpr int ch_ = (ks) >> 2, u_ = (ks) & 3; constexpr int base_ = ch_ == 0 ? AA + 2 * A_SLOT : ch_ == 1 ? AA : ch_ == 2 ? AA + A_SLOT : ch_ == 3 ? C_S3 : C_S4; \
        const lds_cptr c0_ = (lds_cptr)lds + base_ + cbo + ct0 + u_ * 5120, c1_ = (lds_cptr)lds + base_ + cbo + ct1 + u_ * 5120; \
        _Pragma("unroll") for (int j = 0; j < 5; ++j) cb[j] = MFMA16(ldstr2(c0_ + j * 32, c1_ + j * 32), of[ks], cb[j]); \
        asm volatile("" : "+v"(cb[0]), "+v"(cb[1]), "+v"(cb[2]), "+v"(cb[3]), "+v"(cb[4])); SBAR(); } while (0)
#define C_GROUP(q) do { C_READY(20, 5 * (q), 5); C_STEP(5 * (q)); C_STEP(5 * (q) + 1); C_STEP(5 * (q) + 2); C_STEP(5 * (q) + 3); C_STEP(5 * (q) + 4); } while (0)
    C_GROUP(0); C_GROUP(1); C_GROUP(2); C_GROUP(3);
#undef C_GROUP
#undef C_STEP
#undef C_READY
    if (!ok && lane == 0) ctl[C_BAD] = 1u;
    GBARS(ab, an, 4);
    const float poison = rfl(ctl[C_BAD]) ? __builtin_nanf("") : 0.f;
#pragma unroll
    for (int j = 0; j < 5; ++j) { const f32x4 v = ca[j] * gg + cb[j] * (1.f - gg) + poison;
        *(u32x2*)(P.out + (size_t)(s0 + 16 * a + i16) * 640 + 80 * c4 + 16 * j + 4 * g) = (u32x2){pk2(v[0], v[1]), pk2(v[2], v[3])}; }
    PSTAMP(8);
}

__global__ __launch_bounds__(768, 3) void fused(Params P) {
    extern __shared__ __attribute__((aligned(16))) char lds[];
    PDECL
    PSTAMP(0);
    char* const ws = P.ws;
    const unsigned tid = threadIdx.x, lane = tid & 63, wid = rfl(tid >> 6);
    const unsigned b = blockIdx.x & 255, xg = b & 7, li = b >> 3;
    const unsigned rt64 = (xg >> 2) * 16 + (li >> 1), c4 = (xg & 3) * 2 + (li & 1);
    const rsrc_t rw = __builtin_amdgcn_make_buffer_rsrc(ws, 0, WS_END, 0x00020000);
    LAS unsigned* ctl = (LAS unsigned*)((lds_ptr)lds + CTL);
    if (tid < 64) ctl[tid] = 0u;
    LBAR();
    u64* stp = nullptr;
    if (wid < 8) b_waves(P, lds, ctl, rw, wid & 7, lane, b, li, rt64, c4, stp);
    else { if (APRIO) __builtin_amdgcn_s_setprio(APRIO); a_waves(P, lds, ctl, rw, wid & 3, lane, b, li, rt64, c4, stp); }
    PFLUSH();
}

static inline Params mk_params(void* const* d_in, void* d_out, void* d_ws) {
    Params P; P.x = (const u16*)d_in[0]; P.wqkv = (const u16*)d_in[1]; P.woA = (const u16*)d_in[2]; P.wq = (const u16*)d_in[4]; P.wk = (const u16*)d_in[5]; P.wv = (const u16*)d_in[6];
    P.woB = (const u16*)d_in[7]; P.gw = (const u16*)d_in[8]; P.ws = (char*)d_ws; P.out = (u16*)d_out; return P; }
extern "C" void kernel_launch(void* const* d_in, const int* in_sizes, int n_in, void* d_out, int out_size, void* d_ws, size_t ws_size, hipStream_t stream) {
    (void)hipFuncSetAttribute((const void*)fused, hipFuncAttributeMaxDynamicSharedMemorySize, LDS_TOTAL);
    fused<<<256, 768, LDS_TOTAL, stream>>>(mk_params(d_in, d_out, d_ws));
}
#define ST_LAUNCH(d_in, d_out, ws, stream) fused<<<256, 768, LDS_TOTAL, stream>>>(mk_params(d_in, d_out, ws))
```

```cpp
#include <hip/hip_runtime.h>
#include <stdint.h>
#define SWH 5
#define GRP_PIN SBAR()
#define OB_DELAY 6
#define POLL_SLEEP 4
#define POLL_DELAY lds_wait4<true>(ctl + C_QL, 8u)
#define A_PACE (void)0
#define APRIO 2

typedef unsigned short u16;
typedef unsigned long long u64;
typedef short bf16x8 __attribute__((ext_vector_type(8)));
typedef short s16x4 __attribute__((ext_vector_type(4)));
typedef float f32x16 __attribute__((ext_vector_type(16)));
typedef float f32x4 __attribute__((ext_vector_type(4)));
typedef float f32x2 __attribute__((ext_vector_type(2)));
typedef unsigned u32x4 __attribute__((ext_vector_type(4)));
typedef unsigned u32x2 __attribute__((ext_vector_type(2)));
typedef __bf16 bf2_t __attribute__((ext_vector_type(2)));
#define LAS __attribute__((address_space(3)))
#define GAS __attribute__((address_space(1)))
typedef LAS const char* lds_cptr;
typedef LAS char* lds_ptr;
typedef __amdgpu_buffer_rsrc_t rsrc_t;

__device__ __forceinline__ float bf2f(u16 b) { return __uint_as_float(((unsigned)b) << 16); }
__device__ __forceinline__ unsigned pk2(float lo, float hi) { f32x2 v = {lo, hi}; bf2_t r = __builtin_convertvector(v, bf2_t); return __builtin_bit_cast(unsigned, r); }
__device__ __forceinline__ u16 f2bf(float f) { return (u16)(pk2(f, 0.f) & 0xffffu); }
__device__ __forceinline__ float lo2f(unsigned u) { return __uint_as_float(u << 16); }
__device__ __forceinline__ float hi2f(unsigned u) { return __uint_as_float(u & 0xffff0000u); }
__device__ __forceinline__ float dot2(unsigned a, unsigned b, float c) { return __builtin_amdgcn_fdot2_f32_bf16(__builtin_bit_cast(bf2_t, a), __builtin_bit_cast(bf2_t, b), c, false); }

constexpr int S = 2048, D = 640, H = 32, DH = 20;
constexpr float SCALE = 0.22360679774997896f, LOG2E = 1.4426950408889634f;
constexpr float QSCALE = SCALE * LOG2E;

constexpr int STG_KM = 0, STG_KT = 8192, STG_VT = 12288, VT_ROW = 528, STG_BYTES = 24576;
constexpr int WS_FKV_AT = 262144;
constexpr int WS_EPOCH = 0;
constexpr int WS_FKV = WS_FKV_AT;
constexpr int WS_FO = 256;
constexpr int WS_FA = WS_FO + 2048;
constexpr int WS_NRM = 8192;
constexpr int WS_G = WS_NRM + 2048;
constexpr int WS_KV = 32768;
static_assert(WS_KV + 8 * STG_BYTES <= WS_FKV_AT, "map");
constexpr int WS_O = WS_FKV_AT + 32768;
constexpr int WS_END = WS_O + S * 1280 * 2;
constexpr int AUX_SC1 = 16;

#define MFMA32(a, b, c) __builtin_amdgcn_mfma_f32_32x32x16_bf16((a), (b), (c), 0, 0, 0)
#define MFMA16(a, b, c) __builtin_amdgcn_mfma_f32_16x16x32_bf16((a), (b), (c), 0, 0, 0)
#define SBAR() __builtin_amdgcn_sched_barrier(0)
#define EXP2(x) __builtin_amdgcn_exp2f(x)
#define WAIT_BAR(N) asm volatile("s_waitcnt vmcnt(" #N ")\n\ts_barrier" ::: "memory")
#define LBAR() asm volatile("s_waitcnt lgkmcnt(0)\n\ts_barrier" ::: "memory")
#define FULLBAR() asm volatile("s_waitcnt vmcnt(0) lgkmcnt(0)\n\ts_barrier" ::: "memory")
__device__ __forceinline__ float wave_max_pos(float f) {
    unsigned x = __float_as_uint(f);
    x = max(x, (unsigned)__builtin_amdgcn_update_dpp(0, (int)x, 0x111, 0xf, 0xf, false));
    x = max(x, (unsigned)__builtin_amdgcn_update_dpp(0, (int)x, 0x112, 0xf, 0xf, false));
    x = max(x, (unsigned)__builtin_amdgcn_update_dpp(0, (int)x, 0x114, 0xf, 0xf, false));
    x = max(x, (unsigned)__builtin_amdgcn_update_dpp(0, (int)x, 0x118, 0xf, 0xf, false));
    x = max(x, (unsigned)__builtin_amdgcn_update_dpp(0, (int)x, 0x142, 0xa, 0xf, false));
    x = max(x, (unsigned)__builtin_amdgcn_update_dpp(0, (int)x, 0x143, 0xc, 0xf, false));
    return __uint_as_float((unsigned)__builtin_amdgcn_readlane((int)x, 63));
}
__device__ __forceinline__ float sum16(float f) { auto r = __builtin_amdgcn_permlane16_swap(__float_as_uint(f), __float_as_uint(f), false, false); return __uint_as_float(r[0]) + __uint_as_float(r[1]); }
__device__ __forceinline__ float sum32(float f) { auto r = __builtin_amdgcn_permlane32_swap(__float_as_uint(f), __float_as_uint(f), false, false); return __uint_as_float(r[0]) + __uint_as_float(r[1]); }
__device__ __forceinline__ float max32(float f) { auto r = __builtin_amdgcn_permlane32_swap(__float_as_uint(f), __float_as_uint(f), false, false); return fmaxf(__uint_as_float(r[0]), __uint_as_float(r[1])); }
__device__ __forceinline__ int swap23(int x) { return (x & ~12) | ((x & 4) << 1) | ((x & 8) >> 1); }
__device__ __forceinline__ void glds16(const void* g, unsigned lds_base) {
    unsigned sv; asm volatile("s_mov_b32 %0, m0\n\ts_mov_b32 m0, %2\n\ts_nop 0\n\tglobal_load_lds_dwordx4 %1, off\n\ts_mov_b32 m0, %0" : "=&s"(sv) : "v"(g), "s"(lds_base) : "memory"); }
__device__ __forceinline__ void gldsb(const void* sbase, unsigned voff, unsigned lds_base) {
    unsigned sv; asm volatile("s_mov_b32 %0, m0\n\ts_mov_b32 m0, %3\n\ts_nop 0\n\tglobal_load_lds_dwordx4 %1, %2\n\ts_mov_b32 m0, %0" : "=&s"(sv) : "v"(voff), "s"(sbase), "s"(lds_base) : "memory"); }
__device__ __forceinline__ void gldsb_sc1(const void* sbase, unsigned voff, unsigned lds_base) {
    unsigned sv; asm volatile("s_mov_b32 %0, m0\n\ts_mov_b32 m0, %3\n\ts_nop 0\n\tglobal_load_lds_dwordx4 %1, %2 sc1\n\ts_mov_b32 m0, %0" : "=&s"(sv) : "v"(voff), "s"(sbase), "s"(lds_base) : "memory"); }
__device__ __forceinline__ bf16x8 lds128(lds_cptr p) { return *(const LAS bf16x8*)p; }
typedef short v4i16_t __attribute__((ext_vector_type(4)));
__device__ __forceinline__ s16x4 ldstr(lds_cptr p) { return __builtin_bit_cast(s16x4, __builtin_amdgcn_ds_read_tr16_b64_v4i16((LAS v4i16_t*)p)); }
__device__ __forceinline__ bf16x8 ldstr2(lds_cptr p0, lds_cptr p1) { const s16x4 a = ldstr(p0), b = ldstr(p1); return __builtin_shufflevector(a, b, 0, 1, 2, 3, 4, 5, 6, 7); }
__device__ __forceinline__ unsigned rfl(unsigned x) { return (unsigned)__builtin_amdgcn_readfirstlane(x); }
__device__ __forceinline__ void st32(rsrc_t r, int off, unsigned v) { __builtin_amdgcn_raw_buffer_store_b32(v, r, off, 0, AUX_SC1); }
__device__ __forceinline__ void st64(rsrc_t r, int off, u32x2 v) { __builtin_amdgcn_raw_buffer_store_b64(v, r, off, 0, AUX_SC1); }
__device__ __forceinline__ void st128(rsrc_t r, int off, u32x4 v) { __builtin_amdgcn_raw_buffer_store_b128(v, r, off, 0, AUX_SC1); }
__device__ __forceinline__ unsigned ld32(rsrc_t r, int off) { return __builtin_amdgcn_raw_buffer_load_b32(r, off, 0, AUX_SC1); }
__device__ __forceinline__ u32x2 ld64(rsrc_t r, int off) { return __builtin_amdgcn_raw_buffer_load_b64(r, off, 0, AUX_SC1); }
__device__ __forceinline__ u32x4 ld128(rsrc_t r, int off) { return __builtin_amdgcn_raw_buffer_load_b128(r, off, 0, AUX_SC1); }
__device__ __forceinline__ u32x4 ld128s(rsrc_t r, int voff, int soff) { return __builtin_amdgcn_raw_buffer_load_b128(r, voff, soff, AUX_SC1); }

#define PSTAMP(i)
#define PDECL
#define PFLUSH()

#define QT(i)
#define QFLUSH()
struct Params { const u16 *x, *wqkv, *woA, *wq, *wk, *wv, *woB, *gw; char* ws; u16* out; };

constexpr int LDS_TOTAL = 163840, CTL = LDS_TOTAL - 256, DUMMY = CTL - 256;
constexpr int KVB = 2 * STG_BYTES;
constexpr int Q_SLOT = 18432, Q_W = 8192, Q_ST = 73728, QB_ROW = 208;
constexpr int AA = 98304;
constexpr int P0W = AA, P0X = AA + 25664, P0G = P0X + 20480;
constexpr int A_SLOT = 20480, A_X = 15360;
constexpr int ST_QK = AA, ST_V = AA + 32000, QK_ROW = 400, V_ROW = 336;
constexpr int C_S3 = 49152, C_S4 = 69632;
constexpr int C_FALL = 0, C_BB = 1, C_DONE = 2, C_LAND = 4, C_X = 6, C_BD = 7, C_D = 12, C_AB = 13, C_KVOK = 16, C_BAD = 17, C_P0I = 18, C_QL = 20, C_QD = 24, C_TAG = 28, C_QS = 19, C_MKD = 15, C_P = 32, C_WB = 40, C_NRM = 42, C_R = 44;
constexpr unsigned CODE_LINES = 296;
constexpr unsigned EMPTY = 0xFFFFFFFFu;
constexpr unsigned SPIN_CAP = 1u << 18;

template <int N, int SLEEP = 4>
__device__ __forceinline__ bool poll_tags(rsrc_t r, int off, bool active, u64 tag) {
#pragma unroll 1
    for (unsigned spins = 0; spins < SPIN_CAP; ++spins) {
        bool ok = true;
        if (active) {
#pragma unroll
            for (int i = 0; i < N; ++i) { const u32x2 v = ld64(r, off + 8 * i); ok &= (v[0] == (unsigned)tag) & (v[1] == (unsigned)(tag >> 32)); }
        }
        if (__all(ok)) return true;
        __builtin_amdgcn_s_sleep(SLEEP);
    }
    return false;
}
__device__ __forceinline__ void lds_signal(LAS unsigned* cnt, int lane) {
    (void)lane;
    __builtin_amdgcn_sched_barrier(0);
    asm volatile("s_mov_b64 exec, 1\n\tds_add_u32 %0, %1\n\ts_mov_b64 exec, -1\n\ts_nop 3" :: "v"((unsigned)(uintptr_t)cnt), "v"(1u) : "memory");
    __builtin_amdgcn_sched_barrier(0); }
template <bool SLEEP = true>
__device__ __forceinline__ void lds_wait(LAS unsigned* cnt, unsigned n) {
    unsigned v, sv, left;
    if (SLEEP)
        asm volatile("s_mov_b32 %2, 0x100000\n1:\n\tds_read_b32 %0, %3\n\ts_waitcnt lgkmcnt(0)\n\tv_readfirstlane_b32 %1, %0\n\ts_cmp_ge_u32 %1, %4\n\ts_cbranch_scc1 2f\n\ts_sleep 1\n\ts_sub_u32 %2, %2, 1\n\ts_cbranch_scc0 1b\n2:"
                     : "=&v"(v), "=&s"(sv), "=&s"(left) : "v"((unsigned)(uintptr_t)cnt), "s"(n) : "memory", "scc");
    else
        asm volatile("s_mov_b32 %2, 0x100000\n1:\n\tds_read_b32 %0, %3\n\ts_waitcnt lgkmcnt(0)\n\tv_readfirstlane_b32 %1, %0\n\ts_cmp_ge_u32 %1, %4\n\ts_cbranch_scc1 2f\n\ts_sub_u32 %2, %2, 1\n\ts_cbranch_scc0 1b\n2:"
                     : "=&v"(v), "=&s"(sv), "=&s"(left) : "v"((unsigned)(uintptr_t)cnt), "s"(n) : "memory", "scc");
}
template <bool SLEEP = false>
__device__ __forceinline__ void lds_wait4(LAS unsigned* cnt, unsigned n) {
#pragma unroll 1
    for (unsigned spins = 0; spins < (1u << 20); ++spins) {
        const u32x4 v = *(volatile LAS u32x4*)cnt;
        const unsigned lo = min(min(v[0], v[1]), min(v[2], v[3]));
        if (rfl(lo) >= n) break;
        if (SLEEP) __builtin_amdgcn_s_sleep(1);
    }
    asm volatile("" ::: "memory");
}
#define VMW(N) asm volatile("s_waitcnt vmcnt(" #N ")" ::: "memory")
#define GBAR(cnt, n, step) do { lds_signal(cnt, lane); n += (step); lds_wait<false>(cnt, n); } while (0)
#define GBARS(cnt, n, step) do { lds_signal(cnt, lane); n += (step); lds_wait<true>(cnt, n); } while (0)

#define KV_GLDS gldsb_sc1
__device__ __forceinline__ void att_stage_load(const char* kv, int st, unsigned ldsbuf, int w4, int lane) {
    const char* src = kv + (size_t)st * STG_BYTES + w4 * 1024;
#pragma unroll
    for (int i = 0; i < 6; ++i) KV_GLDS(src + i * 4096, lane * 16, ldsbuf + (w4 + 4 * i) * 1024);
}

__device__ __forceinline__ void att_maxpass(const char* kv, char* ldsh, LAS unsigned* bb, unsigned& bn, int kh, int w4, int lane, const bf16x8 (&qm)[2], const bf16x8 (&qt)[2][2], float (&mx)[2]) {
    const unsigned lds0 = (unsigned)(uintptr_t)ldsh;
    const lds_cptr kbase = (lds_cptr)ldsh + lane * 16;
    const f32x16 zero = {};
#pragma unroll 1
    for (int rnd = 0; rnd < 3; ++rnd) {
        if (rnd) { GBAR(bb, bn, 8); att_stage_load(kv, 4 * kh + 2 * (rnd & 1), lds0, w4, lane); att_stage_load(kv, 4 * kh + 2 * (rnd & 1) + 1, lds0 + KVB, w4, lane); }
        VMW(0); GBAR(bb, bn, 8);
        if (rnd < 2) {
#pragma unroll 1
            for (int pr = 0; pr < 8; ++pr) {
                const lds_cptr kb = kbase + (pr >> 2) * KVB; const int p = pr & 3;
                const bf16x8 kt = lds128(kb + STG_KT + p * 1024), k0 = lds128(kb + STG_KM + (2 * p) * 1024), k1 = lds128(kb + STG_KM + (2 * p + 1) * 1024);
#pragma unroll
                for (int h = 0; h < 2; ++h) {
                    f32x16 s0 = MFMA32(k0, qm[h], zero); s0 = MFMA32(kt, qt[h][0], s0);
                    f32x16 s1 = MFMA32(k1, qm[h], zero); s1 = MFMA32(kt, qt[h][1], s1);
#pragma unroll
                    for (int i = 0; i < 16; ++i) mx[h] = fmaxf(mx[h], fmaxf(s0[i], s1[i]));
                }
            }
        }
    }
}

__device__ __forceinline__ void att_main(const char* kv, char* ldsh, LAS unsigned* ctl, rsrc_t rw, int mark_off, int kbase0, int fm_off, int kh, int w4, int lane, const bf16x8 (&qm)[2], const bf16x8 (&qt)[2][2], f32x16& O0, f32x16& O1) {
    const unsigned lds0 = (unsigned)(uintptr_t)ldsh;
    LAS unsigned* cdone = ctl + C_DONE + kh; LAS unsigned* cland = ctl + C_LAND + kh;
    const int r = lane & 31, hh = lane >> 5;
    const int voff = r <= 20 ? r * VT_ROW : (r < 28 ? 21 * VT_ROW : 21 * VT_ROW + 112);
    const lds_cptr kbase = (lds_cptr)ldsh + lane * 16;
    const lds_cptr vbase = (lds_cptr)ldsh + STG_VT + voff + hh * 16;
    const f32x16 zero = {};
    f32x16 sA, sB; u32x4 pA0, pA1, pB0, pB1; bf16x8 kmA, kmB, ktA, ktB, vA0, vA1, vB0, vB1;
    ktA = lds128(kbase + STG_KT); kmA = lds128(kbase + STG_KM);
    sA = MFMA32(kmA, qm[0], zero); sA = MFMA32(ktA, qt[0][0], sA);
    pB0 = (u32x4){0u, 0u, 0u, 0u}; pB1 = pB0; vB0 = kmA; vB1 = kmA; ktB = ktA; vA0 = kmA; vA1 = kmA; kmB = kmA;
    float ee0 = 0.f, ee1 = 0.f, ee2 = 0.f, ee3 = 0.f;
#define GRP(MF, LD, SC, I, PD, J) do { MF; GRP_PIN; LD; const float e0_ = EXP2(SC[I]); unsigned a_ = pk2(ee0, ee1); const float e1_ = EXP2(SC[I + 1]); unsigned b_ = pk2(ee2, ee3); const float e2_ = EXP2(SC[I + 2]), e3_ = EXP2(SC[I + 3]); \
        ee0 = e0_; ee1 = e1_; ee2 = e2_; ee3 = e3_; asm volatile("" : "+v"(a_), "+v"(b_), "+v"(ee0), "+v"(ee1), "+v"(ee2), "+v"(ee3)); PD[J] = a_; PD[J + 1] = b_; SBAR(); } while (0)
#define TILE(KM, KMN, KT, VJ, KTN, VJN, VC0, VC1, VP0, VP1, VADDR, LDK, LDT) do { \
        GRP(sB = MFMA32(KM, qm[1], zero), VC0 = lds128(VADDR), sA, 0, pB1, 2); \
        GRP(O1 = MFMA32(VP0, __builtin_bit_cast(bf16x8, pB0), O1), VC1 = lds128((VADDR) + 32), sA, 4, pA0, 0); \
        GRP(sB = MFMA32(KT, qt[1][VJ], sB), LDK, sA, 8, pA0, 2); \
        GRP(O1 = MFMA32(VP1, __builtin_bit_cast(bf16x8, pB1), O1), LDT, sA, 12, pA1, 0); \
        GRP(sA = MFMA32(KMN, qm[0], zero), (void)0, sB, 0, pA1, 2); \
        GRP(O0 = MFMA32(VC0, __builtin_bit_cast(bf16x8, pA0), O0), (void)0, sB, 4, pB0, 0); \
        GRP(sA = MFMA32(KTN, qt[0][VJN], sA), (void)0, sB, 8, pB0, 2); \
        GRP(O0 = MFMA32(VC1, __builtin_bit_cast(bf16x8, pA1), O0), (void)0, sB, 12, pB1, 0); } while (0)
#pragma unroll 1
    for (int st = 0; st < 4; ++st) {
        if (kh == 1) { if (st == 0) __builtin_amdgcn_s_setprio(1); if (2 * st == SWH) __builtin_amdgcn_s_setprio(0); }
        const int buf = st & 1;
        const lds_cptr kb = kbase + buf * KVB, vb = vbase + buf * KVB, kn = kbase + (buf ^ 1) * KVB;
        TILE(kmA, kmB, ktA, 0, ktA, 1, vA0, vA1, vB0, vB1, vb + 0 * 64, kmB = lds128(kb + STG_KM + 1 * 1024), (void)0);
        if (st) { lds_signal(st == 3 ? ctl + C_X : cdone, lane); SBAR(); }
        TILE(kmB, kmA, ktA, 1, ktB, 0, vB0, vB1, vA0, vA1, vb + 1 * 64, kmA = lds128(kb + STG_KM + 2 * 1024), ktB = lds128(kb + STG_KT + 1024));
        if (st == 0 && kh == 0) {
#pragma unroll 1
            for (int pc = w4; pc < 20; pc += 4) { const int k = kbase0 + 8 * pc + (pc < 10 ? 0 : 560); st128(rw, mark_off + ((k >> 5) * 64 + ((k >> 3) & 3) * 16) * 16, (u32x4){EMPTY, EMPTY, EMPTY, EMPTY}); }
            SBAR(); }
        if (st == 1 || st == 2) { lds_wait(cdone, 4u * st); att_stage_load(kv, 4 * kh + st + 1, lds0 + (buf ^ 1) * KVB, w4, lane); SBAR(); }
        TILE(kmA, kmB, ktB, 0, ktB, 1, vA0, vA1, vB0, vB1, vb + 2 * 64, kmB = lds128(kb + STG_KM + 3 * 1024), (void)0);
        TILE(kmB, kmA, ktB, 1, ktA, 0, vB0, vB1, vA0, vA1, vb + 3 * 64, kmA = lds128(kb + STG_KM + 4 * 1024), ktA = lds128(kb + STG_KT + 2048));
        if (kh == 1 && 2 * st + 1 == SWH) __builtin_amdgcn_s_setprio(0);
        TILE(kmA, kmB, ktA, 0, ktA, 1, vA0, vA1, vB0, vB1, vb + 4 * 64, kmB = lds128(kb + STG_KM + 5 * 1024), (void)0);
        TILE(kmB, kmA, ktA, 1, ktB, 0, vB0, vB1, vA0, vA1, vb + 5 * 64, kmA = lds128(kb + STG_KM + 6 * 1024), ktB = lds128(kb + STG_KT + 3072));
        if (st < 3) { VMW(0); lds_signal(cland, lane); SBAR(); }
        TILE(kmA, kmB, ktB, 0, ktB, 1, vA0, vA1, vB0, vB1, vb + 6 * 64, kmB = lds128(kb + STG_KM + 7 * 1024), (void)0);
        if (st < 3) { lds_wait(cland, 4u * (st + 1));
            if (st == 0 && kh == 0 && w4 == 0) { if (lane == 0) st64(rw, fm_off, (u32x2){ctl[C_TAG + 1], ctl[C_TAG + 2]}); lds_signal(ctl + C_MKD, lane); }
            SBAR(); }
        TILE(kmB, kmA, ktB, 1, ktA, 0, vB0, vB1, vA0, vA1, vb + 7 * 64, kmA = lds128(kn + STG_KM + 0 * 1024), ktA = lds128(kn + STG_KT));
    }
    pB1[2] = pk2(ee0, ee1); pB1[3] = pk2(ee2, ee3);
    O1 = MFMA32(vB0, __builtin_bit_cast(bf16x8, pB0), O1);
    O1 = MFMA32(vB1, __builtin_bit_cast(bf16x8, pB1), O1);
#undef GRP
#undef TILE
}

__device__ __forceinline__ int ofrag_off(int s, int k) { return WS_O + ((((s >> 4) * 40 + (k >> 5)) * 64 + ((k >> 3) & 3) * 16 + (s & 15)) * 16 + (k & 7) * 2); }
__device__ __forceinline__ void touch(const void* g, unsigned lds_dummy) {
    unsigned sv; asm volatile("s_mov_b32 %0, m0\n\ts_mov_b32 m0, %2\n\ts_nop 0\n\tglobal_load_lds_dword %1, off\n\ts_mov_b32 m0, %0" : "=&s"(sv) : "v"(g), "s"(lds_dummy) : "memory"); }

__device__ __forceinline__ constexpr int wb_at(int ks) { return ((ks >> 2) == 0 ? AA + 2 * A_SLOT : (ks >> 2) == 1 ? AA : (ks >> 2) == 2 ? AA + A_SLOT : (ks >> 2) == 3 ? C_S3 : C_S4) + (ks & 3) * 5120; }
template <int KS0>
__device__ __forceinline__ bool ob_part(char* lds, LAS unsigned* ctl, rsrc_t rw, int fo, unsigned lane, f32x4 (&cc)[5]) {
    const unsigned i16 = lane & 15, g = lane >> 4, q4 = i16 >> 2, p4 = i16 & 3;
    const lds_cptr cw0 = (lds_cptr)lds + (8 * g + q4 + 4 * (0 ^ (g & 1))) * 160 + p4 * 8, cw1 = (lds_cptr)lds + (8 * g + q4 + 4 * (1 ^ (g & 1))) * 160 + p4 * 8;
    const int fl = lane * 16;
    bf16x8 of[10], wA[5], wB[5]; bool ok = true;
#pragma unroll
    for (int u = 0; u < 10; ++u) of[u] = __builtin_bit_cast(bf16x8, ld128s(rw, fl, fo + (20 + KS0 + u) * 1024));
    if (KS0 == 0) { VMW(10); lds_signal(ctl + C_WB + 1, lane); }
    lds_wait(ctl + C_WB + (KS0 ? 1 : 0), 4u);
#define O_READY(n0, N) do { bool got = false; \
        _Pragma("unroll 1") for (unsigned spins = 0; spins < SPIN_CAP; ++spins) { unsigned mx = 0; \
            _Pragma("unroll") for (int u = 0; u < (N); ++u) { const u32x4 v = __builtin_bit_cast(u32x4, of[(n0) + u]); mx = max(max(mx, v[0]), max(max(v[1], v[2]), v[3])); } \
            if (!__any(mx == EMPTY)) { got = true; break; } \
            __builtin_amdgcn_s_sleep(2); \
            _Pragma("unroll") for (int u = 0; u < (N); ++u) of[(n0) + u] = __builtin_bit_cast(bf16x8, ld128s(rw, fl, fo + (20 + KS0 + (n0) + u) * 1024)); } \
        ok &= got; } while (0)
#define O_RD(W, u) do { lds_cptr b0_ = cw0 + wb_at(KS0 + (u)), b1_ = cw1 + wb_at(KS0 + (u)); asm volatile("" : "+v"(b0_), "+v"(b1_)); _Pragma("unroll") for (int j = 0; j < 5; ++j) W[j] = ldstr2(b0_ + j * 32, b1_ + j * 32); SBAR(); } while (0)
#define O_MF(W, u) do { _Pragma("unroll") for (int j = 0; j < 5; ++j) cc[j] = MFMA16(W[j], of[u], cc[j]); asm volatile("" : "+v"(cc[0]), "+v"(cc[1]), "+v"(cc[2]), "+v"(cc[3]), "+v"(cc[4])); SBAR(); } while (0)
    O_RD(wA, 0);
    O_READY(0, 5);
    O_RD(wB, 1); O_MF(wA, 0); O_RD(wA, 2); O_MF(wB, 1); O_RD(wB, 3); O_MF(wA, 2); O_RD(wA, 4); O_MF(wB, 3); O_RD(wB, 5); O_MF(wA, 4);
    O_READY(5, 5);
    O_RD(wA, 6); O_MF(wB, 5); O_RD(wB, 7); O_MF(wA, 6); O_RD(wA, 8); O_MF(wB, 7); O_RD(wB, 9); O_MF(wA, 8); O_MF(wB, 9);
#undef O_READY
#undef O_RD
#undef O_MF
    return ok;
}

__device__ __forceinline__ void b_waves(const Params& P, char* lds, LAS unsigned* ctl, rsrc_t rw, unsigned wid, unsigned lane, unsigned b, unsigned li, unsigned rt64, unsigned c4, u64* stamps) {
    char* const ws = P.ws;
    const unsigned lds0 = (unsigned)(uintptr_t)lds;
    const int s0 = 64 * rt64;
    const unsigned i16 = lane & 15, g = lane >> 4, q4 = i16 >> 2, p4 = i16 & 3;
    unsigned bn = 0; LAS unsigned* bb = ctl + C_BB;
    f32x4 qa[5];
#pragma unroll
    for (int j = 0; j < 5; ++j) qa[j] = (f32x4){0.f, 0.f, 0.f, 0.f};
    if (wid >= 4) {
        const unsigned l = wid - 4;
        unsigned off[5];
#pragma unroll
        for (unsigned j = 0; j < 5; ++j) { const unsigned id = l + 4 * j;
            if (j < 2) { const unsigned p = id * 64 + lane, row = p >> 3, ch = (p & 7) ^ ((row >> 1) & 7); off[j] = row * 1280 + ch * 16; }
            else { const unsigned p = ((id < 18 ? id : 8) - 8) * 64 + lane, pos = p / 10, j10 = p - pos * 10, k = pos ^ (4 * ((pos >> 3) & 1)); off[j] = k * 1280 + j10 * 16; } }
        const char* xs = (const char*)(P.x + (size_t)s0 * D); const char* wsq = (const char*)(P.wq + 80 * c4);
        int slot = 0;
        lds_wait<false>(ctl + C_P0I, 4u);
#pragma unroll 1
        for (int t = 0; t < 10; ++t) {
            if (t >= 5) lds_wait4(ctl + C_QD, t - 4);
            const unsigned so = lds0 + slot * Q_SLOT + l * 1024;
            gldsb(xs, off[0], so); gldsb(xs, off[1], so + 4096); gldsb(wsq, off[2], so + 8192); gldsb(wsq, off[3], so + 12288);
            if (l < 2) gldsb(wsq, off[4], so + 16384);
            xs += 128; wsq += 64 * 1280;
            if (t >= 3) { VMW(12); lds_signal(ctl + C_QL + l, lane); }
            slot = slot == 4 ? 0 : slot + 1;
        }
        VMW(8); lds_signal(ctl + C_QL + l, lane); VMW(4); lds_signal(ctl + C_QL + l, lane); VMW(0); lds_signal(ctl + C_QL + l, lane);
        PSTAMP(1);
    } else {
        const lds_cptr qx0 = (lds_cptr)lds + (16 * wid + i16) * 128 + ((g ^ (i16 >> 1)) * 16), qx1 = (lds_cptr)lds + (16 * wid + i16) * 128 + (((4 + g) ^ (i16 >> 1)) * 16);
        const lds_cptr qw0 = (lds_cptr)lds + Q_W + (8 * g + q4 + 4 * (0 ^ (g & 1))) * 160 + p4 * 8, qw1 = (lds_cptr)lds + Q_W + (8 * g + q4 + 4 * (1 ^ (g & 1))) * 160 + p4 * 8;
        if (wid < 3) { const unsigned idx = wid * 64 + lane, row = (li >> 1) + 16 * (idx >> 1);
            if (idx < 160) touch((const char*)((row < 640 ? P.woA + (size_t)row * 640 : P.woB + (size_t)(row - 640) * 640) + 80 * c4) + (idx & 1) * 156, lds0 + DUMMY); }
        {
            const unsigned ri = li >> 1, cc = li & 1, l2 = lane - 32;
#pragma unroll 1
            for (unsigned j = 5 + wid; j < 10; j += 4) {
                const char* p = lane < 32 ? (const char*)(P.x + (size_t)(s0 + 2 * lane + cc) * D) + j * 128 : (const char*)(P.wq + 80 * c4) + (size_t)((64 * j + ri + 16 * ((l2 >> 1) & 3)) * 1280) + (l2 & 1) * 156;
                touch(p, lds0 + DUMMY); } }
        PSTAMP(1);
        bf16x8 xa0, xa1, wa[10], xb0, xb1, wb_[10];
#define Q_READ(X0, X1, W, t_, so_) do { lds_wait4(ctl + C_QL, (t_) + 1); X0 = lds128(qx0 + (so_)); X1 = lds128(qx1 + (so_)); \
            _Pragma("unroll") for (int j = 0; j < 10; ++j) W[j] = ldstr2(qw0 + (so_) + (j / 5) * 5120 + (j % 5) * 32, qw1 + (so_) + (j / 5) * 5120 + (j % 5) * 32); SBAR(); } while (0)
#define Q_MUL(X0, X1, W) do { _Pragma("unroll") for (int j = 0; j < 5; ++j) qa[j] = MFMA16(W[j], X0, qa[j]); _Pragma("unroll") for (int j = 0; j < 5; ++j) qa[j] = MFMA16(W[5 + j], X1, qa[j]); \
            asm volatile("" : "+v"(qa[0]), "+v"(qa[1]), "+v"(qa[2]), "+v"(qa[3]), "+v"(qa[4])); SBAR(); } while (0)
#define Q_DONE() do { asm volatile("s_waitcnt lgkmcnt(0)" ::: "memory"); lds_signal(ctl + C_QD + wid, lane); } while (0)
        Q_READ(xa0, xa1, wa, 0, 0); Q_DONE();
        int slot = 1;
#pragma unroll 1
        for (int t = 0; t < 10; t += 2) {
            Q_READ(xb0, xb1, wb_, t + 1, slot * Q_SLOT); slot = slot == 4 ? 0 : slot + 1;
            Q_MUL(xa0, xa1, wa); Q_DONE();
            if (t + 2 < 10) { Q_READ(xa0, xa1, wa, t + 2, slot * Q_SLOT); slot = slot == 4 ? 0 : slot + 1; }
            Q_MUL(xb0, xb1, wb_);
            if (t + 2 < 10) Q_DONE();
        }
#undef Q_READ
#undef Q_MUL
#undef Q_DONE
    }
    PSTAMP(2);
    if (wid == 4) {
        lds_wait<true>(ctl + C_TAG, 1u);
        const u64 tag = (u64)ctl[C_TAG + 1] | ((u64)ctl[C_TAG + 2] << 32);
        bool ok = false;
#pragma unroll 1
        for (unsigned spins = 0; spins < SPIN_CAP; ++spins) {
            const u32x4 v = ld128(rw, WS_FKV + li * 1024 + lane * 16);
            if (__all((v[0] == (unsigned)tag) & (v[1] == (unsigned)tag) & (v[2] == (unsigned)tag) & (v[3] == (unsigned)tag))) { ok = true; break; }
            __builtin_amdgcn_s_sleep(POLL_SLEEP);
        }
        if (!ok && lane == 0) ctl[C_BAD] = 1u;
        lds_signal(ctl + C_KVOK, lane);
        if (b == 0 && lane == 0) st64(rw, WS_EPOCH, (u32x2){(unsigned)tag, (unsigned)(tag >> 32)});
    }
    const char* kv = ws + WS_KV;
    const unsigned w4 = wid & 3, kh = wid >> 2;
    char* ldsh = lds + kh * STG_BYTES;
    const unsigned ldsh0 = lds0 + kh * STG_BYTES;
    u32x4 nr0 = {0u, 0u, 0u, 0u}, nr1 = nr0;
    if (wid < 4) {
        lds_wait4(ctl + C_QD, 10u);
#pragma unroll
        for (int j = 0; j < 5; ++j) { const unsigned cq = 4 * j + g, hl = (cq * 13) >> 6, dq = cq - 5 * hl;
            *(LAS u32x2*)((lds_ptr)lds + Q_ST + (16 * wid + i16) * QB_ROW + hl * 48 + dq * 8) = (u32x2){pk2(qa[j][0] * QSCALE, qa[j][1] * QSCALE), pk2(qa[j][2] * QSCALE, qa[j][3] * QSCALE)}; }
        lds_signal(ctl + C_QS, lane);
    } else {
        lds_wait<false>(ctl + C_KVOK, 1u);
        asm volatile("global_load_dwordx4 %0, %2, %3 sc1\n\tglobal_load_dwordx4 %1, %2, %3 offset:1024 sc1" : "=&v"(nr0), "=&v"(nr1) : "v"(lane * 16), "s"(ws + WS_NRM) : "memory");
        lds_wait4(ctl + C_QD, 8u);
        att_stage_load(kv, 0, lds0, w4, lane); att_stage_load(kv, 4, lds0 + STG_BYTES, w4, lane);
    }
    lds_wait<false>(ctl + C_QS, 4u);
    const int r = lane & 31, hh = lane >> 5;
    bf16x8 qm[2]; u32x2 qtl[2];
#pragma unroll
    for (int h = 0; h < 2; ++h) {
        const lds_cptr qp = (lds_cptr)lds + Q_ST + (32 * (w4 & 1) + r) * QB_ROW + (2 * (w4 >> 1) + h) * 48;
        qm[h] = lds128(qp + hh * 16); qtl[h] = *(const LAS u32x2*)(qp + 32);
    }
    float q2[2];
#pragma unroll
    for (int h = 0; h < 2; ++h) {
        q2[h] = 0.f;
#pragma unroll
        for (int i = 0; i < 8; ++i) { const float v = bf2f((u16)qm[h][i]); q2[h] += v * v; }
        if (hh == 0) { const float a0 = lo2f(qtl[h][0]), a1 = hi2f(qtl[h][0]), a2 = lo2f(qtl[h][1]), a3 = hi2f(qtl[h][1]); q2[h] += a0 * a0 + a1 * a1 + a2 * a2 + a3 * a3; }
        q2[h] = sum32(q2[h]);
    }
    if (wid == 7) {
        asm volatile("s_waitcnt vmcnt(12)" : "+v"(nr0), "+v"(nr1) :: "memory");
        const float kmax2 = wave_max_pos(fmaxf(__uint_as_float(nr0[0]), __uint_as_float(nr1[0]))) + wave_max_pos(fmaxf(__uint_as_float(nr0[1]), __uint_as_float(nr1[1])));
        const float vmax = wave_max_pos(fmaxf(fmaxf(__uint_as_float(nr0[2]), __uint_as_float(nr1[2])), fmaxf(__uint_as_float(nr0[3]), __uint_as_float(nr1[3]))));
        if (lane == 0) { ctl[C_NRM] = __float_as_uint(kmax2); ctl[C_NRM + 1] = __float_as_uint(112.f - fmaxf(0.f, __builtin_amdgcn_logf(fmaxf(vmax, 1.f)))); }
    }
    PSTAMP(3);
    VMW(0);
    asm volatile("" :: "v"(nr0), "v"(nr1));
    GBAR(bb, bn, 8);
    const float kmax2 = __uint_as_float(ctl[C_NRM]), lim = __uint_as_float(ctl[C_NRM + 1]);
    const float bnd0 = __builtin_sqrtf(q2[0] * kmax2) * 1.001f + 1.f, bnd1 = __builtin_sqrtf(q2[1] * kmax2) * 1.001f + 1.f;
    const u32x4 z4 = {0u, 0u, 0u, 0u};
    bf16x8 qt[2][2];
#pragma unroll
    for (int h = 0; h < 2; ++h) { const u32x4 a = {qtl[h][0], qtl[h][1], 0u, 0u}; qt[h][0] = __builtin_bit_cast(bf16x8, hh ? z4 : a); qt[h][1] = __builtin_bit_cast(bf16x8, hh ? a : z4); }
    const f32x16 zero = {};
    float m[2]; bool unsafe = false;
    { const lds_cptr kb = (lds_cptr)ldsh + lane * 16; const bf16x8 k0 = lds128(kb + STG_KM), kt = lds128(kb + STG_KT);
#pragma unroll
      for (int h = 0; h < 2; ++h) {
        f32x16 sc = MFMA32(k0, qm[h], zero); sc = MFMA32(kt, qt[h][0], sc);
        float mm = sc[0];
#pragma unroll
        for (int i = 1; i < 16; ++i) mm = fmaxf(mm, sc[i]);
        mm = max32(mm);
        m[h] = bf2f(f2bf(mm));
        unsafe |= !((h ? bnd1 : bnd0) - m[h] <= lim);
      } }
    if (__any(unsafe) && lane == 0) ctl[C_FALL] = 1u;
    GBAR(bb, bn, 8);
    att_stage_load(kv, 4 * kh + 1, ldsh0 + KVB, w4, lane);
    if (rfl(ctl[C_FALL]) != 0u) {
        att_maxpass(kv, ldsh, bb, bn, kh, w4, lane, qm, qt, m);
#pragma unroll
        for (int h = 0; h < 2; ++h) m[h] = bf2f(f2bf(max32(m[h])));
    }
#pragma unroll
    for (int h = 0; h < 2; ++h) { const unsigned nm = f2bf(-m[h]); const u32x4 a = {qtl[h][0], qtl[h][1], nm, 0u}; qt[h][0] = __builtin_bit_cast(bf16x8, hh ? z4 : a); qt[h][1] = __builtin_bit_cast(bf16x8, hh ? a : z4); }
    f32x16 O[2] = {{}, {}};
    PSTAMP(4);
    att_main(kv, ldsh, ctl, rw, ofrag_off(s0 + lane, 0), 80 * c4, WS_FA + (rt64 * 8 + c4) * 8, kh, w4, lane, qm, qt, O[0], O[1]);
    PSTAMP(5);
    lds_signal(ctl + C_BD, lane);
    {
        LAS float* xo = (LAS float*)((lds_ptr)lds + (2 * w4 + kh) * 3328);
        LAS float* xi = (LAS float*)((lds_ptr)lds + (2 * w4 + (kh ^ 1)) * 3328);
        lds_wait(ctl + C_X, 8u);
#pragma unroll
        for (int i = 0; i < 12; ++i) xo[i * 64 + lane] = kh ? O[0][i] : O[1][i];
        xo[12 * 64 + lane] = kh ? m[0] : m[1];
        lds_signal(ctl + C_P + 2 * w4 + kh, lane);
        lds_wait<false>(ctl + C_P + 2 * w4 + (kh ^ 1), 1u);
        const float mo = kh ? m[1] : m[0], m1 = xi[12 * 64 + lane];
        const float M = fmaxf(mo, m1), f0 = EXP2(mo - M), f1 = EXP2(m1 - M);
        float o[12];
#pragma unroll
        for (int i = 0; i < 12; ++i) o[i] = (kh ? O[1][i] : O[0][i]) * f0 + xi[i * 64 + lane] * f1;
        auto lr = __builtin_amdgcn_permlane32_swap(__float_as_uint(o[8]), __float_as_uint(o[8]), false, false);
        const float inv = __builtin_amdgcn_rcpf(__uint_as_float(lr[1]));
        const int s = s0 + 32 * (w4 & 1) + r, k0 = 640 + (4 * c4 + 2 * (w4 >> 1) + kh) * DH;
#define NE(x) ((x) == EMPTY ? 0x7fc07fc0u : (x))
        const unsigned w0 = pk2(o[0] * inv, o[1] * inv), w1 = pk2(o[2] * inv, o[3] * inv), w2 = pk2(o[4] * inv, o[5] * inv), w3 = pk2(o[6] * inv, o[7] * inv), w4_ = pk2(o[8] * inv, o[9] * inv), w5 = pk2(o[10] * inv, o[11] * inv);
        st64(rw, ofrag_off(s, k0 + 4 * hh), (u32x2){NE(w0), NE(w1)});
        st64(rw, ofrag_off(s, k0 + 8 + 4 * hh), (u32x2){NE(w2), NE(w3)});
        if (hh == 0) st64(rw, ofrag_off(s, k0 + 16), (u32x2){NE(w4_), NE(w5)});
#undef NE
        lds_signal(ctl + C_D, lane);
    }
    if (kh == 0) {
        f32x4 cc[5];
#pragma unroll
        for (int j = 0; j < 5; ++j) cc[j] = (f32x4){0.f, 0.f, 0.f, 0.f};
        lds_wait(ctl + C_D, 8u);
        __builtin_amdgcn_s_sleep(OB_DELAY);
        const bool ok = ob_part<10>(lds, ctl, rw, WS_O + (4 * rt64 + w4) * 40 * 1024, lane, cc);
        if (!ok) ctl[C_BAD] = 1u;
        LAS f32x4* red = (LAS f32x4*)((lds_ptr)lds + w4 * 5120) + lane;
#pragma unroll
        for (int j = 0; j < 5; ++j) red[j * 64] = cc[j];
        lds_signal(ctl + C_R + w4, lane);
    }
    asm volatile("s_waitcnt lgkmcnt(0)" ::: "memory");
    PSTAMP(6);
}

__device__ __forceinline__ void a_waves(const Params& P, char* lds, LAS unsigned* ctl, rsrc_t rw, unsigned a, unsigned lane, unsigned b, unsigned li, unsigned rt64, unsigned c4, u64* stamps) {
    const unsigned lds0 = (unsigned)(uintptr_t)lds;
    const int s0 = 64 * rt64;
    const unsigned i16 = lane & 15, g = lane >> 4, q4 = i16 >> 2, p4 = i16 & 3;
    unsigned an = 0; LAS unsigned* ab = ctl + C_AB;
    const unsigned part = c4 & 1, r0 = s0 + 16 * (c4 >> 1);
    {
        if (a == 3) { u64 pc; asm volatile("s_getpc_b64 %0" : "=s"(pc)); const unsigned ln = li * 10 + (lane < 10 ? lane : 0); touch((const char*)(pc & ~127ull) + (ln < CODE_LINES ? ln : 0) * 128, lds0 + DUMMY); }
    }
    { const char* wsrc = (const char*)(part ? P.wv : P.wk); const char* xsrc = (const char*)(P.x + (size_t)r0 * D);
#pragma unroll 1
      for (unsigned i = 0; i < 12; ++i) { unsigned id = a + 4 * i; if (id == 47) id = 43;
        if (id < 25) gldsb(wsrc, id * 1024 + lane * 16, lds0 + P0W + id * 1024);
        else if (id < 45) { const unsigned p = (id - 25) * 64 + lane, row = p / 80, cpos = p - row * 80, ch = (cpos & ~15u) | ((cpos & 15) ^ row);
            gldsb(xsrc, row * 1280 + ch * 16, lds0 + P0X + (id - 25) * 1024); }
        else { const unsigned off = (id - 45) * 1024 + lane * 16; gldsb(P.gw, off < 1280 ? off : 1264, lds0 + P0G + (id - 45) * 1024); } } }
    lds_signal(ctl + C_P0I, lane);
    const u32x2 ep = ld64(rw, WS_EPOCH);
    const u64 tag = ((u64)ep[0] | ((u64)ep[1] << 32)) + 0x9E3779B97F4A7C15ull;
    const u32x2 tagv = {(unsigned)tag, (unsigned)(tag >> 32)};
    {
        const unsigned idx = a * 60 + (lane < 60 ? lane : 0), k16 = idx / 6, sub = idx - 6 * k16;
        touch((const char*)(P.wqkv + (sub >> 1) * 640 + 80 * c4) + (size_t)(((li >> 1) + 16 * k16) * 3840) + (sub & 1) * 156, lds0 + DUMMY); }
    PSTAMP(1);
    VMW(1);
    GBARS(ab, an, 4);
    if (a < 2 || (a == 2 && part == 0)) {
        const lds_cptr p0x = (lds_cptr)lds + P0X + i16 * 1280;
        f32x4 pacc = {0.f, 0.f, 0.f, 0.f};
#define P0XF(ks) lds128(p0x + ((((4 * (ks) + g) & ~15u) | (((4 * (ks) + g) & 15) ^ i16)) * 16))
        const int ks0 = r0 & 255, img = WS_KV + (r0 >> 8) * STG_BYTES, nrm = WS_NRM + (r0 >> 4) * 16;
        if (a == 2) {
            const bf16x8 z = {0, 0, 0, 0, 0, 0, 0, 0};
#pragma unroll
            for (int ks = 0; ks < 20; ++ks) { const bf16x8 gv = lds128((lds_cptr)lds + P0G + (32 * ks + 8 * g) * 2); pacc = MFMA16(i16 == 0 ? gv : z, P0XF(ks), pacc); }
            if (g == 0) st32(rw, WS_G + (r0 + i16) * 4, __float_as_uint(1.f / (1.f + __expf(-pacc[0]))));
        } else {
            const lds_cptr p0w = (lds_cptr)lds + P0W + (8 * g + q4) * 40 + (16 * a + 4 * p4) * 2;
            if (part == 0) {
#pragma unroll
                for (int ks = 0; ks < 20; ++ks) pacc = MFMA16(ldstr2(p0w + ks * 1280, p0w + ks * 1280 + 160), P0XF(ks), pacc);
                const unsigned w0 = pk2(pacc[0], pacc[1]), w1 = pk2(pacc[2], pacc[3]);
                const int key = ks0 + i16, tile = key >> 5, r = swap23(key & 31);
                float n2 = lo2f(w0) * lo2f(w0) + hi2f(w0) * hi2f(w0) + lo2f(w1) * lo2f(w1) + hi2f(w1) * hi2f(w1);
                if (a == 0) { st64(rw, img + STG_KM + tile * 1024 + (g >> 1) * 512 + r * 16 + (g & 1) * 8, (u32x2){w0, w1});
                    n2 = sum16(n2); n2 = sum32(n2); }
                else { if (g == 0) st128(rw, img + STG_KT + (tile >> 1) * 1024 + (tile & 1) * 512 + r * 16, (u32x4){w0, w1, 0x3f80u, 0u}); if (g != 0) n2 = 0.f; }
                n2 = wave_max_pos(n2);
                if (lane == 0) st32(rw, nrm + a * 4, __float_as_uint(n2));
            } else {
#pragma unroll
                for (int ks = 0; ks < 20; ++ks) pacc = MFMA16(P0XF(ks), ldstr2(p0w + ks * 1280, p0w + ks * 1280 + 160), pacc);
                const unsigned w0 = pk2(pacc[0], pacc[1]), w1 = pk2(pacc[2], pacc[3]);
                const int d = 16 * a + i16;
                float vm = fmaxf(fmaxf(fabsf(lo2f(w0)), fabsf(hi2f(w0))), fmaxf(fabsf(lo2f(w1)), fabsf(hi2f(w1))));
                if (d >= 20) vm = 0.f;
                if (d <= 20) st64(rw, img + STG_VT + d * VT_ROW + (ks0 + 4 * g) * 2, d == 20 ? (u32x2){0x3f803f80u, 0x3f803f80u} : (u32x2){w0, w1});
                if (a == 1 && ks0 == 0 && lane < 40) st128(rw, img + STG_VT + 21 * VT_ROW + lane * 16, (u32x4){0u, 0u, 0u, 0u});
                vm = wave_max_pos(vm);
                if (lane == 0) st32(rw, nrm + (2 + a) * 4, __float_as_uint(vm));
            }
        }
#undef P0XF
        VMW(0);
    }
    GBARS(ab, an, 4);
    bool ok = true;
    if (a == 3) { if (lane < 32) st32(rw, WS_FKV + lane * 1024 + b * 4, tagv[0]);
        if (lane == 0) { ctl[C_TAG + 1] = tagv[0]; ctl[C_TAG + 2] = tagv[1]; }
        lds_signal(ctl + C_TAG, lane); }
    PSTAMP(2);
    unsigned aoff[5];
#pragma unroll
    for (unsigned j = 0; j < 5; ++j) { const unsigned id = a + 4 * j;
        if (id < 15) { const unsigned seg = id / 5, p = (id - 5 * seg) * 64 + lane, pos = p / 10, j10 = p - pos * 10, k = pos ^ (4 * ((pos >> 3) & 1)); aoff[j] = k * 3840 + seg * 1280 + j10 * 16; }
        else { const unsigned p = (id - 15) * 64 + lane, row = p >> 2, ch = (p & 3) ^ (3 * ((row >> 2) & 1)); int gr = s0 - 16 + (int)row; gr = gr < 0 ? 0 : gr; aoff[j] = gr * 1280 + ch * 16; } }
    const char* awb = (const char*)(P.wqkv + 80 * c4); const char* axb = (const char*)P.x;
#define A_ISSUE(slot) do { gldsb(awb, aoff[0], lds0 + AA + (slot) * A_SLOT + a * 1024); gldsb(awb, aoff[1], lds0 + AA + (slot) * A_SLOT + (a + 4) * 1024); gldsb(awb, aoff[2], lds0 + AA + (slot) * A_SLOT + (a + 8) * 1024); \
        gldsb(a < 3 ? awb : axb, aoff[3], lds0 + AA + (slot) * A_SLOT + (a + 12) * 1024); gldsb(axb, aoff[4], lds0 + AA + (slot) * A_SLOT + (a + 16) * 1024); awb += 32 * 3840; axb += 64; } while (0)
    lds_wait4<true>(ctl + C_QL, 10u);
    A_ISSUE(0); A_ISSUE(1);
    f32x4 acc[5][4];
#pragma unroll
    for (int i = 0; i < 5; ++i)
#pragma unroll
        for (int j = 0; j < 4; ++j) acc[i][j] = (f32x4){0.f, 0.f, 0.f, 0.f};
    const lds_cptr ax = (lds_cptr)lds + AA + A_X + i16 * 64 + ((g ^ (3 * ((i16 >> 2) & 1))) * 16);
    lds_cptr aw0[4], aw1[4];
#pragma unroll
    for (unsigned j = 0; j < 4; ++j) { unsigned jt = 4 * a + j; jt = jt < 15 ? jt : 14; const unsigned sg = jt / 5, wi = jt - 5 * sg;
        aw0[j] = (lds_cptr)lds + AA + sg * 5120 + wi * 32 + (8 * g + q4 + 4 * (0 ^ (g & 1))) * 160 + p4 * 8; aw1[j] = (lds_cptr)lds + AA + sg * 5120 + wi * 32 + (8 * g + q4 + 4 * (1 ^ (g & 1))) * 160 + p4 * 8; }
    VMW(5); lds_signal(ab, lane); an += 4;
    { int slot = 0;
#pragma unroll 1
      for (int t = 0; t < 20; ++t) {
        lds_wait<true>(ab, an);
        const int so = slot * A_SLOT, fs = slot == 0 ? 2 : slot - 1;
        bf16x8 xf[5], w0 = ldstr2(aw0[0] + so, aw1[0] + so), w1;
#pragma unroll
        for (int i = 0; i < 5; ++i) xf[i] = lds128(ax + so + i * 1024);
        w1 = ldstr2(aw0[1] + so, aw1[1] + so);
        SBAR();
#define A_MF(i, j, w) acc[i][j] = MFMA16(w, xf[i], acc[i][j])
#define A_LD(j) do { if (t + 2 < 20) gldsb((j) < 3 || ((j) == 3 && a < 3) ? awb : axb, aoff[j], lds0 + AA + fs * A_SLOT + (a + 4 * (j)) * 1024); } while (0)
        A_MF(0, 0, w0); A_LD(0); A_MF(1, 0, w0); A_PACE; A_MF(2, 0, w0); A_LD(1); A_MF(3, 0, w0); A_PACE; A_MF(4, 0, w0); SBAR();
        w0 = ldstr2(aw0[2] + so, aw1[2] + so);
        A_MF(0, 1, w1); A_LD(2); A_MF(1, 1, w1); A_PACE; A_MF(2, 1, w1); A_LD(3); A_MF(3, 1, w1); A_PACE; A_MF(4, 1, w1); SBAR();
        w1 = ldstr2(aw0[3] + so, aw1[3] + so);
        A_MF(0, 2, w0); A_LD(4); A_MF(1, 2, w0); A_PACE; A_MF(2, 2, w0); A_PACE; SBAR();
        awb += 32 * 3840; axb += 64;
        if (t < 18) VMW(5); else VMW(0);
        lds_signal(ab, lane); an += 4;
        A_MF(3, 2, w0); A_PACE; A_MF(4, 2, w0); A_PACE;
        A_MF(0, 3, w1); A_PACE; A_MF(1, 3, w1); A_PACE; A_MF(2, 3, w1); A_PACE; A_MF(3, 3, w1); A_PACE; A_MF(4, 3, w1); SBAR();
        slot = slot == 2 ? 0 : slot + 1;
      } }
#undef A_MF
#undef A_LD
    PSTAMP(3);
    lds_wait<true>(ab, an);
    { lds_ptr st = (lds_ptr)lds;
#pragma unroll
      for (unsigned j = 0; j < 4; ++j) { const unsigned jt = 4 * a + j, sg = jt / 5, wi = jt - 5 * sg, cq = 4 * wi + g, hl = (cq * 13) >> 6, dq = cq - 5 * hl;
        if (jt < 15) {
#pragma unroll
            for (int i = 0; i < 5; ++i) { const int row = 16 * i + i16; const f32x4 v = acc[i][j];
                if (sg < 2) *(LAS u32x2*)(st + ST_QK + row * QK_ROW + sg * 192 + hl * 48 + dq * 8) = (u32x2){pk2(v[0], v[1]), pk2(v[2], v[3])};
                else *(LAS f32x4*)(st + ST_V + row * V_ROW + hl * 80 + dq * 16) = v; } } } }
    PSTAMP(9);
    GBARS(ab, an, 4);
    PSTAMP(10);
    {
        const int sl = lane, row = 16 + sl, s = s0 + sl;
        const lds_cptr qp = (lds_cptr)lds + ST_QK + row * QK_ROW + a * 48;
        const lds_cptr kp = qp + 192 - 14 * QK_ROW;
        const u32x4 qa = *(const LAS u32x4*)qp, qb = *(const LAS u32x4*)(qp + 16); const u32x2 qc = *(const LAS u32x2*)(qp + 32);
        u32x4 ka[8], kb[8]; u32x2 kc[8];
#pragma unroll
        for (int w = 0; w < 8; ++w) { ka[w] = *(const LAS u32x4*)(kp + (14 - 2 * w) * QK_ROW); kb[w] = *(const LAS u32x4*)(kp + (14 - 2 * w) * QK_ROW + 16); kc[w] = *(const LAS u32x2*)(kp + (14 - 2 * w) * QK_ROW + 32); }
        SBAR();
        const lds_cptr vp = (lds_cptr)lds + ST_V + (row - 14) * V_ROW + a * 80;
        float lg[8]; float m = -3.0e38f;
#pragma unroll
        for (int w = 0; w < 8; ++w) {
            float x = dot2(qa[0], ka[w][0], 0.f), x2 = dot2(qa[1], ka[w][1], 0.f);
            x = dot2(qa[2], ka[w][2], x); x2 = dot2(qa[3], ka[w][3], x2); x = dot2(qb[0], kb[w][0], x); x2 = dot2(qb[1], kb[w][1], x2);
            x = dot2(qb[2], kb[w][2], x); x2 = dot2(qb[3], kb[w][3], x2); x = dot2(qc[0], kc[w][0], x); x2 = dot2(qc[1], kc[w][1], x2);
            x = (x + x2) * QSCALE;
            lg[w] = (s - 2 * w >= 0) ? x : -1.0e9f * LOG2E; m = fmaxf(m, lg[w]);
        }
        f32x4 va[3][5], vb[3][5];
#define LV(dst, w0, n) do { _Pragma("unroll") for (int w = 0; w < (n); ++w) _Pragma("unroll") for (int j = 0; j < 5; ++j) dst[w][j] = *(const LAS f32x4*)(vp + (14 - 2 * ((w0) + w)) * V_ROW + j * 16); SBAR(); } while (0)
#define PIN5(o) asm volatile("" : "+v"(o[0]), "+v"(o[1]), "+v"(o[2]), "+v"(o[3]), "+v"(o[4]))
        LV(va, 0, 3);
        float l = 0.f;
#pragma unroll
        for (int w = 0; w < 8; ++w) { lg[w] = EXP2(lg[w] - m); l += lg[w]; }
        const float inv = __builtin_amdgcn_rcpf(l);
        LV(vb, 3, 3);
        f32x4 o[5];
#pragma unroll
        for (int j = 0; j < 5; ++j) o[j] = va[0][j] * lg[0] + va[1][j] * lg[1] + va[2][j] * lg[2];
        PIN5(o); SBAR();
        LV(va, 6, 2);
#pragma unroll
        for (int j = 0; j < 5; ++j) o[j] += vb[0][j] * lg[3] + vb[1][j] * lg[4] + vb[2][j] * lg[5];
        PIN5(o); SBAR();
#pragma unroll
        for (int j = 0; j < 5; ++j) o[j] += va[0][j] * lg[6] + va[1][j] * lg[7];
#undef LV
#undef PIN5
        lds_wait(ctl + C_MKD, 1u);
#pragma unroll
        for (int j = 0; j < 5; ++j) { const unsigned w0 = pk2(o[j][0] * inv, o[j][1] * inv), w1 = pk2(o[j][2] * inv, o[j][3] * inv);
            st64(rw, ofrag_off(s, (4 * c4 + a) * DH + 4 * j), (u32x2){w0 == EMPTY ? 0x7fc07fc0u : w0, w1 == EMPTY ? 0x7fc07fc0u : w1}); }
        PSTAMP(11);
    }
    GBARS(ab, an, 4);

    PSTAMP(4);
    unsigned coff[5];
#pragma unroll
    for (unsigned i = 0; i < 5; ++i) { const unsigned p = 256 * i + a * 64 + lane, pos = p / 10, j10 = p - pos * 10; coff[i] = (pos ^ (4 * ((pos >> 3) & 1))) * 1280 + j10 * 16; }
    auto c_issue = [&](const u16* W, unsigned chunk, unsigned dst) {
        const char* sb = (const char*)(W + 80 * c4) + (size_t)chunk * (128 * 1280);
#pragma unroll
        for (unsigned i = 0; i < 5; ++i) gldsb(sb, coff[i], lds0 + dst + (4 * i + a) * 1024);
    };
    ok &= poll_tags<1>(rw, WS_FA + (rt64 * 8 + (lane & 7)) * 8, lane < 8, tag);
    c_issue(P.woA, 0, AA); c_issue(P.woA, 1, AA + A_SLOT); c_issue(P.woA, 2, AA + 2 * A_SLOT);
    const int fo = WS_O + (4 * rt64 + a) * 40 * 1024, fl = lane * 16;
    bf16x8 of[20];
#pragma unroll
    for (int ks = 0; ks < 20; ++ks) of[ks] = __builtin_bit_cast(bf16x8, ld128s(rw, fl, fo + ks * 1024));
    const float gg = __uint_as_float(ld32(rw, WS_G + (s0 + 16 * a + i16) * 4));
    const unsigned cbo = (8 * g + q4) * 160 + p4 * 8, ct0 = 4 * (0 ^ (g & 1)) * 160, ct1 = 4 * (1 ^ (g & 1)) * 160;
    const lds_cptr cw0 = (lds_cptr)lds + cbo + ct0, cw1 = (lds_cptr)lds + cbo + ct1;
    f32x4 cc[5]; bf16x8 wA[5], wB[5];
#define C_READY(F, n0, N) do { \
        bool got = false; \
        _Pragma("unroll 1") for (unsigned spins = 0; spins < SPIN_CAP; ++spins) { \
            unsigned mx = 0; \
            _Pragma("unroll") for (int u = 0; u < (N); ++u) { const u32x4 v = __builtin_bit_cast(u32x4, of[(n0) + u]); mx = max(max(mx, v[0]), max(max(v[1], v[2]), v[3])); } \
            if (!__any(mx == EMPTY)) { got = true; break; } \
            __builtin_amdgcn_s_sleep(2); \
            _Pragma("unroll") for (int u = 0; u < (N); ++u) of[(n0) + u] = __builtin_bit_cast(bf16x8, ld128s(rw, fl, fo + ((F) + (n0) + u) * 1024)); \
        } \
        ok &= got; } while (0)
#define C_RD(W, off) do { lds_cptr b0_ = cw0 + (off), b1_ = cw1 + (off); asm volatile("" : "+v"(b0_), "+v"(b1_)); _Pragma("unroll") for (int j = 0; j < 5; ++j) W[j] = ldstr2(b0_ + j * 32, b1_ + j * 32); SBAR(); } while (0)
#define C_MF(W, ks) do { _Pragma("unroll") for (int j = 0; j < 5; ++j) cc[j] = MFMA16(W[j], of[ks], cc[j]); asm volatile("" : "+v"(cc[0]), "+v"(cc[1]), "+v"(cc[2]), "+v"(cc[3]), "+v"(cc[4])); SBAR(); } while (0)
#define C_CHUNK(base, ks0, next) do { C_RD(wB, (base) + 5120); C_MF(wA, (ks0)); C_RD(wA, (base) + 10240); C_MF(wB, (ks0) + 1); C_RD(wB, (base) + 15360); C_MF(wA, (ks0) + 2); \
        if ((next) >= 0) C_RD(wA, (next) >= 0 ? (next) : 0); C_MF(wB, (ks0) + 3); } while (0)
#pragma unroll
    for (int j = 0; j < 5; ++j) cc[j] = (f32x4){0.f, 0.f, 0.f, 0.f};
    C_READY(0, 0, 10); C_READY(0, 10, 10);
    VMW(0); GBARS(ab, an, 4);
    C_RD(wA, AA); C_CHUNK(AA, 0, AA + A_SLOT); C_CHUNK(AA + A_SLOT, 4, AA + 2 * A_SLOT);
    GBARS(ab, an, 4);
    c_issue(P.woA, 3, AA); c_issue(P.woA, 4, AA + A_SLOT);
    C_CHUNK(AA + 2 * A_SLOT, 8, -1);
    VMW(0); GBARS(ab, an, 4);
    c_issue(P.woB, 0, AA + 2 * A_SLOT);
    C_RD(wA, AA); C_CHUNK(AA, 12, AA + A_SLOT); C_CHUNK(AA + A_SLOT, 16, -1);
    u32x2 cA[5];
#pragma unroll
    for (int j = 0; j < 5; ++j) { cA[j] = (u32x2){pk2(cc[j][0], cc[j][1]), pk2(cc[j][2], cc[j][3])}; cc[j] = (f32x4){0.f, 0.f, 0.f, 0.f}; }
    LAS u32x2* park = (LAS u32x2*)((lds_ptr)lds + 28672 + a * 2560) + lane;
    GBARS(ab, an, 4);
    c_issue(P.woB, 1, AA); c_issue(P.woB, 2, AA + A_SLOT);
    VMW(0); lds_signal(ctl + C_WB, lane);
    PSTAMP(5);
    lds_wait(ctl + C_BD, 8u);
    c_issue(P.woB, 3, C_S3); c_issue(P.woB, 4, C_S4);
    lds_wait(ctl + C_D, 8u);
#pragma unroll
    for (int j = 0; j < 5; ++j) park[j * 64] = cA[j];
    __builtin_amdgcn_s_sleep(OB_DELAY);
    PSTAMP(6);
    ok &= ob_part<0>(lds, ctl, rw, fo, lane, cc);
    PSTAMP(7);
#undef C_READY
#undef C_RD
#undef C_MF
#undef C_CHUNK
    if (!ok) ctl[C_BAD] = 1u;
    lds_wait<false>(ctl + C_R + a, 1u);
    { LAS f32x4* red = (LAS f32x4*)((lds_ptr)lds + a * 5120) + lane;
#pragma unroll
      for (int j = 0; j < 5; ++j) cc[j] += red[j * 64]; }
    const float poison = (rfl(ctl[C_BAD]) || !ok) ? __builtin_nanf("") : 0.f;
    unsigned ln = __builtin_amdgcn_mbcnt_hi(~0u, __builtin_amdgcn_mbcnt_lo(~0u, 0u)); asm volatile("" : "+v"(ln));
#pragma unroll
    for (int j = 0; j < 5; ++j) { const u32x2 pa = *((LAS u32x2*)((lds_ptr)lds + 28672 + a * 2560) + ln + j * 64); const f32x4 av = {lo2f(pa[0]), hi2f(pa[0]), lo2f(pa[1]), hi2f(pa[1])}; const f32x4 v = av * gg + cc[j] * (1.f - gg) + poison;
        *(u32x2*)(P.out + (size_t)(s0 + 16 * a + (ln & 15)) * 640 + 80 * c4 + 16 * j + 4 * (ln >> 4)) = (u32x2){pk2(v[0], v[1]), pk2(v[2], v[3])}; }
    PSTAMP(8);
}

__global__ __launch_bounds__(768, 3) void fused(Params P) {
    extern __shared__ __attribute__((aligned(16))) char lds[];
    PDECL
    PSTAMP(0);
    char* const ws = P.ws;
    const unsigned tid = threadIdx.x, wid = rfl(tid >> 6);
    const unsigned lane = __builtin_amdgcn_mbcnt_hi(~0u, __builtin_amdgcn_mbcnt_lo(~0u, 0u));
    const unsigned b = blockIdx.x & 255, xg = b & 7, li = b >> 3;
    const unsigned rt64 = (xg >> 2) * 16 + (li >> 1), c4 = (xg & 3) * 2 + (li & 1);
    const rsrc_t rw = __builtin_amdgcn_make_buffer_rsrc(ws, 0, WS_END, 0x00020000);
    LAS unsigned* ctl = (LAS unsigned*)((lds_ptr)lds + CTL);
    if (tid < 64) ctl[tid] = 0u;
    LBAR();
    u64* stp = nullptr;
    if (wid < 8) b_waves(P, lds, ctl, rw, wid & 7, lane, b, li, rt64, c4, stp);
    else { if (APRIO) __builtin_amdgcn_s_setprio(APRIO); a_waves(P, lds, ctl, rw, wid & 3, lane, b, li, rt64, c4, stp); }
    PFLUSH();
}

static inline Params mk_params(void* const* d_in, void* d_out, void* d_ws) {
    Params P; P.x = (const u16*)d_in[0]; P.wqkv = (const u16*)d_in[1]; P.woA = (const u16*)d_in[2]; P.wq = (const u16*)d_in[4]; P.wk = (const u16*)d_in[5]; P.wv = (const u16*)d_in[6];
    P.woB = (const u16*)d_in[7]; P.gw = (const u16*)d_in[8]; P.ws = (char*)d_ws; P.out = (u16*)d_out; return P; }
extern "C" void kernel_launch(void* const* d_in, const int* in_sizes, int n_in, void* d_out, int out_size, void* d_ws, size_t ws_size, hipStream_t stream) {
    (void)hipFuncSetAttribute((const void*)fused, hipFuncAttributeMaxDynamicSharedMemorySize, LDS_TOTAL);
    fused<<<256, 768, LDS_TOTAL, stream>>>(mk_params(d_in, d_out, d_ws));
}
#define ST_LAUNCH(d_in, d_out, ws, stream) fused<<<256, 768, LDS_TOTAL, stream>>>(mk_params(d_in, d_out, ws))
```

```cpp
#include <hip/hip_runtime.h>
#include <stdint.h>
#define A_START 8u
#define POLL_FIRST 20
#define SWH 6
#define GRP_PIN SBAR()
#define OB_DELAY 6
#define POLL_SLEEP 4
#define POLL_DELAY lds_wait4<true>(ctl + C_QL, 8u)
#define A_PACE (void)0
#define APRIO 2

typedef unsigned short u16;
typedef unsigned long long u64;
typedef short bf16x8 __attribute__((ext_vector_type(8)));
typedef short s16x4 __attribute__((ext_vector_type(4)));
typedef float f32x16 __attribute__((ext_vector_type(16)));
typedef float f32x4 __attribute__((ext_vector_type(4)));
typedef float f32x2 __attribute__((ext_vector_type(2)));
typedef unsigned u32x4 __attribute__((ext_vector_type(4)));
typedef unsigned u32x2 __attribute__((ext_vector_type(2)));
typedef __bf16 bf2_t __attribute__((ext_vector_type(2)));
#define LAS __attribute__((address_space(3)))
#define GAS __attribute__((address_space(1)))
typedef LAS const char* lds_cptr;
typedef LAS char* lds_ptr;
typedef __amdgpu_buffer_rsrc_t rsrc_t;

__device__ __forceinline__ float bf2f(u16 b) { return __uint_as_float(((unsigned)b) << 16); }
__device__ __forceinline__ unsigned pk2(float lo, float hi) { f32x2 v = {lo, hi}; bf2_t r = __builtin_convertvector(v, bf2_t); return __builtin_bit_cast(unsigned, r); }
__device__ __forceinline__ u16 f2bf(float f) { return (u16)(pk2(f, 0.f) & 0xffffu); }
__device__ __forceinline__ float lo2f(unsigned u) { return __uint_as_float(u << 16); }
__device__ __forceinline__ float hi2f(unsigned u) { return __uint_as_float(u & 0xffff0000u); }
__device__ __forceinline__ float dot2(unsigned a, unsigned b, float c) { return __builtin_amdgcn_fdot2_f32_bf16(__builtin_bit_cast(bf2_t, a), __builtin_bit_cast(bf2_t, b), c, false); }

constexpr int S = 2048, D = 640, H = 32, DH = 20;
constexpr float SCALE = 0.22360679774997896f, LOG2E = 1.4426950408889634f;
constexpr float QSCALE = SCALE * LOG2E;

constexpr int STG_KM = 0, STG_KT = 8192, STG_VT = 12288, VT_ROW = 528, STG_BYTES = 24576;
constexpr int WS_FKV_AT = 262144;
constexpr int WS_EPOCH = 0;
constexpr int WS_FKV = WS_FKV_AT;
constexpr int WS_FO = 256;
constexpr int WS_FA = WS_FO + 2048;
constexpr int WS_NRM = 8192;
constexpr int WS_G = WS_NRM + 2048;
constexpr int WS_KV = 32768;
static_assert(WS_KV + 8 * STG_BYTES <= WS_FKV_AT, "map");
constexpr int WS_O = WS_FKV_AT + 32768;
constexpr int WS_END = WS_O + S * 1280 * 2;
constexpr int AUX_SC1 = 16;

#define MFMA32(a, b, c) __builtin_amdgcn_mfma_f32_32x32x16_bf16((a), (b), (c), 0, 0, 0)
#define MFMA16(a, b, c) __builtin_amdgcn_mfma_f32_16x16x32_bf16((a), (b), (c), 0, 0, 0)
#define SBAR() __builtin_amdgcn_sched_barrier(0)
#define EXP2(x) __builtin_amdgcn_exp2f(x)
#define WAIT_BAR(N) asm volatile("s_waitcnt vmcnt(" #N ")\n\ts_barrier" ::: "memory")
#define LBAR() asm volatile("s_waitcnt lgkmcnt(0)\n\ts_barrier" ::: "memory")
#define FULLBAR() asm volatile("s_waitcnt vmcnt(0) lgkmcnt(0)\n\ts_barrier" ::: "memory")
__device__ __forceinline__ float wave_max_pos(float f) {
    unsigned x = __float_as_uint(f);
    x = max(x, (unsigned)__builtin_amdgcn_update_dpp(0, (int)x, 0x111, 0xf, 0xf, false));
    x = max(x, (unsigned)__builtin_amdgcn_update_dpp(0, (int)x, 0x112, 0xf, 0xf, false));
    x = max(x, (unsigned)__builtin_amdgcn_update_dpp(0, (int)x, 0x114, 0xf, 0xf, false));
    x = max(x, (unsigned)__builtin_amdgcn_update_dpp(0, (int)x, 0x118, 0xf, 0xf, false));
    x = max(x, (unsigned)__builtin_amdgcn_update_dpp(0, (int)x, 0x142, 0xa, 0xf, false));
    x = max(x, (unsigned)__builtin_amdgcn_update_dpp(0, (int)x, 0x143, 0xc, 0xf, false));
    return __uint_as_float((unsigned)__builtin_amdgcn_readlane((int)x, 63));
}
__device__ __forceinline__ float sum16(float f) { auto r = __builtin_amdgcn_permlane16_swap(__float_as_uint(f), __float_as_uint(f), false, false); return __uint_as_float(r[0]) + __uint_as_float(r[1]); }
__device__ __forceinline__ float sum32(float f) { auto r = __builtin_amdgcn_permlane32_swap(__float_as_uint(f), __float_as_uint(f), false, false); return __uint_as_float(r[0]) + __uint_as_float(r[1]); }
__device__ __forceinline__ float max32(float f) { auto r = __builtin_amdgcn_permlane32_swap(__float_as_uint(f), __float_as_uint(f), false, false); return fmaxf(__uint_as_float(r[0]), __uint_as_float(r[1])); }
__device__ __forceinline__ int swap23(int x) { return (x & ~12) | ((x & 4) << 1) | ((x & 8) >> 1); }
__device__ __forceinline__ void glds16(const void* g, unsigned lds_base) {
    unsigned sv; asm volatile("s_mov_b32 %0, m0\n\ts_mov_b32 m0, %2\n\ts_nop 0\n\tglobal_load_lds_dwordx4 %1, off\n\ts_mov_b32 m0, %0" : "=&s"(sv) : "v"(g), "s"(lds_base) : "memory"); }
__device__ __forceinline__ void gldsb(const void* sbase, unsigned voff, unsigned lds_base) {
    unsigned sv; asm volatile("s_mov_b32 %0, m0\n\ts_mov_b32 m0, %3\n\ts_nop 0\n\tglobal_load_lds_dwordx4 %1, %2\n\ts_mov_b32 m0, %0" : "=&s"(sv) : "v"(voff), "s"(sbase), "s"(lds_base) : "memory"); }
__device__ __forceinline__ void gldsb_sc1(const void* sbase, unsigned voff, unsigned lds_base) {
    unsigned sv; asm volatile("s_mov_b32 %0, m0\n\ts_mov_b32 m0, %3\n\ts_nop 0\n\tglobal_load_lds_dwordx4 %1, %2 sc1\n\ts_mov_b32 m0, %0" : "=&s"(sv) : "v"(voff), "s"(sbase), "s"(lds_base) : "memory"); }
__device__ __forceinline__ bf16x8 lds128(lds_cptr p) { return *(const LAS bf16x8*)p; }
typedef short v4i16_t __attribute__((ext_vector_type(4)));
__device__ __forceinline__ s16x4 ldstr(lds_cptr p) { return __builtin_bit_cast(s16x4, __builtin_amdgcn_ds_read_tr16_b64_v4i16((LAS v4i16_t*)p)); }
__device__ __forceinline__ bf16x8 ldstr2(lds_cptr p0, lds_cptr p1) { const s16x4 a = ldstr(p0), b = ldstr(p1); return __builtin_shufflevector(a, b, 0, 1, 2, 3, 4, 5, 6, 7); }
__device__ __forceinline__ unsigned rfl(unsigned x) { return (unsigned)__builtin_amdgcn_readfirstlane(x); }
__device__ __forceinline__ void st32(rsrc_t r, int off, unsigned v) { __builtin_amdgcn_raw_buffer_store_b32(v, r, off, 0, AUX_SC1); }
__device__ __forceinline__ void st64(rsrc_t r, int off, u32x2 v) { __builtin_amdgcn_raw_buffer_store_b64(v, r, off, 0, AUX_SC1); }
__device__ __forceinline__ void st128(rsrc_t r, int off, u32x4 v) { __builtin_amdgcn_raw_buffer_store_b128(v, r, off, 0, AUX_SC1); }
__device__ __forceinline__ unsigned ld32(rsrc_t r, int off) { return __builtin_amdgcn_raw_buffer_load_b32(r, off, 0, AUX_SC1); }
__device__ __forceinline__ u32x2 ld64(rsrc_t r, int off) { return __builtin_amdgcn_raw_buffer_load_b64(r, off, 0, AUX_SC1); }
__device__ __forceinline__ u32x4 ld128(rsrc_t r, int off) { return __builtin_amdgcn_raw_buffer_load_b128(r, off, 0, AUX_SC1); }
__device__ __forceinline__ u32x4 ld128s(rsrc_t r, int voff, int soff) { return __builtin_amdgcn_raw_buffer_load_b128(r, voff, soff, AUX_SC1); }

#define PSTAMP(i)
#define PDECL
#define PFLUSH()

#define QT(i)
#define QFLUSH()
struct Params { const u16 *x, *wqkv, *woA, *wq, *wk, *wv, *woB, *gw; char* ws; u16* out; };

constexpr int LDS_TOTAL = 163840, CTL = LDS_TOTAL - 256, DUMMY = CTL - 256;
constexpr int KVB = 2 * STG_BYTES;
constexpr int Q_SLOT = 18432, Q_W = 8192, Q_ST = 73728, QB_ROW = 208;
constexpr int AA = 98304;
constexpr int P0W = AA, P0X = AA + 25664, P0G = P0X + 20480;
constexpr int A_SLOT = 20480, A_X = 15360;
constexpr int ST_QK = AA, ST_V = AA + 32000, QK_ROW = 400, V_ROW = 336;
constexpr int C_S3 = 49152, C_S4 = 69632;
constexpr int C_FALL = 0, C_BB = 1, C_DONE = 2, C_LAND = 4, C_X = 6, C_BD = 7, C_D = 12, C_AB = 13, C_KVOK = 16, C_BAD = 17, C_P0I = 18, C_QL = 20, C_QD = 24, C_TAG = 28, C_QS = 19, C_MKD = 15, C_P = 32, C_WB = 40, C_NRM = 42, C_R = 44;
constexpr unsigned CODE_LINES = 296;
constexpr unsigned EMPTY = 0xFFFFFFFFu;
constexpr unsigned SPIN_CAP = 1u << 18;

template <int N, int SLEEP = 4>
__device__ __forceinline__ bool poll_tags(rsrc_t r, int off, bool active, u64 tag) {
#pragma unroll 1
    for (unsigned spins = 0; spins < SPIN_CAP; ++spins) {
        bool ok = true;
        if (active) {
#pragma unroll
            for (int i = 0; i < N; ++i) { const u32x2 v = ld64(r, off + 8 * i); ok &= (v[0] == (unsigned)tag) & (v[1] == (unsigned)(tag >> 32)); }
        }
        if (__all(ok)) return true;
        __builtin_amdgcn_s_sleep(SLEEP);
    }
    return false;
}
__device__ __forceinline__ void lds_signal(LAS unsigned* cnt, int lane) {
    (void)lane;
    __builtin_amdgcn_sched_barrier(0);
    asm volatile("s_mov_b64 exec, 1\n\tds_add_u32 %0, %1\n\ts_mov_b64 exec, -1\n\ts_nop 3" :: "v"((unsigned)(uintptr_t)cnt), "v"(1u) : "memory");
    __builtin_amdgcn_sched_barrier(0); }
template <bool SLEEP = true>
__device__ __forceinline__ void lds_wait(LAS unsigned* cnt, unsigned n) {
    unsigned v, sv, left;
    if (SLEEP)
        asm volatile("s_mov_b32 %2, 0x100000\n1:\n\tds_read_b32 %0, %3\n\ts_waitcnt lgkmcnt(0)\n\tv_readfirstlane_b32 %1, %0\n\ts_cmp_ge_u32 %1, %4\n\ts_cbranch_scc1 2f\n\ts_sleep 1\n\ts_sub_u32 %2, %2, 1\n\ts_cbranch_scc0 1b\n2:"
                     : "=&v"(v), "=&s"(sv), "=&s"(left) : "v"((unsigned)(uintptr_t)cnt), "s"(n) : "memory", "scc");
    else
        asm volatile("s_mov_b32 %2, 0x100000\n1:\n\tds_read_b32 %0, %3\n\ts_waitcnt lgkmcnt(0)\n\tv_readfirstlane_b32 %1, %0\n\ts_cmp_ge_u32 %1, %4\n\ts_cbranch_scc1 2f\n\ts_sub_u32 %2, %2, 1\n\ts_cbranch_scc0 1b\n2:"
                     : "=&v"(v), "=&s"(sv), "=&s"(left) : "v"((unsigned)(uintptr_t)cnt), "s"(n) : "memory", "scc");
}
template <bool SLEEP = false>
__device__ __forceinline__ void lds_wait4(LAS unsigned* cnt, unsigned n) {
#pragma unroll 1
    for (unsigned spins = 0; spins < (1u << 20); ++spins) {
        const u32x4 v = *(volatile LAS u32x4*)cnt;
        const unsigned lo = min(min(v[0], v[1]), min(v[2], v[3]));
        if (rfl(lo) >= n) break;
        if (SLEEP) __builtin_amdgcn_s_sleep(1);
    }
    asm volatile("" ::: "memory");
}
#define VMW(N) asm volatile("s_waitcnt vmcnt(" #N ")" ::: "memory")
#define GBAR(cnt, n, step) do { lds_signal(cnt, lane); n += (step); lds_wait<false>(cnt, n); } while (0)
#define GBARS(cnt, n, step) do { lds_signal(cnt, lane); n += (step); lds_wait<true>(cnt, n); } while (0)

#define KV_GLDS gldsb_sc1
__device__ __forceinline__ void att_stage_load(const char* kv, int st, unsigned ldsbuf, int w4, int lane) {
    const char* src = kv + (size_t)st * STG_BYTES + w4 * 1024;
#pragma unroll
    for (int i = 0; i < 6; ++i) KV_GLDS(src + i * 4096, lane * 16, ldsbuf + (w4 + 4 * i) * 1024);
}

__device__ __forceinline__ void att_maxpass(const char* kv, char* ldsh, LAS unsigned* bb, unsigned& bn, int kh, int w4, int lane, const bf16x8 (&qm)[2], const bf16x8 (&qt)[2][2], float (&mx)[2]) {
    const unsigned lds0 = (unsigned)(uintptr_t)ldsh;
    const lds_cptr kbase = (lds_cptr)ldsh + lane * 16;
    const f32x16 zero = {};
#pragma unroll 1
    for (int rnd = 0; rnd < 3; ++rnd) {
        if (rnd) { GBAR(bb, bn, 8); att_stage_load(kv, 4 * kh + 2 * (rnd & 1), lds0, w4, lane); att_stage_load(kv, 4 * kh + 2 * (rnd & 1) + 1, lds0 + KVB, w4, lane); }
        VMW(0); GBAR(bb, bn, 8);
        if (rnd < 2) {
#pragma unroll 1
            for (int pr = 0; pr < 8; ++pr) {
                const lds_cptr kb = kbase + (pr >> 2) * KVB; const int p = pr & 3;
                const bf16x8 kt = lds128(kb + STG_KT + p * 1024), k0 = lds128(kb + STG_KM + (2 * p) * 1024), k1 = lds128(kb + STG_KM + (2 * p + 1) * 1024);
#pragma unroll
                for (int h = 0; h < 2; ++h) {
                    f32x16 s0 = MFMA32(k0, qm[h], zero); s0 = MFMA32(kt, qt[h][0], s0);
                    f32x16 s1 = MFMA32(k1, qm[h], zero); s1 = MFMA32(kt, qt[h][1], s1);
#pragma unroll
                    for (int i = 0; i < 16; ++i) mx[h] = fmaxf(mx[h], fmaxf(s0[i], s1[i]));
                }
            }
        }
    }
}

__device__ __forceinline__ void att_main(const char* kv, char* ldsh, LAS unsigned* ctl, rsrc_t rw, int mark_off, int kbase0, int fm_off, int kh, int w4, int lane, const bf16x8 (&qm)[2], const bf16x8 (&qt)[2][2], f32x16& O0, f32x16& O1) {
    const unsigned lds0 = (unsigned)(uintptr_t)ldsh;
    LAS unsigned* cdone = ctl + C_DONE + kh; LAS unsigned* cland = ctl + C_LAND + kh;
    const int r = lane & 31, hh = lane >> 5;
    const int voff = r <= 20 ? r * VT_ROW : (r < 28 ? 21 * VT_ROW : 21 * VT_ROW + 112);
    const lds_cptr kbase = (lds_cptr)ldsh + lane * 16;
    const lds_cptr vbase = (lds_cptr)ldsh + STG_VT + voff + hh * 16;
    const f32x16 zero = {};
    f32x16 sA, sB; u32x4 pA0, pA1, pB0, pB1; bf16x8 kmA, kmB, ktA, ktB, vA0, vA1, vB0, vB1;
    ktA = lds128(kbase + STG_KT); kmA = lds128(kbase + STG_KM);
    sA = MFMA32(kmA, qm[0], zero); sA = MFMA32(ktA, qt[0][0], sA);
    pB0 = (u32x4){0u, 0u, 0u, 0u}; pB1 = pB0; vB0 = kmA; vB1 = kmA; ktB = ktA; vA0 = kmA; vA1 = kmA; kmB = kmA;
    float ee0 = 0.f, ee1 = 0.f, ee2 = 0.f, ee3 = 0.f;
#define GRP(MF, LD, SC, I, PD, J) do { MF; GRP_PIN; LD; const float e0_ = EXP2(SC[I]); unsigned a_ = pk2(ee0, ee1); const float e1_ = EXP2(SC[I + 1]); unsigned b_ = pk2(ee2, ee3); const float e2_ = EXP2(SC[I + 2]), e3_ = EXP2(SC[I + 3]); \
        ee0 = e0_; ee1 = e1_; ee2 = e2_; ee3 = e3_; asm volatile("" : "+v"(a_), "+v"(b_), "+v"(ee0), "+v"(ee1), "+v"(ee2), "+v"(ee3)); PD[J] = a_; PD[J + 1] = b_; SBAR(); } while (0)
#define TILE(KM, KMN, KT, VJ, KTN, VJN, VC0, VC1, VP0, VP1, VADDR, LDK, LDT) do { \
        GRP(sB = MFMA32(KM, qm[1], zero), VC0 = lds128(VADDR), sA, 0, pB1, 2); \
        GRP(O1 = MFMA32(VP0, __builtin_bit_cast(bf16x8, pB0), O1), VC1 = lds128((VADDR) + 32), sA, 4, pA0, 0); \
        GRP(sB = MFMA32(KT, qt[1][VJ], sB), LDK, sA, 8, pA0, 2); \
        GRP(O1 = MFMA32(VP1, __builtin_bit_cast(bf16x8, pB1), O1), LDT, sA, 12, pA1, 0); \
        GRP(sA = MFMA32(KMN, qm[0], zero), (void)0, sB, 0, pA1, 2); \
        GRP(O0 = MFMA32(VC0, __builtin_bit_cast(bf16x8, pA0), O0), (void)0, sB, 4, pB0, 0); \
        GRP(sA = MFMA32(KTN, qt[0][VJN], sA), (void)0, sB, 8, pB0, 2); \
        GRP(O0 = MFMA32(VC1, __builtin_bit_cast(bf16x8, pA1), O0), (void)0, sB, 12, pB1, 0); } while (0)
#pragma unroll 1
    for (int st = 0; st < 4; ++st) {
        if (kh == 1) { if (st == 0) __builtin_amdgcn_s_setprio(1); if (2 * st == SWH) __builtin_amdgcn_s_setprio(0); }
        const int buf = st & 1;
        const lds_cptr kb = kbase + buf * KVB, vb = vbase + buf * KVB, kn = kbase + (buf ^ 1) * KVB;
        TILE(kmA, kmB, ktA, 0, ktA, 1, vA0, vA1, vB0, vB1, vb + 0 * 64, kmB = lds128(kb + STG_KM + 1 * 1024), (void)0);
        if (st) { lds_signal(st == 3 ? ctl + C_X : cdone, lane); SBAR(); }
        TILE(kmB, kmA, ktA, 1, ktB, 0, vB0, vB1, vA0, vA1, vb + 1 * 64, kmA = lds128(kb + STG_KM + 2 * 1024), ktB = lds128(kb + STG_KT + 1024));
        if (st == 0 && kh == 0) {
#pragma unroll 1
            for (int pc = w4; pc < 20; pc += 4) { const int k = kbase0 + 8 * pc + (pc < 10 ? 0 : 560); st128(rw, mark_off + ((k >> 5) * 64 + ((k >> 3) & 3) * 16) * 16, (u32x4){EMPTY, EMPTY, EMPTY, EMPTY}); }
            SBAR(); }
        if (st == 1 || st == 2) { lds_wait(cdone, 4u * st); att_stage_load(kv, 4 * kh + st + 1, lds0 + (buf ^ 1) * KVB, w4, lane); SBAR(); }
        TILE(kmA, kmB, ktB, 0, ktB, 1, vA0, vA1, vB0, vB1, vb + 2 * 64, kmB = lds128(kb + STG_KM + 3 * 1024), (void)0);
        TILE(kmB, kmA, ktB, 1, ktA, 0, vB0, vB1, vA0, vA1, vb + 3 * 64, kmA = lds128(kb + STG_KM + 4 * 1024), ktA = lds128(kb + STG_KT + 2048));
        if (kh == 1 && 2 * st + 1 == SWH) __builtin_amdgcn_s_setprio(0);
        TILE(kmA, kmB, ktA, 0, ktA, 1, vA0, vA1, vB0, vB1, vb + 4 * 64, kmB = lds128(kb + STG_KM + 5 * 1024), (void)0);
        TILE(kmB, kmA, ktA, 1, ktB, 0, vB0, vB1, vA0, vA1, vb + 5 * 64, kmA = lds128(kb + STG_KM + 6 * 1024), ktB = lds128(kb + STG_KT + 3072));
        if (st < 3) { VMW(0); lds_signal(cland, lane); SBAR(); }
        TILE(kmA, kmB, ktB, 0, ktB, 1, vA0, vA1, vB0, vB1, vb + 6 * 64, kmB = lds128(kb + STG_KM + 7 * 1024), (void)0);
        if (st < 3) { lds_wait(cland, 4u * (st + 1));
            if (st == 0 && kh == 0 && w4 == 0) { if (lane == 0) st64(rw, fm_off, (u32x2){ctl[C_TAG + 1], ctl[C_TAG + 2]}); lds_signal(ctl + C_MKD, lane); }
            SBAR(); }
        TILE(kmB, kmA, ktB, 1, ktA, 0, vB0, vB1, vA0, vA1, vb + 7 * 64, kmA = lds128(kn + STG_KM + 0 * 1024), ktA = lds128(kn + STG_KT));
    }
    pB1[2] = pk2(ee0, ee1); pB1[3] = pk2(ee2, ee3);
    O1 = MFMA32(vB0, __builtin_bit_cast(bf16x8, pB0), O1);
    O1 = MFMA32(vB1, __builtin_bit_cast(bf16x8, pB1), O1);
#undef GRP
#undef TILE
}

__device__ __forceinline__ int ofrag_off(int s, int k) { return WS_O + ((((s >> 4) * 40 + (k >> 5)) * 64 + ((k >> 3) & 3) * 16 + (s & 15)) * 16 + (k & 7) * 2); }
__device__ __forceinline__ void touch(const void* g, unsigned lds_dummy) {
    unsigned sv; asm volatile("s_mov_b32 %0, m0\n\ts_mov_b32 m0, %2\n\ts_nop 0\n\tglobal_load_lds_dword %1, off\n\ts_mov_b32 m0, %0" : "=&s"(sv) : "v"(g), "s"(lds_dummy) : "memory"); }

__device__ __forceinline__ constexpr int wb_at(int ks) { return ((ks >> 2) == 0 ? AA + 2 * A_SLOT : (ks >> 2) == 1 ? AA : (ks >> 2) == 2 ? AA + A_SLOT : (ks >> 2) == 3 ? C_S3 : C_S4) + (ks & 3) * 5120; }
template <int KS0>
__device__ __forceinline__ bool ob_part(char* lds, LAS unsigned* ctl, rsrc_t rw, int fo, unsigned lane, f32x4 (&cc)[5]) {
    const unsigned i16 = lane & 15, g = lane >> 4, q4 = i16 >> 2, p4 = i16 & 3;
    const lds_cptr cw0 = (lds_cptr)lds + (8 * g + q4 + 4 * (0 ^ (g & 1))) * 160 + p4 * 8, cw1 = (lds_cptr)lds + (8 * g + q4 + 4 * (1 ^ (g & 1))) * 160 + p4 * 8;
    const int fl = lane * 16;
    bf16x8 of[10], wA[5], wB[5]; bool ok = true;
#pragma unroll
    for (int u = 0; u < 10; ++u) of[u] = __builtin_bit_cast(bf16x8, ld128s(rw, fl, fo + (20 + KS0 + u) * 1024));
    lds_wait(ctl + C_WB, 4u);
    if (KS0) lds_wait(ctl + C_WB + 1, 4u);
#define O_READY(n0, N) do { bool got = false; \
        _Pragma("unroll 1") for (unsigned spins = 0; spins < SPIN_CAP; ++spins) { unsigned mx = 0; \
            _Pragma("unroll") for (int u = 0; u < (N); ++u) { const u32x4 v = __builtin_bit_cast(u32x4, of[(n0) + u]); mx = max(max(mx, v[0]), max(max(v[1], v[2]), v[3])); } \
            if (!__any(mx == EMPTY)) { got = true; break; } \
            __builtin_amdgcn_s_sleep(2); \
            _Pragma("unroll") for (int u = 0; u < (N); ++u) of[(n0) + u] = __builtin_bit_cast(bf16x8, ld128s(rw, fl, fo + (20 + KS0 + (n0) + u) * 1024)); } \
        ok &= got; } while (0)
#define O_RD(W, u) do { lds_cptr b0_ = cw0 + wb_at(KS0 + (u)), b1_ = cw1 + wb_at(KS0 + (u)); asm volatile("" : "+v"(b0_), "+v"(b1_)); _Pragma("unroll") for (int j = 0; j < 5; ++j) W[j] = ldstr2(b0_ + j * 32, b1_ + j * 32); SBAR(); } while (0)
#define O_MF(W, u) do { _Pragma("unroll") for (int j = 0; j < 5; ++j) cc[j] = MFMA16(W[j], of[u], cc[j]); asm volatile("" : "+v"(cc[0]), "+v"(cc[1]), "+v"(cc[2]), "+v"(cc[3]), "+v"(cc[4])); SBAR(); } while (0)
    O_RD(wA, 0);
    O_READY(0, 5);
    O_RD(wB, 1); O_MF(wA, 0); O_RD(wA, 2); O_MF(wB, 1); O_RD(wB, 3); O_MF(wA, 2); O_RD(wA, 4); O_MF(wB, 3); O_RD(wB, 5); O_MF(wA, 4);
    O_READY(5, 5);
    O_RD(wA, 6); O_MF(wB, 5); O_RD(wB, 7); O_MF(wA, 6); O_RD(wA, 8); O_MF(wB, 7); O_RD(wB, 9); O_MF(wA, 8); O_MF(wB, 9);
#undef O_READY
#undef O_RD
#undef O_MF
    return ok;
}

__device__ __forceinline__ void b_waves(const Params& P, char* lds, LAS unsigned* ctl, rsrc_t rw, unsigned wid, unsigned lane, unsigned b, unsigned li, unsigned rt64, unsigned c4, u64* stamps) {
    char* const ws = P.ws;
    const unsigned lds0 = (unsigned)(uintptr_t)lds;
    const int s0 = 64 * rt64;
    const unsigned i16 = lane & 15, g = lane >> 4, q4 = i16 >> 2, p4 = i16 & 3;
    unsigned bn = 0; LAS unsigned* bb = ctl + C_BB;
    f32x4 qa[5];
#pragma unroll
    for (int j = 0; j < 5; ++j) qa[j] = (f32x4){0.f, 0.f, 0.f, 0.f};
    if (wid >= 4) {
        const unsigned l = wid - 4;
        unsigned off[5];
#pragma unroll
        for (unsigned j = 0; j < 5; ++j) { const unsigned id = l + 4 * j;
            if (j < 2) { const unsigned p = id * 64 + lane, row = p >> 3, ch = (p & 7) ^ ((row >> 1) & 7); off[j] = row * 1280 + ch * 16; }
            else { const unsigned p = ((id < 18 ? id : 8) - 8) * 64 + lane, pos = p / 10, j10 = p - pos * 10, k = pos ^ (4 * ((pos >> 3) & 1)); off[j] = k * 1280 + j10 * 16; } }
        const char* xs = (const char*)(P.x + (size_t)s0 * D); const char* wsq = (const char*)(P.wq + 80 * c4);
        int slot = 0;
        lds_wait<false>(ctl + C_P0I, 4u);
#pragma unroll 1
        for (int t = 0; t < 10; ++t) {
            if (t == 5) { VMW(16); lds_signal(ctl + C_QL + l, lane); }
            if (t >= 5) { VMW(12); lds_signal(ctl + C_QL + l, lane);
                lds_wait4(ctl + C_QD, t - 4); }
            const unsigned so = lds0 + slot * Q_SLOT + l * 1024;
            gldsb(xs, off[0], so); gldsb(xs, off[1], so + 4096); gldsb(wsq, off[2], so + 8192); gldsb(wsq, off[3], so + 12288);
            if (l < 2) gldsb(wsq, off[4], so + 16384);
            xs += 128; wsq += 64 * 1280;
            slot = slot == 4 ? 0 : slot + 1;
        }
        VMW(12); lds_signal(ctl + C_QL + l, lane); VMW(8); lds_signal(ctl + C_QL + l, lane); VMW(4); lds_signal(ctl + C_QL + l, lane); VMW(0); lds_signal(ctl + C_QL + l, lane);
        PSTAMP(1);
    } else {
        const lds_cptr qx0 = (lds_cptr)lds + (16 * wid + i16) * 128 + ((g ^ (i16 >> 1)) * 16), qx1 = (lds_cptr)lds + (16 * wid + i16) * 128 + (((4 + g) ^ (i16 >> 1)) * 16);
        const lds_cptr qw0 = (lds_cptr)lds + Q_W + (8 * g + q4 + 4 * (0 ^ (g & 1))) * 160 + p4 * 8, qw1 = (lds_cptr)lds + Q_W + (8 * g + q4 + 4 * (1 ^ (g & 1))) * 160 + p4 * 8;
        if (wid < 3) { const unsigned idx = wid * 64 + lane, row = (li >> 1) + 16 * (idx >> 1);
            if (idx < 160) touch((const char*)((row < 640 ? P.woA + (size_t)row * 640 : P.woB + (size_t)(row - 640) * 640) + 80 * c4) + (idx & 1) * 156, lds0 + DUMMY); }
        PSTAMP(1);
        bf16x8 xa0, xa1, wa[10], xb0, xb1, wb_[10];
#define Q_READ(X0, X1, W, t_, so_) do { lds_wait4(ctl + C_QL, (t_) + 1); X0 = lds128(qx0 + (so_)); X1 = lds128(qx1 + (so_)); \
            _Pragma("unroll") for (int j = 0; j < 10; ++j) W[j] = ldstr2(qw0 + (so_) + (j / 5) * 5120 + (j % 5) * 32, qw1 + (so_) + (j / 5) * 5120 + (j % 5) * 32); SBAR(); } while (0)
#define Q_MUL(X0, X1, W) do { _Pragma("unroll") for (int j = 0; j < 5; ++j) qa[j] = MFMA16(W[j], X0, qa[j]); _Pragma("unroll") for (int j = 0; j < 5; ++j) qa[j] = MFMA16(W[5 + j], X1, qa[j]); \
            asm volatile("" : "+v"(qa[0]), "+v"(qa[1]), "+v"(qa[2]), "+v"(qa[3]), "+v"(qa[4])); SBAR(); } while (0)
#define Q_DONE() do { asm volatile("s_waitcnt lgkmcnt(0)" ::: "memory"); lds_signal(ctl + C_QD + wid, lane); } while (0)
        Q_READ(xa0, xa1, wa, 0, 0); Q_DONE();
        int slot = 1;
#pragma unroll 1
        for (int t = 0; t < 10; t += 2) {
            Q_READ(xb0, xb1, wb_, t + 1, slot * Q_SLOT); slot = slot == 4 ? 0 : slot + 1;
            Q_MUL(xa0, xa1, wa); Q_DONE();
            if (t + 2 < 10) { Q_READ(xa0, xa1, wa, t + 2, slot * Q_SLOT); slot = slot == 4 ? 0 : slot + 1; }
            Q_MUL(xb0, xb1, wb_);
            if (t + 2 < 10) Q_DONE();
        }
#undef Q_READ
#undef Q_MUL
#undef Q_DONE
    }
    PSTAMP(2);
    if (wid == 4) {
        lds_wait<true>(ctl + C_TAG, 1u);
        const u64 tag = (u64)ctl[C_TAG + 1] | ((u64)ctl[C_TAG + 2] << 32);
        bool ok = false;
#pragma unroll 1
        for (unsigned spins = 0; spins < SPIN_CAP; ++spins) {
            const u32x4 v = ld128(rw, WS_FKV + li * 1024 + lane * 16);
            if (__all((v[0] == (unsigned)tag) & (v[1] == (unsigned)tag) & (v[2] == (unsigned)tag) & (v[3] == (unsigned)tag))) { ok = true; break; }
            __builtin_amdgcn_s_sleep(POLL_SLEEP);
        }
        if (!ok && lane == 0) ctl[C_BAD] = 1u;
        lds_signal(ctl + C_KVOK, lane);
        if (b == 0 && lane == 0) st64(rw, WS_EPOCH, (u32x2){(unsigned)tag, (unsigned)(tag >> 32)});
    }
    const char* kv = ws + WS_KV;
    const unsigned w4 = wid & 3, kh = wid >> 2;
    char* ldsh = lds + kh * STG_BYTES;
    const unsigned ldsh0 = lds0 + kh * STG_BYTES;
    u32x4 nr0 = {0u, 0u, 0u, 0u}, nr1 = nr0;
    if (wid < 4) {
        lds_wait4(ctl + C_QD, 10u);
#pragma unroll
        for (int j = 0; j < 5; ++j) { const unsigned cq = 4 * j + g, hl = (cq * 13) >> 6, dq = cq - 5 * hl;
            *(LAS u32x2*)((lds_ptr)lds + Q_ST + (16 * wid + i16) * QB_ROW + hl * 48 + dq * 8) = (u32x2){pk2(qa[j][0] * QSCALE, qa[j][1] * QSCALE), pk2(qa[j][2] * QSCALE, qa[j][3] * QSCALE)}; }
        lds_signal(ctl + C_QS, lane);
    } else {
        lds_wait<false>(ctl + C_KVOK, 1u);
        asm volatile("global_load_dwordx4 %0, %2, %3 sc1\n\tglobal_load_dwordx4 %1, %2, %3 offset:1024 sc1" : "=&v"(nr0), "=&v"(nr1) : "v"(lane * 16), "s"(ws + WS_NRM) : "memory");
        lds_wait4(ctl + C_QD, 8u);
        att_stage_load(kv, 0, lds0, w4, lane); att_stage_load(kv, 4, lds0 + STG_BYTES, w4, lane);
    }
    lds_wait<false>(ctl + C_QS, 4u);
    const int r = lane & 31, hh = lane >> 5;
    bf16x8 qm[2]; u32x2 qtl[2];
#pragma unroll
    for (int h = 0; h < 2; ++h) {
        const lds_cptr qp = (lds_cptr)lds + Q_ST + (32 * (w4 & 1) + r) * QB_ROW + (2 * (w4 >> 1) + h) * 48;
        qm[h] = lds128(qp + hh * 16); qtl[h] = *(const LAS u32x2*)(qp + 32);
    }
    float q2[2];
#pragma unroll
    for (int h = 0; h < 2; ++h) {
        q2[h] = 0.f;
#pragma unroll
        for (int i = 0; i < 8; ++i) { const float v = bf2f((u16)qm[h][i]); q2[h] += v * v; }
        if (hh == 0) { const float a0 = lo2f(qtl[h][0]), a1 = hi2f(qtl[h][0]), a2 = lo2f(qtl[h][1]), a3 = hi2f(qtl[h][1]); q2[h] += a0 * a0 + a1 * a1 + a2 * a2 + a3 * a3; }
        q2[h] = sum32(q2[h]);
    }
    if (wid == 7) {
        asm volatile("s_waitcnt vmcnt(12)" : "+v"(nr0), "+v"(nr1) :: "memory");
        const float kmax2 = wave_max_pos(fmaxf(__uint_as_float(nr0[0]), __uint_as_float(nr1[0]))) + wave_max_pos(fmaxf(__uint_as_float(nr0[1]), __uint_as_float(nr1[1])));
        const float vmax = wave_max_pos(fmaxf(fmaxf(__uint_as_float(nr0[2]), __uint_as_float(nr1[2])), fmaxf(__uint_as_float(nr0[3]), __uint_as_float(nr1[3]))));
        if (lane == 0) { ctl[C_NRM] = __float_as_uint(kmax2); ctl[C_NRM + 1] = __float_as_uint(112.f - fmaxf(0.f, __builtin_amdgcn_logf(fmaxf(vmax, 1.f)))); }
    }
    PSTAMP(3);
    VMW(0);
    asm volatile("" :: "v"(nr0), "v"(nr1));
    GBAR(bb, bn, 8);
    const float kmax2 = __uint_as_float(ctl[C_NRM]), lim = __uint_as_float(ctl[C_NRM + 1]);
    const float bnd0 = __builtin_sqrtf(q2[0] * kmax2) * 1.001f + 1.f, bnd1 = __builtin_sqrtf(q2[1] * kmax2) * 1.001f + 1.f;
    const u32x4 z4 = {0u, 0u, 0u, 0u};
    bf16x8 qt[2][2];
#pragma unroll
    for (int h = 0; h < 2; ++h) { const u32x4 a = {qtl[h][0], qtl[h][1], 0u, 0u}; qt[h][0] = __builtin_bit_cast(bf16x8, hh ? z4 : a); qt[h][1] = __builtin_bit_cast(bf16x8, hh ? a : z4); }
    const f32x16 zero = {};
    float m[2]; bool unsafe = false;
    { const lds_cptr kb = (lds_cptr)ldsh + lane * 16; const bf16x8 k0 = lds128(kb + STG_KM), kt = lds128(kb + STG_KT);
#pragma unroll
      for (int h = 0; h < 2; ++h) {
        f32x16 sc = MFMA32(k0, qm[h], zero); sc = MFMA32(kt, qt[h][0], sc);
        float mm = sc[0];
#pragma unroll
        for (int i = 1; i < 16; ++i) mm = fmaxf(mm, sc[i]);
        mm = max32(mm);
        m[h] = bf2f(f2bf(mm));
        unsafe |= !((h ? bnd1 : bnd0) - m[h] <= lim);
      } }
    if (__any(unsafe) && lane == 0) ctl[C_FALL] = 1u;
    GBAR(bb, bn, 8);
    att_stage_load(kv, 4 * kh + 1, ldsh0 + KVB, w4, lane);
    if (rfl(ctl[C_FALL]) != 0u) {
        att_maxpass(kv, ldsh, bb, bn, kh, w4, lane, qm, qt, m);
#pragma unroll
        for (int h = 0; h < 2; ++h) m[h] = bf2f(f2bf(max32(m[h])));
    }
#pragma unroll
    for (int h = 0; h < 2; ++h) { const unsigned nm = f2bf(-m[h]); const u32x4 a = {qtl[h][0], qtl[h][1], nm, 0u}; qt[h][0] = __builtin_bit_cast(bf16x8, hh ? z4 : a); qt[h][1] = __builtin_bit_cast(bf16x8, hh ? a : z4); }
    f32x16 O[2] = {{}, {}};
    PSTAMP(4);
    att_main(kv, ldsh, ctl, rw, ofrag_off(s0 + lane, 0), 80 * c4, WS_FA + (rt64 * 8 + c4) * 8, kh, w4, lane, qm, qt, O[0], O[1]);
    PSTAMP(5);
    lds_signal(ctl + C_BD, lane);
    __builtin_amdgcn_s_setprio(3);
    {
        LAS f32x4* xo = (LAS f32x4*)((lds_ptr)lds + (2 * w4 + kh) * 4096) + lane;
        LAS f32x4* xi = (LAS f32x4*)((lds_ptr)lds + (2 * w4 + (kh ^ 1)) * 4096) + lane;
        const f32x16& Og = kh ? O[0] : O[1]; const f32x16& Ok = kh ? O[1] : O[0];
        const int s = s0 + 32 * (w4 & 1) + r, k0 = 640 + (4 * c4 + 2 * (w4 >> 1) + kh) * DH;
        const int a0 = ofrag_off(s, k0 + 4 * hh), a1 = ofrag_off(s, k0 + 8 + 4 * hh), a2 = ofrag_off(s, k0 + 16);
        lds_wait<false>(ctl + C_X, 8u);
        xo[0] = (f32x4){Og[0], Og[1], Og[2], Og[3]}; xo[64] = (f32x4){Og[4], Og[5], Og[6], Og[7]}; xo[128] = (f32x4){Og[8], Og[9], Og[10], Og[11]};
        xo[192] = (f32x4){kh ? m[0] : m[1], 0.f, 0.f, 0.f};
        lds_signal(ctl + C_P + 2 * w4 + kh, lane);
        lds_wait<false>(ctl + C_P + 2 * w4 + (kh ^ 1), 1u);
        const f32x4 p0 = xi[0], p1 = xi[64], p2 = xi[128]; const float m1 = xi[192][0];
        const float mo = kh ? m[1] : m[0];
        const float M = fmaxf(mo, m1), f0 = EXP2(mo - M), f1 = EXP2(m1 - M);
        float o[12];
#pragma unroll
        for (int i = 0; i < 4; ++i) { o[i] = Ok[i] * f0 + p0[i] * f1; o[4 + i] = Ok[4 + i] * f0 + p1[i] * f1; o[8 + i] = Ok[8 + i] * f0 + p2[i] * f1; }
        auto lr = __builtin_amdgcn_permlane32_swap(__float_as_uint(o[8]), __float_as_uint(o[8]), false, false);
        const float inv = __builtin_amdgcn_rcpf(__uint_as_float(lr[1]));
#define NE(x) ((x) == EMPTY ? 0x7fc07fc0u : (x))
        const unsigned w0 = pk2(o[0] * inv, o[1] * inv), w1 = pk2(o[2] * inv, o[3] * inv), w2 = pk2(o[4] * inv, o[5] * inv), w3 = pk2(o[6] * inv, o[7] * inv), w4_ = pk2(o[8] * inv, o[9] * inv), w5 = pk2(o[10] * inv, o[11] * inv);
        st64(rw, a0, (u32x2){NE(w0), NE(w1)});
        st64(rw, a1, (u32x2){NE(w2), NE(w3)});
        if (hh == 0) st64(rw, a2, (u32x2){NE(w4_), NE(w5)});
#undef NE
        lds_signal(ctl + C_D, lane);
    }
    if (kh == 1) {
        lds_wait<false>(ctl + C_BD, 8u);
#pragma unroll
        for (unsigned i = 0; i < 5; ++i) { const unsigned p = 256 * i + w4 * 64 + lane, pos = p / 10, j10 = p - pos * 10, off = (pos ^ (4 * ((pos >> 3) & 1))) * 1280 + j10 * 16;
            const char* sb = (const char*)(P.woB + 80 * c4) + 3 * (128 * 1280);
            gldsb(sb, off, rfl(lds0 + C_S3 + (4 * i + w4) * 1024)); gldsb(sb + 128 * 1280, off, rfl(lds0 + C_S4 + (4 * i + w4) * 1024)); }
        VMW(0); lds_signal(ctl + C_WB + 1, lane);
    }
    if (kh == 0) {
        f32x4 cc[5];
#pragma unroll
        for (int j = 0; j < 5; ++j) cc[j] = (f32x4){0.f, 0.f, 0.f, 0.f};
        lds_wait(ctl + C_D, 8u);
        __builtin_amdgcn_s_sleep(OB_DELAY);
        const bool ok = ob_part<10>(lds, ctl, rw, WS_O + (4 * rt64 + w4) * 40 * 1024, lane, cc);
        if (!ok) ctl[C_BAD] = 1u;
        LAS f32x4* red = (LAS f32x4*)((lds_ptr)lds + w4 * 5120) + lane;
#pragma unroll
        for (int j = 0; j < 5; ++j) red[j * 64] = cc[j];
        lds_signal(ctl + C_R + w4, lane);
    }
    asm volatile("s_waitcnt lgkmcnt(0)" ::: "memory");
    PSTAMP(6);
}

__device__ __forceinline__ void a_waves(const Params& P, char* lds, LAS unsigned* ctl, rsrc_t rw, unsigned a, unsigned lane, unsigned b, unsigned li, unsigned rt64, unsigned c4, u64* stamps) {
    const unsigned lds0 = (unsigned)(uintptr_t)lds;
    const int s0 = 64 * rt64;
    const unsigned i16 = lane & 15, g = lane >> 4, q4 = i16 >> 2, p4 = i16 & 3;
    unsigned an = 0; LAS unsigned* ab = ctl + C_AB;
    const unsigned part = c4 & 1, r0 = s0 + 16 * (c4 >> 1);
    {
        if (a == 3) { u64 pc; asm volatile("s_getpc_b64 %0" : "=s"(pc)); const unsigned ln = li * 10 + (lane < 10 ? lane : 0); touch((const char*)(pc & ~127ull) + (ln < CODE_LINES ? ln : 0) * 128, lds0 + DUMMY); }
    }
    { const char* wsrc = (const char*)(part ? P.wv : P.wk); const char* xsrc = (const char*)(P.x + (size_t)r0 * D);
#pragma unroll 1
      for (unsigned i = 0; i < 12; ++i) { unsigned id = a + 4 * i; if (id == 47) id = 43;
        if (id < 25) gldsb(wsrc, id * 1024 + lane * 16, lds0 + P0W + id * 1024);
        else if (id < 45) { const unsigned p = (id - 25) * 64 + lane, row = p / 80, cpos = p - row * 80, ch = (cpos & ~15u) | ((cpos & 15) ^ row);
            gldsb(xsrc, row * 1280 + ch * 16, lds0 + P0X + (id - 25) * 1024); }
        else { const unsigned off = (id - 45) * 1024 + lane * 16; gldsb(P.gw, off < 1280 ? off : 1264, lds0 + P0G + (id - 45) * 1024); } } }
    lds_signal(ctl + C_P0I, lane);
    const u32x2 ep = ld64(rw, WS_EPOCH);
    const u64 tag = ((u64)ep[0] | ((u64)ep[1] << 32)) + 0x9E3779B97F4A7C15ull;
    const u32x2 tagv = {(unsigned)tag, (unsigned)(tag >> 32)};
    PSTAMP(1);
    VMW(0);
    GBARS(ab, an, 4);
    if (a < 2 || (a == 2 && part == 0)) {
        const lds_cptr p0x = (lds_cptr)lds + P0X + i16 * 1280;
        f32x4 pacc = {0.f, 0.f, 0.f, 0.f};
#define P0XF(ks) lds128(p0x + ((((4 * (ks) + g) & ~15u) | (((4 * (ks) + g) & 15) ^ i16)) * 16))
        const int ks0 = r0 & 255, img = WS_KV + (r0 >> 8) * STG_BYTES, nrm = WS_NRM + (r0 >> 4) * 16;
        if (a == 2) {
            const bf16x8 z = {0, 0, 0, 0, 0, 0, 0, 0};
#pragma unroll
            for (int ks = 0; ks < 20; ++ks) { const bf16x8 gv = lds128((lds_cptr)lds + P0G + (32 * ks + 8 * g) * 2); pacc = MFMA16(i16 == 0 ? gv : z, P0XF(ks), pacc); }
            if (g == 0) st32(rw, WS_G + (r0 + i16) * 4, __float_as_uint(1.f / (1.f + __expf(-pacc[0]))));
        } else {
            const lds_cptr p0w = (lds_cptr)lds + P0W + (8 * g + q4) * 40 + (16 * a + 4 * p4) * 2;
            if (part == 0) {
#pragma unroll
                for (int ks = 0; ks < 20; ++ks) pacc = MFMA16(ldstr2(p0w + ks * 1280, p0w + ks * 1280 + 160), P0XF(ks), pacc);
                const unsigned w0 = pk2(pacc[0], pacc[1]), w1 = pk2(pacc[2], pacc[3]);
                const int key = ks0 + i16, tile = key >> 5, r = swap23(key & 31);
                float n2 = lo2f(w0) * lo2f(w0) + hi2f(w0) * hi2f(w0) + lo2f(w1) * lo2f(w1) + hi2f(w1) * hi2f(w1);
                if (a == 0) { st64(rw, img + STG_KM + tile * 1024 + (g >> 1) * 512 + r * 16 + (g & 1) * 8, (u32x2){w0, w1});
                    n2 = sum16(n2); n2 = sum32(n2); }
                else { if (g == 0) st128(rw, img + STG_KT + (tile >> 1) * 1024 + (tile & 1) * 512 + r * 16, (u32x4){w0, w1, 0x3f80u, 0u}); if (g != 0) n2 = 0.f; }
                n2 = wave_max_pos(n2);
                if (lane == 0) st32(rw, nrm + a * 4, __float_as_uint(n2));
            } else {
#pragma unroll
                for (int ks = 0; ks < 20; ++ks) pacc = MFMA16(P0XF(ks), ldstr2(p0w + ks * 1280, p0w + ks * 1280 + 160), pacc);
                const unsigned w0 = pk2(pacc[0], pacc[1]), w1 = pk2(pacc[2], pacc[3]);
                const int d = 16 * a + i16;
                float vm = fmaxf(fmaxf(fabsf(lo2f(w0)), fabsf(hi2f(w0))), fmaxf(fabsf(lo2f(w1)), fabsf(hi2f(w1))));
                if (d >= 20) vm = 0.f;
                if (d <= 20) st64(rw, img + STG_VT + d * VT_ROW + (ks0 + 4 * g) * 2, d == 20 ? (u32x2){0x3f803f80u, 0x3f803f80u} : (u32x2){w0, w1});
                if (a == 1 && ks0 == 0 && lane < 40) st128(rw, img + STG_VT + 21 * VT_ROW + lane * 16, (u32x4){0u, 0u, 0u, 0u});
                vm = wave_max_pos(vm);
                if (lane == 0) st32(rw, nrm + (2 + a) * 4, __float_as_uint(vm));
            }
        }
#undef P0XF
        VMW(0);
    }
    GBARS(ab, an, 4);
    bool ok = true;
    if (a == 3) { if (lane < 32) st32(rw, WS_FKV + lane * 1024 + b * 4, tagv[0]);
        if (lane == 0) { ctl[C_TAG + 1] = tagv[0]; ctl[C_TAG + 2] = tagv[1]; }
        lds_signal(ctl + C_TAG, lane);
    }
    PSTAMP(2);
    unsigned aoff[5];
#pragma unroll
    for (unsigned j = 0; j < 5; ++j) { const unsigned id = a + 4 * j;
        if (id < 15) { const unsigned seg = id / 5, p = (id - 5 * seg) * 64 + lane, pos = p / 10, j10 = p - pos * 10, k = pos ^ (4 * ((pos >> 3) & 1)); aoff[j] = k * 3840 + seg * 1280 + j10 * 16; }
        else { const unsigned p = (id - 15) * 64 + lane, row = p >> 2, ch = (p & 3) ^ (3 * ((row >> 2) & 1)); int gr = s0 - 16 + (int)row; gr = gr < 0 ? 0 : gr; aoff[j] = gr * 1280 + ch * 16; } }
    const char* awb = (const char*)(P.wqkv + 80 * c4); const char* axb = (const char*)P.x;
#define A_ISSUE(slot) do { gldsb(awb, aoff[0], lds0 + AA + (slot) * A_SLOT + a * 1024); gldsb(awb, aoff[1], lds0 + AA + (slot) * A_SLOT + (a + 4) * 1024); gldsb(awb, aoff[2], lds0 + AA + (slot) * A_SLOT + (a + 8) * 1024); \
        gldsb(a < 3 ? awb : axb, aoff[3], lds0 + AA + (slot) * A_SLOT + (a + 12) * 1024); gldsb(axb, aoff[4], lds0 + AA + (slot) * A_SLOT + (a + 16) * 1024); awb += 32 * 3840; axb += 64; } while (0)
    lds_wait4<true>(ctl + C_QL, A_START);
    A_ISSUE(0); A_ISSUE(1);
    f32x4 acc[5][4];
#pragma unroll
    for (int i = 0; i < 5; ++i)
#pragma unroll
        for (int j = 0; j < 4; ++j) acc[i][j] = (f32x4){0.f, 0.f, 0.f, 0.f};
    const lds_cptr ax = (lds_cptr)lds + AA + A_X + i16 * 64 + ((g ^ (3 * ((i16 >> 2) & 1))) * 16);
    lds_cptr aw0[4], aw1[4];
#pragma unroll
    for (unsigned j = 0; j < 4; ++j) { unsigned jt = 4 * a + j; jt = jt < 15 ? jt : 14; const unsigned sg = jt / 5, wi = jt - 5 * sg;
        aw0[j] = (lds_cptr)lds + AA + sg * 5120 + wi * 32 + (8 * g + q4 + 4 * (0 ^ (g & 1))) * 160 + p4 * 8; aw1[j] = (lds_cptr)lds + AA + sg * 5120 + wi * 32 + (8 * g + q4 + 4 * (1 ^ (g & 1))) * 160 + p4 * 8; }
    VMW(5); lds_signal(ab, lane); an += 4;
    { int slot = 0;
#pragma unroll 1
      for (int t = 0; t < 20; ++t) {
        lds_wait<true>(ab, an);
        const int so = slot * A_SLOT, fs = slot == 0 ? 2 : slot - 1;
        bf16x8 xf[5], w0 = ldstr2(aw0[0] + so, aw1[0] + so), w1;
#pragma unroll
        for (int i = 0; i < 5; ++i) xf[i] = lds128(ax + so + i * 1024);
        w1 = ldstr2(aw0[1] + so, aw1[1] + so);
        SBAR();
#define A_MF(i, j, w) acc[i][j] = MFMA16(w, xf[i], acc[i][j])
#define A_LD(j) do { if (t + 2 < 20) gldsb((j) < 3 || ((j) == 3 && a < 3) ? awb : axb, aoff[j], lds0 + AA + fs * A_SLOT + (a + 4 * (j)) * 1024); } while (0)
        A_MF(0, 0, w0); A_LD(0); A_MF(1, 0, w0); A_PACE; A_MF(2, 0, w0); A_LD(1); A_MF(3, 0, w0); A_PACE; A_MF(4, 0, w0); SBAR();
        w0 = ldstr2(aw0[2] + so, aw1[2] + so);
        A_MF(0, 1, w1); A_LD(2); A_MF(1, 1, w1); A_PACE; A_MF(2, 1, w1); A_LD(3); A_MF(3, 1, w1); A_PACE; A_MF(4, 1, w1); SBAR();
        w1 = ldstr2(aw0[3] + so, aw1[3] + so);
        A_MF(0, 2, w0); A_LD(4); A_MF(1, 2, w0); A_PACE; A_MF(2, 2, w0); A_PACE; SBAR();
        awb += 32 * 3840; axb += 64;
        if (t < 18) VMW(5); else VMW(0);
        lds_signal(ab, lane); an += 4;
        A_MF(3, 2, w0); A_PACE; A_MF(4, 2, w0); A_PACE;
        A_MF(0, 3, w1); A_PACE; A_MF(1, 3, w1); A_PACE; A_MF(2, 3, w1); A_PACE; A_MF(3, 3, w1); A_PACE; A_MF(4, 3, w1); SBAR();
        slot = slot == 2 ? 0 : slot + 1;
      } }
#undef A_MF
#undef A_LD
    PSTAMP(3);
    lds_wait<true>(ab, an);
    { lds_ptr st = (lds_ptr)lds;
#pragma unroll
      for (unsigned j = 0; j < 4; ++j) { const unsigned jt = 4 * a + j, sg = jt / 5, wi = jt - 5 * sg, cq = 4 * wi + g, hl = (cq * 13) >> 6, dq = cq - 5 * hl;
        if (jt < 15) {
#pragma unroll
            for (int i = 0; i < 5; ++i) { const int row = 16 * i + i16; const f32x4 v = acc[i][j];
                if (sg < 2) *(LAS u32x2*)(st + ST_QK + row * QK_ROW + sg * 192 + hl * 48 + dq * 8) = (u32x2){pk2(v[0], v[1]), pk2(v[2], v[3])};
                else *(LAS f32x4*)(st + ST_V + row * V_ROW + hl * 80 + dq * 16) = v; } } } }
    PSTAMP(9);
    GBARS(ab, an, 4);
    PSTAMP(10);
    {
        const int sl = lane, row = 16 + sl, s = s0 + sl;
        const lds_cptr qp = (lds_cptr)lds + ST_QK + row * QK_ROW + a * 48;
        const lds_cptr kp = qp + 192 - 14 * QK_ROW;
        const u32x4 qa = *(const LAS u32x4*)qp, qb = *(const LAS u32x4*)(qp + 16); const u32x2 qc = *(const LAS u32x2*)(qp + 32);
        u32x4 ka[8], kb[8]; u32x2 kc[8];
#pragma unroll
        for (int w = 0; w < 8; ++w) { ka[w] = *(const LAS u32x4*)(kp + (14 - 2 * w) * QK_ROW); kb[w] = *(const LAS u32x4*)(kp + (14 - 2 * w) * QK_ROW + 16); kc[w] = *(const LAS u32x2*)(kp + (14 - 2 * w) * QK_ROW + 32); }
        SBAR();
        const lds_cptr vp = (lds_cptr)lds + ST_V + (row - 14) * V_ROW + a * 80;
        float lg[8]; float m = -3.0e38f;
#pragma unroll
        for (int w = 0; w < 8; ++w) {
            float x = dot2(qa[0], ka[w][0], 0.f), x2 = dot2(qa[1], ka[w][1], 0.f);
            x = dot2(qa[2], ka[w][2], x); x2 = dot2(qa[3], ka[w][3], x2); x = dot2(qb[0], kb[w][0], x); x2 = dot2(qb[1], kb[w][1], x2);
            x = dot2(qb[2], kb[w][2], x); x2 = dot2(qb[3], kb[w][3], x2); x = dot2(qc[0], kc[w][0], x); x2 = dot2(qc[1], kc[w][1], x2);
            x = (x + x2) * QSCALE;
            lg[w] = (s - 2 * w >= 0) ? x : -1.0e9f * LOG2E; m = fmaxf(m, lg[w]);
        }
        f32x4 va[3][5], vb[3][5];
#define LV(dst, w0, n) do { _Pragma("unroll") for (int w = 0; w < (n); ++w) _Pragma("unroll") for (int j = 0; j < 5; ++j) dst[w][j] = *(const LAS f32x4*)(vp + (14 - 2 * ((w0) + w)) * V_ROW + j * 16); SBAR(); } while (0)
#define PIN5(o) asm volatile("" : "+v"(o[0]), "+v"(o[1]), "+v"(o[2]), "+v"(o[3]), "+v"(o[4]))
        LV(va, 0, 3);
        float l = 0.f;
#pragma unroll
        for (int w = 0; w < 8; ++w) { lg[w] = EXP2(lg[w] - m); l += lg[w]; }
        const float inv = __builtin_amdgcn_rcpf(l);
        LV(vb, 3, 3);
        f32x4 o[5];
#pragma unroll
        for (int j = 0; j < 5; ++j) o[j] = va[0][j] * lg[0] + va[1][j] * lg[1] + va[2][j] * lg[2];
        PIN5(o); SBAR();
        LV(va, 6, 2);
#pragma unroll
        for (int j = 0; j < 5; ++j) o[j] += vb[0][j] * lg[3] + vb[1][j] * lg[4] + vb[2][j] * lg[5];
        PIN5(o); SBAR();
#pragma unroll
        for (int j = 0; j < 5; ++j) o[j] += va[0][j] * lg[6] + va[1][j] * lg[7];
#undef LV
#undef PIN5
        lds_wait(ctl + C_MKD, 1u);
#pragma unroll
        for (int j = 0; j < 5; ++j) { const unsigned w0 = pk2(o[j][0] * inv, o[j][1] * inv), w1 = pk2(o[j][2] * inv, o[j][3] * inv);
            st64(rw, ofrag_off(s, (4 * c4 + a) * DH + 4 * j), (u32x2){w0 == EMPTY ? 0x7fc07fc0u : w0, w1 == EMPTY ? 0x7fc07fc0u : w1}); }
        PSTAMP(11);
    }
    GBARS(ab, an, 4);

    PSTAMP(4);
    unsigned coff[5];
#pragma unroll
    for (unsigned i = 0; i < 5; ++i) { const unsigned p = 256 * i + a * 64 + lane, pos = p / 10, j10 = p - pos * 10; coff[i] = (pos ^ (4 * ((pos >> 3) & 1))) * 1280 + j10 * 16; }
    auto c_issue = [&](const u16* W, unsigned chunk, unsigned dst) {
        const char* sb = (const char*)(W + 80 * c4) + (size_t)chunk * (128 * 1280);
#pragma unroll
        for (unsigned i = 0; i < 5; ++i) gldsb(sb, coff[i], lds0 + dst + (4 * i + a) * 1024);
    };
    ok &= poll_tags<1>(rw, WS_FA + (rt64 * 8 + (lane & 7)) * 8, lane < 8, tag);
    c_issue(P.woA, 0, AA); c_issue(P.woA, 1, AA + A_SLOT); c_issue(P.woA, 2, AA + 2 * A_SLOT);
    const int fo = WS_O + (4 * rt64 + a) * 40 * 1024, fl = lane * 16;
    bf16x8 of[20];
#pragma unroll
    for (int ks = 0; ks < 20; ++ks) of[ks] = __builtin_bit_cast(bf16x8, ld128s(rw, fl, fo + ks * 1024));
    const float gg = __uint_as_float(ld32(rw, WS_G + (s0 + 16 * a + i16) * 4));
    const unsigned cbo = (8 * g + q4) * 160 + p4 * 8, ct0 = 4 * (0 ^ (g & 1)) * 160, ct1 = 4 * (1 ^ (g & 1)) * 160;
    const lds_cptr cw0 = (lds_cptr)lds + cbo + ct0, cw1 = (lds_cptr)lds + cbo + ct1;
    f32x4 cc[5]; bf16x8 wA[5], wB[5];
#define C_READY(F, n0, N) do { \
        bool got = false; \
        _Pragma("unroll 1") for (unsigned spins = 0; spins < SPIN_CAP; ++spins) { \
            unsigned mx = 0; \
            _Pragma("unroll") for (int u = 0; u < (N); ++u) { const u32x4 v = __builtin_bit_cast(u32x4, of[(n0) + u]); mx = max(max(mx, v[0]), max(max(v[1], v[2]), v[3])); } \
            if (!__any(mx == EMPTY)) { got = true; break; } \
            __builtin_amdgcn_s_sleep(2); \
            _Pragma("unroll") for (int u = 0; u < (N); ++u) of[(n0) + u] = __builtin_bit_cast(bf16x8, ld128s(rw, fl, fo + ((F) + (n0) + u) * 1024)); \
        } \
        ok &= got; } while (0)
#define C_RD(W, off) do { lds_cptr b0_ = cw0 + (off), b1_ = cw1 + (off); asm volatile("" : "+v"(b0_), "+v"(b1_)); _Pragma("unroll") for (int j = 0; j < 5; ++j) W[j] = ldstr2(b0_ + j * 32, b1_ + j * 32); SBAR(); } while (0)
#define C_MF(W, ks) do { _Pragma("unroll") for (int j = 0; j < 5; ++j) cc[j] = MFMA16(W[j], of[ks], cc[j]); asm volatile("" : "+v"(cc[0]), "+v"(cc[1]), "+v"(cc[2]), "+v"(cc[3]), "+v"(cc[4])); SBAR(); } while (0)
#define C_CHUNK(base, ks0, next) do { C_RD(wB, (base) + 5120); C_MF(wA, (ks0)); C_RD(wA, (base) + 10240); C_MF(wB, (ks0) + 1); C_RD(wB, (base) + 15360); C_MF(wA, (ks0) + 2); \
        if ((next) >= 0) C_RD(wA, (next) >= 0 ? (next) : 0); C_MF(wB, (ks0) + 3); } while (0)
#pragma unroll
    for (int j = 0; j < 5; ++j) cc[j] = (f32x4){0.f, 0.f, 0.f, 0.f};
    C_READY(0, 0, 10); C_READY(0, 10, 10);
    VMW(0); GBARS(ab, an, 4);
    C_RD(wA, AA); C_CHUNK(AA, 0, AA + A_SLOT); C_CHUNK(AA + A_SLOT, 4, AA + 2 * A_SLOT);
    GBARS(ab, an, 4);
    c_issue(P.woA, 3, AA); c_issue(P.woA, 4, AA + A_SLOT);
    C_CHUNK(AA + 2 * A_SLOT, 8, -1);
    VMW(0); GBARS(ab, an, 4);
    c_issue(P.woB, 0, AA + 2 * A_SLOT);
    C_RD(wA, AA); C_CHUNK(AA, 12, AA + A_SLOT); C_CHUNK(AA + A_SLOT, 16, -1);
    u32x2 cA[5];
#pragma unroll
    for (int j = 0; j < 5; ++j) { cA[j] = (u32x2){pk2(cc[j][0], cc[j][1]), pk2(cc[j][2], cc[j][3])}; cc[j] = (f32x4){0.f, 0.f, 0.f, 0.f}; }
    LAS u32x2* park = (LAS u32x2*)((lds_ptr)lds + 36864 + a * 2560) + lane;
    GBARS(ab, an, 4);
    c_issue(P.woB, 1, AA); c_issue(P.woB, 2, AA + A_SLOT);
    VMW(0); lds_signal(ctl + C_WB, lane);
    PSTAMP(5);
    __builtin_amdgcn_s_setprio(3);
    lds_wait(ctl + C_D, 8u);
#pragma unroll
    for (int j = 0; j < 5; ++j) park[j * 64] = cA[j];
    __builtin_amdgcn_s_sleep(OB_DELAY);
    PSTAMP(6);
    ok &= ob_part<0>(lds, ctl, rw, fo, lane, cc);
    PSTAMP(7);
#undef C_READY
#undef C_RD
#undef C_MF
#undef C_CHUNK
    if (!ok) ctl[C_BAD] = 1u;
    lds_wait<false>(ctl + C_R + a, 1u);
    { LAS f32x4* red = (LAS f32x4*)((lds_ptr)lds + a * 5120) + lane;
#pragma unroll
      for (int j = 0; j < 5; ++j) cc[j] += red[j * 64]; }
    const float poison = (rfl(ctl[C_BAD]) || !ok) ? __builtin_nanf("") : 0.f;
    unsigned ln = __builtin_amdgcn_mbcnt_hi(~0u, __builtin_amdgcn_mbcnt_lo(~0u, 0u)); asm volatile("" : "+v"(ln));
#pragma unroll
    for (int j = 0; j < 5; ++j) { const u32x2 pa = *((LAS u32x2*)((lds_ptr)lds + 36864 + a * 2560) + ln + j * 64); const f32x4 av = {lo2f(pa[0]), hi2f(pa[0]), lo2f(pa[1]), hi2f(pa[1])}; const f32x4 v = av * gg + cc[j] * (1.f - gg) + poison;
        *(u32x2*)(P.out + (size_t)(s0 + 16 * a + (ln & 15)) * 640 + 80 * c4 + 16 * j + 4 * (ln >> 4)) = (u32x2){pk2(v[0], v[1]), pk2(v[2], v[3])}; }
    PSTAMP(8);
}

__global__ __launch_bounds__(768, 3) void fused(Params P) {
    extern __shared__ __attribute__((aligned(16))) char lds[];
    PDECL
    PSTAMP(0);
    char* const ws = P.ws;
    const unsigned tid = threadIdx.x, wid = rfl(tid >> 6);
    const unsigned lane = __builtin_amdgcn_mbcnt_hi(~0u, __builtin_amdgcn_mbcnt_lo(~0u, 0u));
    const unsigned b = blockIdx.x & 255, xg = b & 7, li = b >> 3;
    const unsigned rt64 = (xg >> 2) * 16 + (li >> 1), c4 = (xg & 3) * 2 + (li & 1);
    const rsrc_t rw = __builtin_amdgcn_make_buffer_rsrc(ws, 0, WS_END, 0x00020000);
    LAS unsigned* ctl = (LAS unsigned*)((lds_ptr)lds + CTL);
    if (tid < 64) ctl[tid] = 0u;
    LBAR();
    u64* stp = nullptr;
    if (wid < 8) b_waves(P, lds, ctl, rw, wid & 7, lane, b, li, rt64, c4, stp);
    else { if (APRIO) __builtin_amdgcn_s_setprio(APRIO); a_waves(P, lds, ctl, rw, wid & 3, lane, b, li, rt64, c4, stp); }
    PFLUSH();
}

static inline Params mk_params(void* const* d_in, void* d_out, void* d_ws) {
    Params P; P.x = (const u16*)d_in[0]; P.wqkv = (const u16*)d_in[1]; P.woA = (const u16*)d_in[2]; P.wq = (const u16*)d_in[4]; P.wk = (const u16*)d_in[5]; P.wv = (const u16*)d_in[6];
    P.woB = (const u16*)d_in[7]; P.gw = (const u16*)d_in[8]; P.ws = (char*)d_ws; P.out = (u16*)d_out; return P; }
extern "C" void kernel_launch(void* const* d_in, const int* in_sizes, int n_in, void* d_out, int out_size, void* d_ws, size_t ws_size, hipStream_t stream) {
    (void)hipFuncSetAttribute((const void*)fused, hipFuncAttributeMaxDynamicSharedMemorySize, LDS_TOTAL);
    fused<<<256, 768, LDS_TOTAL, stream>>>(mk_params(d_in, d_out, d_ws));
}
#define ST_LAUNCH(d_in, d_out, ws, stream) fused<<<256, 768, LDS_TOTAL, stream>>>(mk_params(d_in, d_out, ws))
```

```cpp
#include <hip/hip_runtime.h>
#include <stdint.h>

typedef unsigned short u16;
typedef unsigned long long u64;
typedef short bf16x8 __attribute__((ext_vector_type(8)));
typedef short s16x4 __attribute__((ext_vector_type(4)));
typedef float f32x16 __attribute__((ext_vector_type(16)));
typedef float f32x4 __attribute__((ext_vector_type(4)));
typedef float f32x2 __attribute__((ext_vector_type(2)));
typedef unsigned u32x4 __attribute__((ext_vector_type(4)));
typedef unsigned u32x2 __attribute__((ext_vector_type(2)));
typedef __bf16 bf2_t __attribute__((ext_vector_type(2)));
#define LAS __attribute__((address_space(3)))
typedef LAS const char* lds_cptr;
typedef LAS char* lds_ptr;
typedef __amdgpu_buffer_rsrc_t rsrc_t;

__device__ __forceinline__ float bf2f(u16 b) { return __uint_as_float(((unsigned)b) << 16); }
__device__ __forceinline__ unsigned pk2(float lo, float hi) { f32x2 v = {lo, hi}; bf2_t r = __builtin_convertvector(v, bf2_t); return __builtin_bit_cast(unsigned, r); }
__device__ __forceinline__ u16 f2bf(float f) { return (u16)(pk2(f, 0.f) & 0xffffu); }
__device__ __forceinline__ float lo2f(unsigned u) { return __uint_as_float(u << 16); }
__device__ __forceinline__ float hi2f(unsigned u) { return __uint_as_float(u & 0xffff0000u); }
__device__ __forceinline__ float dot2(unsigned a, unsigned b, float c) { return __builtin_amdgcn_fdot2_f32_bf16(__builtin_bit_cast(bf2_t, a), __builtin_bit_cast(bf2_t, b), c, false); }

constexpr int S = 2048, D = 640, H = 32, DH = 20;
constexpr float SCALE = 0.22360679774997896f, LOG2E = 1.4426950408889634f;
constexpr float QSCALE = SCALE * LOG2E;

constexpr int STG_KM = 0, STG_KT = 8192, STG_VT = 12288, VT_ROW = 528, STG_BYTES = 24576;
constexpr int WS_FKV_AT = 262144;
constexpr int WS_EPOCH = 0;
constexpr int WS_FKV = WS_FKV_AT;
constexpr int WS_FO = 256;
constexpr int WS_FA = WS_FO + 2048;
constexpr int WS_NRM = 8192;
constexpr int WS_G = WS_NRM + 2048;
constexpr int WS_KV = 32768;
static_assert(WS_KV + 8 * STG_BYTES <= WS_FKV_AT, "map");
constexpr int WS_O = WS_FKV_AT + 32768;
constexpr int WS_END = WS_O + S * 1280 * 2;
constexpr int AUX_SC1 = 16;

#define MFMA32(a, b, c) __builtin_amdgcn_mfma_f32_32x32x16_bf16((a), (b), (c), 0, 0, 0)
#define MFMA16(a, b, c) __builtin_amdgcn_mfma_f32_16x16x32_bf16((a), (b), (c), 0, 0, 0)
#define SBAR() __builtin_amdgcn_sched_barrier(0)
#define EXP2(x) __builtin_amdgcn_exp2f(x)
#define LBAR() asm volatile("s_waitcnt lgkmcnt(0)\n\ts_barrier" ::: "memory")
__device__ __forceinline__ float wave_max_pos(float f) {
    unsigned x = __float_as_uint(f);
    x = max(x, (unsigned)__builtin_amdgcn_update_dpp(0, (int)x, 0x111, 0xf, 0xf, false));
    x = max(x, (unsigned)__builtin_amdgcn_update_dpp(0, (int)x, 0x112, 0xf, 0xf, false));
    x = max(x, (unsigned)__builtin_amdgcn_update_dpp(0, (int)x, 0x114, 0xf, 0xf, false));
    x = max(x, (unsigned)__builtin_amdgcn_update_dpp(0, (int)x, 0x118, 0xf, 0xf, false));
    x = max(x, (unsigned)__builtin_amdgcn_update_dpp(0, (int)x, 0x142, 0xa, 0xf, false));
    x = max(x, (unsigned)__builtin_amdgcn_update_dpp(0, (int)x, 0x143, 0xc, 0xf, false));
    return __uint_as_float((unsigned)__builtin_amdgcn_readlane((int)x, 63));
}
__device__ __forceinline__ float sum16(float f) { auto r = __builtin_amdgcn_permlane16_swap(__float_as_uint(f), __float_as_uint(f), false, false); return __uint_as_float(r[0]) + __uint_as_float(r[1]); }
__device__ __forceinline__ float sum32(float f) { auto r = __builtin_amdgcn_permlane32_swap(__float_as_uint(f), __float_as_uint(f), false, false); return __uint_as_float(r[0]) + __uint_as_float(r[1]); }
__device__ __forceinline__ float max32(float f) { auto r = __builtin_amdgcn_permlane32_swap(__float_as_uint(f), __float_as_uint(f), false, false); return fmaxf(__uint_as_float(r[0]), __uint_as_float(r[1])); }
__device__ __forceinline__ int swap23(int x) { return (x & ~12) | ((x & 4) << 1) | ((x & 8) >> 1); }
__device__ __forceinline__ void glds16(const void* g, unsigned lds_base) {
    unsigned sv; asm volatile("s_mov_b32 %0, m0\n\ts_mov_b32 m0, %2\n\ts_nop 0\n\tglobal_load_lds_dwordx4 %1, off\n\ts_mov_b32 m0, %0" : "=&s"(sv) : "v"(g), "s"(lds_base) : "memory"); }
__device__ __forceinline__ void gldsb(const void* sbase, unsigned voff, unsigned lds_base) {
    unsigned sv; asm volatile("s_mov_b32 %0, m0\n\ts_mov_b32 m0, %3\n\ts_nop 0\n\tglobal_load_lds_dwordx4 %1, %2\n\ts_mov_b32 m0, %0" : "=&s"(sv) : "v"(voff), "s"(sbase), "s"(lds_base) : "memory"); }
__device__ __forceinline__ void gldsb_sc1(const void* sbase, unsigned voff, unsigned lds_base) {
    unsigned sv; asm volatile("s_mov_b32 %0, m0\n\ts_mov_b32 m0, %3\n\ts_nop 0\n\tglobal_load_lds_dwordx4 %1, %2 sc1\n\ts_mov_b32 m0, %0" : "=&s"(sv) : "v"(voff), "s"(sbase), "s"(lds_base) : "memory"); }
__device__ __forceinline__ bf16x8 lds128(lds_cptr p) { return *(const LAS bf16x8*)p; }
typedef short v4i16_t __attribute__((ext_vector_type(4)));
__device__ __forceinline__ s16x4 ldstr(lds_cptr p) { return __builtin_bit_cast(s16x4, __builtin_amdgcn_ds_read_tr16_b64_v4i16((LAS v4i16_t*)p)); }
__device__ __forceinline__ bf16x8 ldstr2(lds_cptr p0, lds_cptr p1) { const s16x4 a = ldstr(p0), b = ldstr(p1); return __builtin_shufflevector(a, b, 0, 1, 2, 3, 4, 5, 6, 7); }
__device__ __forceinline__ unsigned rfl(unsigned x) { return (unsigned)__builtin_amdgcn_readfirstlane(x); }
__device__ __forceinline__ void st32(rsrc_t r, int off, unsigned v) { __builtin_amdgcn_raw_buffer_store_b32(v, r, off, 0, AUX_SC1); }
__device__ __forceinline__ void st64(rsrc_t r, int off, u32x2 v) { __builtin_amdgcn_raw_buffer_store_b64(v, r, off, 0, AUX_SC1); }
__device__ __forceinline__ void st128(rsrc_t r, int off, u32x4 v) { __builtin_amdgcn_raw_buffer_store_b128(v, r, off, 0, AUX_SC1); }
__device__ __forceinline__ unsigned ld32(rsrc_t r, int off) { return __builtin_amdgcn_raw_buffer_load_b32(r, off, 0, AUX_SC1); }
__device__ __forceinline__ u32x2 ld64(rsrc_t r, int off) { return __builtin_amdgcn_raw_buffer_load_b64(r, off, 0, AUX_SC1); }
__device__ __forceinline__ u32x4 ld128(rsrc_t r, int off) { return __builtin_amdgcn_raw_buffer_load_b128(r, off, 0, AUX_SC1); }
__device__ __forceinline__ u32x4 ld128s(rsrc_t r, int voff, int soff) { return __builtin_amdgcn_raw_buffer_load_b128(r, voff, soff, AUX_SC1); }


struct Params { const u16 *x, *wqkv, *woA, *wq, *wk, *wv, *woB, *gw; char* ws; u16* out; };

constexpr int LDS_TOTAL = 163840, CTL = LDS_TOTAL - 256, DUMMY = CTL - 256;
constexpr int KVB = 2 * STG_BYTES;
constexpr int Q_SLOT = 18432, Q_W = 8192, Q_ST = 73728, QB_ROW = 208;
constexpr int AA = 98304;
constexpr int P0W = AA, P0X = AA + 25664, P0G = P0X + 20480;
constexpr int A_SLOT = 20480, A_X = 15360;
constexpr int ST_QK = AA, ST_V = AA + 32000, QK_ROW = 400, V_ROW = 336;
constexpr int C_S3 = 49152, C_S4 = 69632;
constexpr int C_FALL = 0, C_BB = 1, C_DONE = 2, C_LAND = 4, C_X = 6, C_BD = 7, C_D = 12, C_AB = 13, C_KVOK = 16, C_BAD = 17, C_P0I = 18, C_QL = 20, C_QD = 24, C_TAG = 28, C_QS = 19, C_MKD = 15, C_P = 32, C_WB = 40, C_NRM = 42, C_R = 44;
constexpr unsigned CODE_LINES = 296;
constexpr unsigned EMPTY = 0xFFFFFFFFu;
constexpr unsigned SPIN_CAP = 1u << 18;

template <int N, int SLEEP = 4>
__device__ __forceinline__ bool poll_tags(rsrc_t r, int off, bool active, u64 tag) {
#pragma unroll 1
    for (unsigned spins = 0; spins < SPIN_CAP; ++spins) {
        bool ok = true;
        if (active) {
#pragma unroll
            for (int i = 0; i < N; ++i) { const u32x2 v = ld64(r, off + 8 * i); ok &= (v[0] == (unsigned)tag) & (v[1] == (unsigned)(tag >> 32)); }
        }
        if (__all(ok)) return true;
        __builtin_amdgcn_s_sleep(SLEEP);
    }
    return false;
}
__device__ __forceinline__ void lds_signal(LAS unsigned* cnt, int lane) {
    (void)lane;
    __builtin_amdgcn_sched_barrier(0);
    asm volatile("s_mov_b64 exec, 1\n\tds_add_u32 %0, %1\n\ts_mov_b64 exec, -1\n\ts_nop 3" :: "v"((unsigned)(uintptr_t)cnt), "v"(1u) : "memory");
    __builtin_amdgcn_sched_barrier(0); }
template <bool SLEEP = true>
__device__ __forceinline__ void lds_wait(LAS unsigned* cnt, unsigned n) {
    unsigned v, sv, left;
    if (SLEEP)
        asm volatile("s_mov_b32 %2, 0x100000\n1:\n\tds_read_b32 %0, %3\n\ts_waitcnt lgkmcnt(0)\n\tv_readfirstlane_b32 %1, %0\n\ts_cmp_ge_u32 %1, %4\n\ts_cbranch_scc1 2f\n\ts_sleep 1\n\ts_sub_u32 %2, %2, 1\n\ts_cbranch_scc0 1b\n2:"
                     : "=&v"(v), "=&s"(sv), "=&s"(left) : "v"((unsigned)(uintptr_t)cnt), "s"(n) : "memory", "scc");
    else
        asm volatile("s_mov_b32 %2, 0x100000\n1:\n\tds_read_b32 %0, %3\n\ts_waitcnt lgkmcnt(0)\n\tv_readfirstlane_b32 %1, %0\n\ts_cmp_ge_u32 %1, %4\n\ts_cbranch_scc1 2f\n\ts_sub_u32 %2, %2, 1\n\ts_cbranch_scc0 1b\n2:"
                     : "=&v"(v), "=&s"(sv), "=&s"(left) : "v"((unsigned)(uintptr_t)cnt), "s"(n) : "memory", "scc");
}
template <bool SLEEP = false>
__device__ __forceinline__ void lds_wait4(LAS unsigned* cnt, unsigned n) {
#pragma unroll 1
    for (unsigned spins = 0; spins < (1u << 20); ++spins) {
        const u32x4 v = *(volatile LAS u32x4*)cnt;
        const unsigned lo = min(min(v[0], v[1]), min(v[2], v[3]));
        if (rfl(lo) >= n) break;
        if (SLEEP) __builtin_amdgcn_s_sleep(1);
    }
    asm volatile("" ::: "memory");
}
#define VMW(N) asm volatile("s_waitcnt vmcnt(" #N ")" ::: "memory")
#define GBAR(cnt, n, step) do { lds_signal(cnt, lane); n += (step); lds_wait<false>(cnt, n); } while (0)
#define GBARS(cnt, n, step) do { lds_signal(cnt, lane); n += (step); lds_wait<true>(cnt, n); } while (0)

#define KV_GLDS gldsb_sc1
__device__ __forceinline__ void att_stage_load(const char* kv, int st, unsigned ldsbuf, int w4, int lane) {
    const char* src = kv + (size_t)st * STG_BYTES + w4 * 1024;
#pragma unroll
    for (int i = 0; i < 6; ++i) KV_GLDS(src + i * 4096, lane * 16, ldsbuf + (w4 + 4 * i) * 1024);
}

__device__ __forceinline__ void att_maxpass(const char* kv, char* ldsh, LAS unsigned* bb, unsigned& bn, int kh, int w4, int lane, const bf16x8 (&qm)[2], const bf16x8 (&qt)[2][2], float (&mx)[2]) {
    const unsigned lds0 = (unsigned)(uintptr_t)ldsh;
    const lds_cptr kbase = (lds_cptr)ldsh + lane * 16;
    const f32x16 zero = {};
#pragma unroll 1
    for (int rnd = 0; rnd < 3; ++rnd) {
        if (rnd) { GBAR(bb, bn, 8); att_stage_load(kv, 4 * kh + 2 * (rnd & 1), lds0, w4, lane); att_stage_load(kv, 4 * kh + 2 * (rnd & 1) + 1, lds0 + KVB, w4, lane); }
        VMW(0); GBAR(bb, bn, 8);
        if (rnd < 2) {
#pragma unroll 1
            for (int pr = 0; pr < 8; ++pr) {
                const lds_cptr kb = kbase + (pr >> 2) * KVB; const int p = pr & 3;
                const bf16x8 kt = lds128(kb + STG_KT + p * 1024), k0 = lds128(kb + STG_KM + (2 * p) * 1024), k1 = lds128(kb + STG_KM + (2 * p + 1) * 1024);
#pragma unroll
                for (int h = 0; h < 2; ++h) {
                    f32x16 s0 = MFMA32(k0, qm[h], zero); s0 = MFMA32(kt, qt[h][0], s0);
                    f32x16 s1 = MFMA32(k1, qm[h], zero); s1 = MFMA32(kt, qt[h][1], s1);
#pragma unroll
                    for (int i = 0; i < 16; ++i) mx[h] = fmaxf(mx[h], fmaxf(s0[i], s1[i]));
                }
            }
        }
    }
}

__device__ __forceinline__ void att_main(const char* kv, char* ldsh, LAS unsigned* ctl, rsrc_t rw, int mark_off, int kbase0, int fm_off, bool st1_issued, int kh, int w4, int lane, const bf16x8 (&qm)[2], const bf16x8 (&qt)[2][2], f32x16& O0, f32x16& O1) {
    const unsigned lds0 = (unsigned)(uintptr_t)ldsh;
    LAS unsigned* cdone = ctl + C_DONE + kh; LAS unsigned* cland = ctl + C_LAND + kh;
    const int r = lane & 31, hh = lane >> 5;
    const int voff = r <= 20 ? r * VT_ROW : (r < 28 ? 21 * VT_ROW : 21 * VT_ROW + 112);
    const lds_cptr kbase = (lds_cptr)ldsh + lane * 16;
    const lds_cptr vbase = (lds_cptr)ldsh + STG_VT + voff + hh * 16;
    const f32x16 zero = {};
    f32x16 sA, sB; u32x4 pA0, pA1, pB0, pB1; bf16x8 kmA, kmB, ktA, ktB, vA0, vA1, vB0, vB1;
    ktA = lds128(kbase + STG_KT); kmA = lds128(kbase + STG_KM);
    sA = MFMA32(kmA, qm[0], zero); sA = MFMA32(ktA, qt[0][0], sA);
    pB0 = (u32x4){0u, 0u, 0u, 0u}; pB1 = pB0; vB0 = kmA; vB1 = kmA; ktB = ktA; vA0 = kmA; vA1 = kmA; kmB = kmA;
    float ee0 = 0.f, ee1 = 0.f, ee2 = 0.f, ee3 = 0.f;
#define GRP(MF, LD, SC, I, PD, J) do { MF; SBAR(); LD; const float e0_ = EXP2(SC[I]); unsigned a_ = pk2(ee0, ee1); const float e1_ = EXP2(SC[I + 1]); unsigned b_ = pk2(ee2, ee3); const float e2_ = EXP2(SC[I + 2]), e3_ = EXP2(SC[I + 3]); \
        ee0 = e0_; ee1 = e1_; ee2 = e2_; ee3 = e3_; asm volatile("" : "+v"(a_), "+v"(b_), "+v"(ee0), "+v"(ee1), "+v"(ee2), "+v"(ee3)); PD[J] = a_; PD[J + 1] = b_; SBAR(); } while (0)
#define TILE(KM, KMN, KT, VJ, KTN, VJN, VC0, VC1, VP0, VP1, VADDR, LDK, LDT) do { \
        GRP(sB = MFMA32(KM, qm[1], zero), VC0 = lds128(VADDR), sA, 0, pB1, 2); \
        GRP(O1 = MFMA32(VP0, __builtin_bit_cast(bf16x8, pB0), O1), VC1 = lds128((VADDR) + 32), sA, 4, pA0, 0); \
        GRP(sB = MFMA32(KT, qt[1][VJ], sB), LDK, sA, 8, pA0, 2); \
        GRP(O1 = MFMA32(VP1, __builtin_bit_cast(bf16x8, pB1), O1), LDT, sA, 12, pA1, 0); \
        GRP(sA = MFMA32(KMN, qm[0], zero), (void)0, sB, 0, pA1, 2); \
        GRP(O0 = MFMA32(VC0, __builtin_bit_cast(bf16x8, pA0), O0), (void)0, sB, 4, pB0, 0); \
        GRP(sA = MFMA32(KTN, qt[0][VJN], sA), (void)0, sB, 8, pB0, 2); \
        GRP(O0 = MFMA32(VC1, __builtin_bit_cast(bf16x8, pA1), O0), (void)0, sB, 12, pB1, 0); } while (0)
#pragma unroll 1
    for (int st = 0; st < 4; ++st) {
        if (kh == 1) { if (st == 0) __builtin_amdgcn_s_setprio(1); if (2 * st == 5) __builtin_amdgcn_s_setprio(0); }
        const int buf = st & 1;
        const lds_cptr kb = kbase + buf * KVB, vb = vbase + buf * KVB, kn = kbase + (buf ^ 1) * KVB;
        TILE(kmA, kmB, ktA, 0, ktA, 1, vA0, vA1, vB0, vB1, vb + 0 * 64, kmB = lds128(kb + STG_KM + 1 * 1024), (void)0);
        if (st) { lds_signal(st == 3 ? ctl + C_X : cdone, lane); SBAR(); }
        else if (!st1_issued) { att_stage_load(kv, 4 * kh + 1, lds0 + KVB, w4, lane); SBAR(); }
        TILE(kmB, kmA, ktA, 1, ktB, 0, vB0, vB1, vA0, vA1, vb + 1 * 64, kmA = lds128(kb + STG_KM + 2 * 1024), ktB = lds128(kb + STG_KT + 1024));
        if (st == 0 && kh == 0) {
#pragma unroll 1
            for (int pc = w4; pc < 20; pc += 4) { const int k = kbase0 + 8 * pc + (pc < 10 ? 0 : 560); st128(rw, mark_off + ((k >> 5) * 64 + ((k >> 3) & 3) * 16) * 16, (u32x4){EMPTY, EMPTY, EMPTY, EMPTY}); }
            SBAR(); }
        if (st == 1 || st == 2) { lds_wait(cdone, 4u * st); att_stage_load(kv, 4 * kh + st + 1, lds0 + (buf ^ 1) * KVB, w4, lane); SBAR(); }
        TILE(kmA, kmB, ktB, 0, ktB, 1, vA0, vA1, vB0, vB1, vb + 2 * 64, kmB = lds128(kb + STG_KM + 3 * 1024), (void)0);
        TILE(kmB, kmA, ktB, 1, ktA, 0, vB0, vB1, vA0, vA1, vb + 3 * 64, kmA = lds128(kb + STG_KM + 4 * 1024), ktA = lds128(kb + STG_KT + 2048));
        if (kh == 1 && 2 * st + 1 == 5) __builtin_amdgcn_s_setprio(0);
        TILE(kmA, kmB, ktA, 0, ktA, 1, vA0, vA1, vB0, vB1, vb + 4 * 64, kmB = lds128(kb + STG_KM + 5 * 1024), (void)0);
        TILE(kmB, kmA, ktA, 1, ktB, 0, vB0, vB1, vA0, vA1, vb + 5 * 64, kmA = lds128(kb + STG_KM + 6 * 1024), ktB = lds128(kb + STG_KT + 3072));
        if (st < 3) { VMW(0); lds_signal(cland, lane); SBAR(); }
        TILE(kmA, kmB, ktB, 0, ktB, 1, vA0, vA1, vB0, vB1, vb + 6 * 64, kmB = lds128(kb + STG_KM + 7 * 1024), (void)0);
        if (st < 3) { lds_wait(cland, 4u * (st + 1));
            if (st == 0 && kh == 0 && w4 == 0) { if (lane == 0) st64(rw, fm_off, (u32x2){ctl[C_TAG + 1], ctl[C_TAG + 2]}); lds_signal(ctl + C_MKD, lane); }
            SBAR(); }
        TILE(kmB, kmA, ktB, 1, ktA, 0, vB0, vB1, vA0, vA1, vb + 7 * 64, kmA = lds128(kn + STG_KM + 0 * 1024), ktA = lds128(kn + STG_KT));
    }
    pB1[2] = pk2(ee0, ee1); pB1[3] = pk2(ee2, ee3);
    O1 = MFMA32(vB0, __builtin_bit_cast(bf16x8, pB0), O1);
    O1 = MFMA32(vB1, __builtin_bit_cast(bf16x8, pB1), O1);
#undef GRP
#undef TILE
}

__device__ __forceinline__ int ofrag_off(int s, int k) { return WS_O + ((((s >> 4) * 40 + (k >> 5)) * 64 + ((k >> 3) & 3) * 16 + (s & 15)) * 16 + (k & 7) * 2); }
__device__ __forceinline__ void touch(const void* g, unsigned lds_dummy) {
    unsigned sv; asm volatile("s_mov_b32 %0, m0\n\ts_mov_b32 m0, %2\n\ts_nop 0\n\tglobal_load_lds_dword %1, off\n\ts_mov_b32 m0, %0" : "=&s"(sv) : "v"(g), "s"(lds_dummy) : "memory"); }


__device__ __forceinline__ constexpr int wb_at(int ks) { return ((ks >> 2) == 0 ? AA + 2 * A_SLOT : (ks >> 2) == 1 ? AA : (ks >> 2) == 2 ? AA + A_SLOT : (ks >> 2) == 3 ? C_S3 : C_S4) + (ks & 3) * 5120; }
template <int KS0>
__device__ __forceinline__ bool ob_part(char* lds, LAS unsigned* ctl, rsrc_t rw, int fo, unsigned lane, f32x4 (&cc)[5]) {
    const unsigned i16 = lane & 15, g = lane >> 4, q4 = i16 >> 2, p4 = i16 & 3;
    const lds_cptr cw0 = (lds_cptr)lds + (8 * g + q4 + 4 * (0 ^ (g & 1))) * 160 + p4 * 8, cw1 = (lds_cptr)lds + (8 * g + q4 + 4 * (1 ^ (g & 1))) * 160 + p4 * 8;
    const int fl = lane * 16;
    bf16x8 of[10], wA[5], wB[5]; bool ok = true;
#pragma unroll
    for (int u = 0; u < 10; ++u) of[u] = __builtin_bit_cast(bf16x8, ld128s(rw, fl, fo + (20 + KS0 + u) * 1024));
    lds_wait(ctl + C_WB, 4u);
    if (KS0) lds_wait(ctl + C_WB + 1, 4u);
#define O_READY(n0, N) do { bool got = false; \
        _Pragma("unroll 1") for (unsigned spins = 0; spins < SPIN_CAP; ++spins) { unsigned mx = 0; \
            _Pragma("unroll") for (int u = 0; u < (N); ++u) { const u32x4 v = __builtin_bit_cast(u32x4, of[(n0) + u]); mx = max(max(mx, v[0]), max(max(v[1], v[2]), v[3])); } \
            if (!__any(mx == EMPTY)) { got = true; break; } \
            __builtin_amdgcn_s_sleep(2); \
            _Pragma("unroll") for (int u = 0; u < (N); ++u) of[(n0) + u] = __builtin_bit_cast(bf16x8, ld128s(rw, fl, fo + (20 + KS0 + (n0) + u) * 1024)); } \
        ok &= got; } while (0)
#define O_RD(W, u) do { lds_cptr b0_ = cw0 + wb_at(KS0 + (u)), b1_ = cw1 + wb_at(KS0 + (u)); asm volatile("" : "+v"(b0_), "+v"(b1_)); _Pragma("unroll") for (int j = 0; j < 5; ++j) W[j] = ldstr2(b0_ + j * 32, b1_ + j * 32); SBAR(); } while (0)
#define O_MF(W, u) do { _Pragma("unroll") for (int j = 0; j < 5; ++j) cc[j] = MFMA16(W[j], of[u], cc[j]); asm volatile("" : "+v"(cc[0]), "+v"(cc[1]), "+v"(cc[2]), "+v"(cc[3]), "+v"(cc[4])); SBAR(); } while (0)
    O_RD(wA, 0);
    O_READY(0, 5);
    O_RD(wB, 1); O_MF(wA, 0); O_RD(wA, 2); O_MF(wB, 1); O_RD(wB, 3); O_MF(wA, 2); O_RD(wA, 4); O_MF(wB, 3); O_RD(wB, 5); O_MF(wA, 4);
    O_READY(5, 5);
    O_RD(wA, 6); O_MF(wB, 5); O_RD(wB, 7); O_MF(wA, 6); O_RD(wA, 8); O_MF(wB, 7); O_RD(wB, 9); O_MF(wA, 8); O_MF(wB, 9);
#undef O_READY
#undef O_RD
#undef O_MF
    return ok;
}

__device__ __forceinline__ void b_waves(const Params& P, char* lds, LAS unsigned* ctl, rsrc_t rw, unsigned wid, unsigned lane, unsigned b, unsigned li, unsigned rt64, unsigned c4) {
    char* const ws = P.ws;
    const unsigned lds0 = (unsigned)(uintptr_t)lds;
    const int s0 = 64 * rt64;
    const unsigned i16 = lane & 15, g = lane >> 4, q4 = i16 >> 2, p4 = i16 & 3;
    unsigned bn = 0; LAS unsigned* bb = ctl + C_BB;
    f32x4 qa[5];
#pragma unroll
    for (int j = 0; j < 5; ++j) qa[j] = (f32x4){0.f, 0.f, 0.f, 0.f};
    if (wid >= 4) {
        const unsigned l = wid - 4;
        unsigned off[5];
#pragma unroll
        for (unsigned j = 0; j < 5; ++j) { const unsigned id = l + 4 * j;
            if (j < 2) { const unsigned p = id * 64 + lane, row = p >> 3, ch = (p & 7) ^ ((row >> 1) & 7); off[j] = row * 1280 + ch * 16; }
            else { const unsigned p = ((id < 18 ? id : 8) - 8) * 64 + lane, pos = p / 10, j10 = p - pos * 10, k = pos ^ (4 * ((pos >> 3) & 1)); off[j] = k * 1280 + j10 * 16; } }
        const char* xs = (const char*)(P.x + (size_t)s0 * D); const char* wsq = (const char*)(P.wq + 80 * c4);
        int slot = 0;
        lds_wait<false>(ctl + C_P0I, 4u);
#pragma unroll 1
        for (int t = 0; t < 10; ++t) {
            if (t == 5) { VMW(16); lds_signal(ctl + C_QL + l, lane); }
            if (t >= 5) { VMW(12); lds_signal(ctl + C_QL + l, lane);
                lds_wait4(ctl + C_QD, t - 4); }
            const unsigned so = lds0 + slot * Q_SLOT + l * 1024;
            gldsb(xs, off[0], so); gldsb(xs, off[1], so + 4096); gldsb(wsq, off[2], so + 8192); gldsb(wsq, off[3], so + 12288);
            if (l < 2) gldsb(wsq, off[4], so + 16384);
            xs += 128; wsq += 64 * 1280;
            slot = slot == 4 ? 0 : slot + 1;
        }
        VMW(12); lds_signal(ctl + C_QL + l, lane); VMW(8); lds_signal(ctl + C_QL + l, lane); VMW(4); lds_signal(ctl + C_QL + l, lane); VMW(0); lds_signal(ctl + C_QL + l, lane);
    } else {
        const lds_cptr qx0 = (lds_cptr)lds + (16 * wid + i16) * 128 + ((g ^ (i16 >> 1)) * 16), qx1 = (lds_cptr)lds + (16 * wid + i16) * 128 + (((4 + g) ^ (i16 >> 1)) * 16);
        const lds_cptr qw0 = (lds_cptr)lds + Q_W + (8 * g + q4 + 4 * (0 ^ (g & 1))) * 160 + p4 * 8, qw1 = (lds_cptr)lds + Q_W + (8 * g + q4 + 4 * (1 ^ (g & 1))) * 160 + p4 * 8;
        if (wid < 3) { const unsigned idx = wid * 64 + lane, row = (li >> 1) + 16 * (idx >> 1);
            if (idx < 160) touch((const char*)((row < 640 ? P.woA + (size_t)row * 640 : P.woB + (size_t)(row - 640) * 640) + 80 * c4) + (idx & 1) * 156, lds0 + DUMMY); }
        bf16x8 xa0, xa1, wa[10], xb0, xb1, wb_[10];
#define Q_READ(X0, X1, W, t_, so_) do { lds_wait4(ctl + C_QL, (t_) + 1); X0 = lds128(qx0 + (so_)); X1 = lds128(qx1 + (so_)); \
            _Pragma("unroll") for (int j = 0; j < 10; ++j) W[j] = ldstr2(qw0 + (so_) + (j / 5) * 5120 + (j % 5) * 32, qw1 + (so_) + (j / 5) * 5120 + (j % 5) * 32); SBAR(); } while (0)
#define Q_MUL(X0, X1, W) do { _Pragma("unroll") for (int j = 0; j < 5; ++j) qa[j] = MFMA16(W[j], X0, qa[j]); _Pragma("unroll") for (int j = 0; j < 5; ++j) qa[j] = MFMA16(W[5 + j], X1, qa[j]); \
            asm volatile("" : "+v"(qa[0]), "+v"(qa[1]), "+v"(qa[2]), "+v"(qa[3]), "+v"(qa[4])); SBAR(); } while (0)
#define Q_DONE() do { asm volatile("s_waitcnt lgkmcnt(0)" ::: "memory"); lds_signal(ctl + C_QD + wid, lane); } while (0)
        Q_READ(xa0, xa1, wa, 0, 0); Q_DONE();
        int slot = 1;
#pragma unroll 1
        for (int t = 0; t < 10; t += 2) {
            Q_READ(xb0, xb1, wb_, t + 1, slot * Q_SLOT); slot = slot == 4 ? 0 : slot + 1;
            Q_MUL(xa0, xa1, wa); Q_DONE();
            if (t + 2 < 10) { Q_READ(xa0, xa1, wa, t + 2, slot * Q_SLOT); slot = slot == 4 ? 0 : slot + 1; }
            Q_MUL(xb0, xb1, wb_);
            if (t + 2 < 10) Q_DONE();
        }
#undef Q_READ
#undef Q_MUL
#undef Q_DONE
    }
    if (wid == 4) {
        lds_wait<true>(ctl + C_TAG, 1u);
        const u64 tag = (u64)ctl[C_TAG + 1] | ((u64)ctl[C_TAG + 2] << 32);
        bool ok = false;
#pragma unroll 1
        for (unsigned spins = 0; spins < SPIN_CAP; ++spins) {
            const u32x4 v = ld128(rw, WS_FKV + li * 1024 + lane * 16);
            if (__all((v[0] == (unsigned)tag) & (v[1] == (unsigned)tag) & (v[2] == (unsigned)tag) & (v[3] == (unsigned)tag))) { ok = true; break; }
            __builtin_amdgcn_s_sleep(4);
        }
        if (!ok && lane == 0) ctl[C_BAD] = 1u;
        lds_signal(ctl + C_KVOK, lane);
        if (b == 0 && lane == 0) st64(rw, WS_EPOCH, (u32x2){(unsigned)tag, (unsigned)(tag >> 32)});
    }
    const char* kv = ws + WS_KV;
    const unsigned w4 = wid & 3, kh = wid >> 2;
    char* ldsh = lds + kh * STG_BYTES;
    const unsigned ldsh0 = lds0 + kh * STG_BYTES;
    u32x4 nr0 = {0u, 0u, 0u, 0u}, nr1 = nr0;
    if (wid < 4) {
        lds_wait4(ctl + C_QD, 10u);
#pragma unroll
        for (int j = 0; j < 5; ++j) { const unsigned cq = 4 * j + g, hl = (cq * 13) >> 6, dq = cq - 5 * hl;
            *(LAS u32x2*)((lds_ptr)lds + Q_ST + (16 * wid + i16) * QB_ROW + hl * 48 + dq * 8) = (u32x2){pk2(qa[j][0] * QSCALE, qa[j][1] * QSCALE), pk2(qa[j][2] * QSCALE, qa[j][3] * QSCALE)}; }
        lds_signal(ctl + C_QS, lane);
    } else {
        lds_wait<false>(ctl + C_KVOK, 1u);
        asm volatile("global_load_dwordx4 %0, %2, %3 sc1\n\tglobal_load_dwordx4 %1, %2, %3 offset:1024 sc1" : "=&v"(nr0), "=&v"(nr1) : "v"(lane * 16), "s"(ws + WS_NRM) : "memory");
        lds_wait4(ctl + C_QD, 8u);
        att_stage_load(kv, 0, lds0, w4, lane); att_stage_load(kv, 4, lds0 + STG_BYTES, w4, lane);
    }
    lds_wait<false>(ctl + C_QS, 4u);
    const int r = lane & 31, hh = lane >> 5;
    bf16x8 qm[2]; u32x2 qtl[2];
#pragma unroll
    for (int h = 0; h < 2; ++h) {
        const lds_cptr qp = (lds_cptr)lds + Q_ST + (32 * (w4 & 1) + r) * QB_ROW + (2 * (w4 >> 1) + h) * 48;
        qm[h] = lds128(qp + hh * 16); qtl[h] = *(const LAS u32x2*)(qp + 32);
    }
    float q2[2];
#pragma unroll
    for (int h = 0; h < 2; ++h) {
        q2[h] = 0.f;
#pragma unroll
        for (int i = 0; i < 8; ++i) { const float v = bf2f((u16)qm[h][i]); q2[h] += v * v; }
        if (hh == 0) { const float a0 = lo2f(qtl[h][0]), a1 = hi2f(qtl[h][0]), a2 = lo2f(qtl[h][1]), a3 = hi2f(qtl[h][1]); q2[h] += a0 * a0 + a1 * a1 + a2 * a2 + a3 * a3; }
        q2[h] = sum32(q2[h]);
    }
    if (wid == 7) {
        asm volatile("s_waitcnt vmcnt(12)" : "+v"(nr0), "+v"(nr1) :: "memory");
        const float kmax2 = wave_max_pos(fmaxf(__uint_as_float(nr0[0]), __uint_as_float(nr1[0]))) + wave_max_pos(fmaxf(__uint_as_float(nr0[1]), __uint_as_float(nr1[1])));
        const float vmax = wave_max_pos(fmaxf(fmaxf(__uint_as_float(nr0[2]), __uint_as_float(nr1[2])), fmaxf(__uint_as_float(nr0[3]), __uint_as_float(nr1[3]))));
        if (lane == 0) { ctl[C_NRM] = __float_as_uint(kmax2); ctl[C_NRM + 1] = __float_as_uint(112.f - fmaxf(0.f, __builtin_amdgcn_logf(fmaxf(vmax, 1.f)))); }
    }
    VMW(0);
    asm volatile("" :: "v"(nr0), "v"(nr1));
    GBAR(bb, bn, 8);
    const float kmax2 = __uint_as_float(ctl[C_NRM]), lim = __uint_as_float(ctl[C_NRM + 1]);
    const float bnd0 = __builtin_sqrtf(q2[0] * kmax2) * 1.001f + 1.f, bnd1 = __builtin_sqrtf(q2[1] * kmax2) * 1.001f + 1.f;
    const u32x4 z4 = {0u, 0u, 0u, 0u};
    bf16x8 qt[2][2];
#pragma unroll
    for (int h = 0; h < 2; ++h) { const u32x4 a = {qtl[h][0], qtl[h][1], 0u, 0u}; qt[h][0] = __builtin_bit_cast(bf16x8, hh ? z4 : a); qt[h][1] = __builtin_bit_cast(bf16x8, hh ? a : z4); }
    const f32x16 zero = {};
    float m[2]; bool unsafe = false;
    { const lds_cptr kb = (lds_cptr)ldsh + lane * 16; const bf16x8 k0 = lds128(kb + STG_KM), kt = lds128(kb + STG_KT);
#pragma unroll
      for (int h = 0; h < 2; ++h) {
        f32x16 sc = MFMA32(k0, qm[h], zero); sc = MFMA32(kt, qt[h][0], sc);
        float mm = sc[0];
#pragma unroll
        for (int i = 1; i < 16; ++i) mm = fmaxf(mm, sc[i]);
        mm = max32(mm);
        m[h] = bf2f(f2bf(mm));
        unsafe |= !((h ? bnd1 : bnd0) - m[h] <= lim);
      } }
    if (__any(unsafe) && lane == 0) ctl[C_FALL] = 1u;
    GBAR(bb, bn, 8);
    bool st1_issued = false;
    if (rfl(ctl[C_FALL]) != 0u) {
        att_stage_load(kv, 4 * kh + 1, ldsh0 + KVB, w4, lane); st1_issued = true;
        att_maxpass(kv, ldsh, bb, bn, kh, w4, lane, qm, qt, m);
#pragma unroll
        for (int h = 0; h < 2; ++h) m[h] = bf2f(f2bf(max32(m[h])));
    }
#pragma unroll
    for (int h = 0; h < 2; ++h) { const unsigned nm = f2bf(-m[h]); const u32x4 a = {qtl[h][0], qtl[h][1], nm, 0u}; qt[h][0] = __builtin_bit_cast(bf16x8, hh ? z4 : a); qt[h][1] = __builtin_bit_cast(bf16x8, hh ? a : z4); }
    f32x16 O[2] = {{}, {}};
    att_main(kv, ldsh, ctl, rw, ofrag_off(s0 + lane, 0), 80 * c4, WS_FA + (rt64 * 8 + c4) * 8, st1_issued, kh, w4, lane, qm, qt, O[0], O[1]);
    lds_signal(ctl + C_BD, lane);
    __builtin_amdgcn_s_setprio(3);
    {
        LAS f32x4* xo = (LAS f32x4*)((lds_ptr)lds + (2 * w4 + kh) * 4096) + lane;
        LAS f32x4* xi = (LAS f32x4*)((lds_ptr)lds + (2 * w4 + (kh ^ 1)) * 4096) + lane;
        const f32x16& Og = kh ? O[0] : O[1]; const f32x16& Ok = kh ? O[1] : O[0];
        const int s = s0 + 32 * (w4 & 1) + r, k0 = 640 + (4 * c4 + 2 * (w4 >> 1) + kh) * DH;
        const int a0 = ofrag_off(s, k0 + 4 * hh), a1 = ofrag_off(s, k0 + 8 + 4 * hh), a2 = ofrag_off(s, k0 + 16);
        lds_wait<false>(ctl + C_X, 8u);
        xo[0] = (f32x4){Og[0], Og[1], Og[2], Og[3]}; xo[64] = (f32x4){Og[4], Og[5], Og[6], Og[7]}; xo[128] = (f32x4){Og[8], Og[9], Og[10], Og[11]};
        xo[192] = (f32x4){kh ? m[0] : m[1], 0.f, 0.f, 0.f};
        lds_signal(ctl + C_P + 2 * w4 + kh, lane);
        lds_wait<false>(ctl + C_P + 2 * w4 + (kh ^ 1), 1u);
        const f32x4 p0 = xi[0], p1 = xi[64], p2 = xi[128]; const float m1 = xi[192][0];
        const float mo = kh ? m[1] : m[0];
        const float M = fmaxf(mo, m1), f0 = EXP2(mo - M), f1 = EXP2(m1 - M);
        float o[12];
#pragma unroll
        for (int i = 0; i < 4; ++i) { o[i] = Ok[i] * f0 + p0[i] * f1; o[4 + i] = Ok[4 + i] * f0 + p1[i] * f1; o[8 + i] = Ok[8 + i] * f0 + p2[i] * f1; }
        auto lr = __builtin_amdgcn_permlane32_swap(__float_as_uint(o[8]), __float_as_uint(o[8]), false, false);
        const float inv = __builtin_amdgcn_rcpf(__uint_as_float(lr[1]));
#define NE(x) ((x) == EMPTY ? 0x7fc07fc0u : (x))
        const unsigned w0 = pk2(o[0] * inv, o[1] * inv), w1 = pk2(o[2] * inv, o[3] * inv), w2 = pk2(o[4] * inv, o[5] * inv), w3 = pk2(o[6] * inv, o[7] * inv), w4_ = pk2(o[8] * inv, o[9] * inv), w5 = pk2(o[10] * inv, o[11] * inv);
        st64(rw, a0, (u32x2){NE(w0), NE(w1)});
        st64(rw, a1, (u32x2){NE(w2), NE(w3)});
        if (hh == 0) st64(rw, a2, (u32x2){NE(w4_), NE(w5)});
#undef NE
        lds_signal(ctl + C_D, lane);
    }
    if (kh == 1) {
        lds_wait<false>(ctl + C_BD, 8u);
#pragma unroll
        for (unsigned i = 0; i < 5; ++i) { const unsigned p = 256 * i + w4 * 64 + lane, pos = p / 10, j10 = p - pos * 10, off = (pos ^ (4 * ((pos >> 3) & 1))) * 1280 + j10 * 16;
            const char* sb = (const char*)(P.woB + 80 * c4) + 3 * (128 * 1280);
            gldsb(sb, off, rfl(lds0 + C_S3 + (4 * i + w4) * 1024)); gldsb(sb + 128 * 1280, off, rfl(lds0 + C_S4 + (4 * i + w4) * 1024)); }
        VMW(0); lds_signal(ctl + C_WB + 1, lane);
    }
    if (kh == 0) {
        f32x4 cc[5];
#pragma unroll
        for (int j = 0; j < 5; ++j) cc[j] = (f32x4){0.f, 0.f, 0.f, 0.f};
        lds_wait(ctl + C_D, 8u);
        __builtin_amdgcn_s_sleep(6);
        const bool ok = ob_part<10>(lds, ctl, rw, WS_O + (4 * rt64 + w4) * 40 * 1024, lane, cc);
        if (!ok) ctl[C_BAD] = 1u;
        LAS f32x4* red = (LAS f32x4*)((lds_ptr)lds + w4 * 5120) + lane;
#pragma unroll
        for (int j = 0; j < 5; ++j) red[j * 64] = cc[j];
        lds_signal(ctl + C_R + w4, lane);
    }
    asm volatile("s_waitcnt lgkmcnt(0)" ::: "memory");
}

__device__ __forceinline__ void a_waves(const Params& P, char* lds, LAS unsigned* ctl, rsrc_t rw, unsigned a, unsigned lane, unsigned b, unsigned li, unsigned rt64, unsigned c4) {
    const unsigned lds0 = (unsigned)(uintptr_t)lds;
    const int s0 = 64 * rt64;
    const unsigned i16 = lane & 15, g = lane >> 4, q4 = i16 >> 2, p4 = i16 & 3;
    unsigned an = 0; LAS unsigned* ab = ctl + C_AB;
    const unsigned part = c4 & 1, r0 = s0 + 16 * (c4 >> 1);
    {
        if (a == 3) { u64 pc; asm volatile("s_getpc_b64 %0" : "=s"(pc)); const unsigned ln = li * 10 + (lane < 10 ? lane : 0); touch((const char*)(pc & ~127ull) + (ln < CODE_LINES ? ln : 0) * 128, lds0 + DUMMY); }
    }
    { const char* wsrc = (const char*)(part ? P.wv : P.wk); const char* xsrc = (const char*)(P.x + (size_t)r0 * D);
#pragma unroll 1
      for (unsigned i = 0; i < 12; ++i) { unsigned id = a + 4 * i; if (id == 47) id = 43;
        if (id < 25) gldsb(wsrc, id * 1024 + lane * 16, lds0 + P0W + id * 1024);
        else if (id < 45) { const unsigned p = (id - 25) * 64 + lane, row = p / 80, cpos = p - row * 80, ch = (cpos & ~15u) | ((cpos & 15) ^ row);
            gldsb(xsrc, row * 1280 + ch * 16, lds0 + P0X + (id - 25) * 1024); }
        else { const unsigned off = (id - 45) * 1024 + lane * 16; gldsb(P.gw, off < 1280 ? off : 1264, lds0 + P0G + (id - 45) * 1024); } } }
    lds_signal(ctl + C_P0I, lane);
    const u32x2 ep = ld64(rw, WS_EPOCH);
    const u64 tag = ((u64)ep[0] | ((u64)ep[1] << 32)) + 0x9E3779B97F4A7C15ull;
    const u32x2 tagv = {(unsigned)tag, (unsigned)(tag >> 32)};
    VMW(0);
    GBARS(ab, an, 4);
    if (a < 2 || (a == 2 && part == 0)) {
        const lds_cptr p0x = (lds_cptr)lds + P0X + i16 * 1280;
        f32x4 pacc = {0.f, 0.f, 0.f, 0.f};
#define P0XF(ks) lds128(p0x + ((((4 * (ks) + g) & ~15u) | (((4 * (ks) + g) & 15) ^ i16)) * 16))
        const int ks0 = r0 & 255, img = WS_KV + (r0 >> 8) * STG_BYTES, nrm = WS_NRM + (r0 >> 4) * 16;
        if (a == 2) {
            const bf16x8 z = {0, 0, 0, 0, 0, 0, 0, 0};
#pragma unroll
            for (int ks = 0; ks < 20; ++ks) { const bf16x8 gv = lds128((lds_cptr)lds + P0G + (32 * ks + 8 * g) * 2); pacc = MFMA16(i16 == 0 ? gv : z, P0XF(ks), pacc); }
            if (g == 0) st32(rw, WS_G + (r0 + i16) * 4, __float_as_uint(1.f / (1.f + __expf(-pacc[0]))));
        } else {
            const lds_cptr p0w = (lds_cptr)lds + P0W + (8 * g + q4) * 40 + (16 * a + 4 * p4) * 2;
            if (part == 0) {
#pragma unroll
                for (int ks = 0; ks < 20; ++ks) pacc = MFMA16(ldstr2(p0w + ks * 1280, p0w + ks * 1280 + 160), P0XF(ks), pacc);
                const unsigned w0 = pk2(pacc[0], pacc[1]), w1 = pk2(pacc[2], pacc[3]);
                const int key = ks0 + i16, tile = key >> 5, r = swap23(key & 31);
                float n2 = lo2f(w0) * lo2f(w0) + hi2f(w0) * hi2f(w0) + lo2f(w1) * lo2f(w1) + hi2f(w1) * hi2f(w1);
                if (a == 0) { st64(rw, img + STG_KM + tile * 1024 + (g >> 1) * 512 + r * 16 + (g & 1) * 8, (u32x2){w0, w1});
                    n2 = sum16(n2); n2 = sum32(n2); }
                else { if (g == 0) st128(rw, img + STG_KT + (tile >> 1) * 1024 + (tile & 1) * 512 + r * 16, (u32x4){w0, w1, 0x3f80u, 0u}); if (g != 0) n2 = 0.f; }
                n2 = wave_max_pos(n2);
                if (lane == 0) st32(rw, nrm + a * 4, __float_as_uint(n2));
            } else {
#pragma unroll
                for (int ks = 0; ks < 20; ++ks) pacc = MFMA16(P0XF(ks), ldstr2(p0w + ks * 1280, p0w + ks * 1280 + 160), pacc);
                const unsigned w0 = pk2(pacc[0], pacc[1]), w1 = pk2(pacc[2], pacc[3]);
                const int d = 16 * a + i16;
                float vm = fmaxf(fmaxf(fabsf(lo2f(w0)), fabsf(hi2f(w0))), fmaxf(fabsf(lo2f(w1)), fabsf(hi2f(w1))));
                if (d >= 20) vm = 0.f;
                if (d <= 20) st64(rw, img + STG_VT + d * VT_ROW + (ks0 + 4 * g) * 2, d == 20 ? (u32x2){0x3f803f80u, 0x3f803f80u} : (u32x2){w0, w1});
                if (a == 1 && ks0 == 0 && lane < 40) st128(rw, img + STG_VT + 21 * VT_ROW + lane * 16, (u32x4){0u, 0u, 0u, 0u});
                vm = wave_max_pos(vm);
                if (lane == 0) st32(rw, nrm + (2 + a) * 4, __float_as_uint(vm));
            }
        }
#undef P0XF
        VMW(0);
    }
    GBARS(ab, an, 4);
    bool ok = true;
    if (a == 3) { if (lane < 32) st32(rw, WS_FKV + lane * 1024 + b * 4, tagv[0]);
        if (lane == 0) { ctl[C_TAG + 1] = tagv[0]; ctl[C_TAG + 2] = tagv[1]; }
        lds_signal(ctl + C_TAG, lane);
    }
    unsigned aoff[5];
#pragma unroll
    for (unsigned j = 0; j < 5; ++j) { const unsigned id = a + 4 * j;
        if (id < 15) { const unsigned seg = id / 5, p = (id - 5 * seg) * 64 + lane, pos = p / 10, j10 = p - pos * 10, k = pos ^ (4 * ((pos >> 3) & 1)); aoff[j] = k * 3840 + seg * 1280 + j10 * 16; }
        else { const unsigned p = (id - 15) * 64 + lane, row = p >> 2, ch = (p & 3) ^ (3 * ((row >> 2) & 1)); int gr = s0 - 16 + (int)row; gr = gr < 0 ? 0 : gr; aoff[j] = gr * 1280 + ch * 16; } }
    const char* awb = (const char*)(P.wqkv + 80 * c4); const char* axb = (const char*)P.x;
#define A_ISSUE(slot) do { gldsb(awb, aoff[0], lds0 + AA + (slot) * A_SLOT + a * 1024); gldsb(awb, aoff[1], lds0 + AA + (slot) * A_SLOT + (a + 4) * 1024); gldsb(awb, aoff[2], lds0 + AA + (slot) * A_SLOT + (a + 8) * 1024); \
        gldsb(a < 3 ? awb : axb, aoff[3], lds0 + AA + (slot) * A_SLOT + (a + 12) * 1024); gldsb(axb, aoff[4], lds0 + AA + (slot) * A_SLOT + (a + 16) * 1024); awb += 32 * 3840; axb += 64; } while (0)
    lds_wait4<true>(ctl + C_QL, 8u);
    A_ISSUE(0); A_ISSUE(1);
    f32x4 acc[5][4];
#pragma unroll
    for (int i = 0; i < 5; ++i)
#pragma unroll
        for (int j = 0; j < 4; ++j) acc[i][j] = (f32x4){0.f, 0.f, 0.f, 0.f};
    const lds_cptr ax = (lds_cptr)lds + AA + A_X + i16 * 64 + ((g ^ (3 * ((i16 >> 2) & 1))) * 16);
    lds_cptr aw0[4], aw1[4];
#pragma unroll
    for (unsigned j = 0; j < 4; ++j) { unsigned jt = 4 * a + j; jt = jt < 15 ? jt : 14; const unsigned sg = jt / 5, wi = jt - 5 * sg;
        aw0[j] = (lds_cptr)lds + AA + sg * 5120 + wi * 32 + (8 * g + q4 + 4 * (0 ^ (g & 1))) * 160 + p4 * 8; aw1[j] = (lds_cptr)lds + AA + sg * 5120 + wi * 32 + (8 * g + q4 + 4 * (1 ^ (g & 1))) * 160 + p4 * 8; }
    VMW(5); lds_signal(ab, lane); an += 4;
    { int slot = 0;
#pragma unroll 1
      for (int t = 0; t < 20; ++t) {
        lds_wait<true>(ab, an);
        const int so = slot * A_SLOT, fs = slot == 0 ? 2 : slot - 1;
        bf16x8 xf[5], w0 = ldstr2(aw0[0] + so, aw1[0] + so), w1;
#pragma unroll
        for (int i = 0; i < 5; ++i) xf[i] = lds128(ax + so + i * 1024);
        w1 = ldstr2(aw0[1] + so, aw1[1] + so);
        SBAR();
#define A_MF(i, j, w) acc[i][j] = MFMA16(w, xf[i], acc[i][j])
#define A_LD(j) do { if (t + 2 < 20) gldsb((j) < 3 || ((j) == 3 && a < 3) ? awb : axb, aoff[j], lds0 + AA + fs * A_SLOT + (a + 4 * (j)) * 1024); } while (0)
        A_MF(0, 0, w0); A_LD(0); A_MF(1, 0, w0); A_MF(2, 0, w0); A_LD(1); A_MF(3, 0, w0); A_MF(4, 0, w0); SBAR();
        w0 = ldstr2(aw0[2] + so, aw1[2] + so);
        A_MF(0, 1, w1); A_LD(2); A_MF(1, 1, w1); A_MF(2, 1, w1); A_LD(3); A_MF(3, 1, w1); A_MF(4, 1, w1); SBAR();
        w1 = ldstr2(aw0[3] + so, aw1[3] + so);
        A_MF(0, 2, w0); A_LD(4); A_MF(1, 2, w0); A_MF(2, 2, w0); SBAR();
        awb += 32 * 3840; axb += 64;
        if (t < 18) VMW(5); else VMW(0);
        lds_signal(ab, lane); an += 4;
        A_MF(3, 2, w0); A_MF(4, 2, w0);
        A_MF(0, 3, w1); A_MF(1, 3, w1); A_MF(2, 3, w1); A_MF(3, 3, w1); A_MF(4, 3, w1); SBAR();
        slot = slot == 2 ? 0 : slot + 1;
      } }
#undef A_MF
#undef A_LD
    ok &= poll_tags<1>(rw, WS_FA + (rt64 * 8 + (lane & 7)) * 8, lane < 8, tag);
    lds_wait<true>(ab, an);
    { lds_ptr st = (lds_ptr)lds;
#pragma unroll
      for (unsigned j = 0; j < 4; ++j) { const unsigned jt = 4 * a + j, sg = jt / 5, wi = jt - 5 * sg, cq = 4 * wi + g, hl = (cq * 13) >> 6, dq = cq - 5 * hl;
        if (jt < 15) {
#pragma unroll
            for (int i = 0; i < 5; ++i) { const int row = 16 * i + i16; const f32x4 v = acc[i][j];
                if (sg < 2) *(LAS u32x2*)(st + ST_QK + row * QK_ROW + sg * 192 + hl * 48 + dq * 8) = (u32x2){pk2(v[0], v[1]), pk2(v[2], v[3])};
                else *(LAS f32x4*)(st + ST_V + row * V_ROW + hl * 80 + dq * 16) = v; } } } }
    GBARS(ab, an, 4);
    {
        const int sl = lane, row = 16 + sl, s = s0 + sl;
        const lds_cptr qp = (lds_cptr)lds + ST_QK + row * QK_ROW + a * 48;
        const lds_cptr kp = qp + 192 - 14 * QK_ROW;
        const u32x4 qa = *(const LAS u32x4*)qp, qb = *(const LAS u32x4*)(qp + 16); const u32x2 qc = *(const LAS u32x2*)(qp + 32);
        u32x4 ka[8], kb[8]; u32x2 kc[8];
#pragma unroll
        for (int w = 0; w < 8; ++w) { ka[w] = *(const LAS u32x4*)(kp + (14 - 2 * w) * QK_ROW); kb[w] = *(const LAS u32x4*)(kp + (14 - 2 * w) * QK_ROW + 16); kc[w] = *(const LAS u32x2*)(kp + (14 - 2 * w) * QK_ROW + 32); }
        SBAR();
        const lds_cptr vp = (lds_cptr)lds + ST_V + (row - 14) * V_ROW + a * 80;
        float lg[8]; float m = -3.0e38f;
#pragma unroll
        for (int w = 0; w < 8; ++w) {
            float x = dot2(qa[0], ka[w][0], 0.f), x2 = dot2(qa[1], ka[w][1], 0.f);
            x = dot2(qa[2], ka[w][2], x); x2 = dot2(qa[3], ka[w][3], x2); x = dot2(qb[0], kb[w][0], x); x2 = dot2(qb[1], kb[w][1], x2);
            x = dot2(qb[2], kb[w][2], x); x2 = dot2(qb[3], kb[w][3], x2); x = dot2(qc[0], kc[w][0], x); x2 = dot2(qc[1], kc[w][1], x2);
            x = (x + x2) * QSCALE;
            lg[w] = (s - 2 * w >= 0) ? x : -1.0e9f * LOG2E; m = fmaxf(m, lg[w]);
        }
        f32x4 va[3][5], vb[3][5];
#define LV(dst, w0, n) do { _Pragma("unroll") for (int w = 0; w < (n); ++w) _Pragma("unroll") for (int j = 0; j < 5; ++j) dst[w][j] = *(const LAS f32x4*)(vp + (14 - 2 * ((w0) + w)) * V_ROW + j * 16); SBAR(); } while (0)
#define PIN5(o) asm volatile("" : "+v"(o[0]), "+v"(o[1]), "+v"(o[2]), "+v"(o[3]), "+v"(o[4]))
        LV(va, 0, 3);
        float l = 0.f;
#pragma unroll
        for (int w = 0; w < 8; ++w) { lg[w] = EXP2(lg[w] - m); l += lg[w]; }
        const float inv = __builtin_amdgcn_rcpf(l);
        LV(vb, 3, 3);
        f32x4 o[5];
#pragma unroll
        for (int j = 0; j < 5; ++j) o[j] = va[0][j] * lg[0] + va[1][j] * lg[1] + va[2][j] * lg[2];
        PIN5(o); SBAR();
        LV(va, 6, 2);
#pragma unroll
        for (int j = 0; j < 5; ++j) o[j] += vb[0][j] * lg[3] + vb[1][j] * lg[4] + vb[2][j] * lg[5];
        PIN5(o); SBAR();
#pragma unroll
        for (int j = 0; j < 5; ++j) o[j] += va[0][j] * lg[6] + va[1][j] * lg[7];
#undef LV
#undef PIN5
        lds_wait(ctl + C_MKD, 1u);
#pragma unroll
        for (int j = 0; j < 5; ++j) { const unsigned w0 = pk2(o[j][0] * inv, o[j][1] * inv), w1 = pk2(o[j][2] * inv, o[j][3] * inv);
            st64(rw, ofrag_off(s, (4 * c4 + a) * DH + 4 * j), (u32x2){w0 == EMPTY ? 0x7fc07fc0u : w0, w1 == EMPTY ? 0x7fc07fc0u : w1}); }
    }
    GBARS(ab, an, 4);
    unsigned coff[5];
#pragma unroll
    for (unsigned i = 0; i < 5; ++i) { const unsigned p = 256 * i + a * 64 + lane, pos = p / 10, j10 = p - pos * 10; coff[i] = (pos ^ (4 * ((pos >> 3) & 1))) * 1280 + j10 * 16; }
    auto c_issue = [&](const u16* W, unsigned chunk, unsigned dst) {
        const char* sb = (const char*)(W + 80 * c4) + (size_t)chunk * (128 * 1280);
#pragma unroll
        for (unsigned i = 0; i < 5; ++i) gldsb(sb, coff[i], lds0 + dst + (4 * i + a) * 1024);
    };
    c_issue(P.woA, 0, AA); c_issue(P.woA, 1, AA + A_SLOT); c_issue(P.woA, 2, AA + 2 * A_SLOT);
    const int fo = WS_O + (4 * rt64 + a) * 40 * 1024, fl = lane * 16;
    bf16x8 of[20];
#pragma unroll
    for (int ks = 0; ks < 20; ++ks) of[ks] = __builtin_bit_cast(bf16x8, ld128s(rw, fl, fo + ks * 1024));
    const float gg = __uint_as_float(ld32(rw, WS_G + (s0 + 16 * a + i16) * 4));
    const unsigned cbo = (8 * g + q4) * 160 + p4 * 8, ct0 = 4 * (0 ^ (g & 1)) * 160, ct1 = 4 * (1 ^ (g & 1)) * 160;
    const lds_cptr cw0 = (lds_cptr)lds + cbo + ct0, cw1 = (lds_cptr)lds + cbo + ct1;
    f32x4 cc[5]; bf16x8 wA[5], wB[5];
#define C_READY(F, n0, N) do { \
        bool got = false; \
        _Pragma("unroll 1") for (unsigned spins = 0; spins < SPIN_CAP; ++spins) { \
            unsigned mx = 0; \
            _Pragma("unroll") for (int u = 0; u < (N); ++u) { const u32x4 v = __builtin_bit_cast(u32x4, of[(n0) + u]); mx = max(max(mx, v[0]), max(max(v[1], v[2]), v[3])); } \
            if (!__any(mx == EMPTY)) { got = true; break; } \
            __builtin_amdgcn_s_sleep(2); \
            _Pragma("unroll") for (int u = 0; u < (N); ++u) of[(n0) + u] = __builtin_bit_cast(bf16x8, ld128s(rw, fl, fo + ((F) + (n0) + u) * 1024)); \
        } \
        ok &= got; } while (0)
#define C_RD(W, off) do { lds_cptr b0_ = cw0 + (off), b1_ = cw1 + (off); asm volatile("" : "+v"(b0_), "+v"(b1_)); _Pragma("unroll") for (int j = 0; j < 5; ++j) W[j] = ldstr2(b0_ + j * 32, b1_ + j * 32); SBAR(); } while (0)
#define C_MF(W, ks) do { _Pragma("unroll") for (int j = 0; j < 5; ++j) cc[j] = MFMA16(W[j], of[ks], cc[j]); asm volatile("" : "+v"(cc[0]), "+v"(cc[1]), "+v"(cc[2]), "+v"(cc[3]), "+v"(cc[4])); SBAR(); } while (0)
#define C_CHUNK(base, ks0, next) do { C_RD(wB, (base) + 5120); C_MF(wA, (ks0)); C_RD(wA, (base) + 10240); C_MF(wB, (ks0) + 1); C_RD(wB, (base) + 15360); C_MF(wA, (ks0) + 2); \
        if ((next) >= 0) C_RD(wA, (next) >= 0 ? (next) : 0); C_MF(wB, (ks0) + 3); } while (0)
#pragma unroll
    for (int j = 0; j < 5; ++j) cc[j] = (f32x4){0.f, 0.f, 0.f, 0.f};
    C_READY(0, 0, 10); C_READY(0, 10, 10);
    VMW(0); GBARS(ab, an, 4);
    C_RD(wA, AA); C_CHUNK(AA, 0, AA + A_SLOT);
    GBARS(ab, an, 4); c_issue(P.woA, 3, AA);
    C_CHUNK(AA + A_SLOT, 4, AA + 2 * A_SLOT);
    GBARS(ab, an, 4); c_issue(P.woA, 4, AA + A_SLOT);
    C_CHUNK(AA + 2 * A_SLOT, 8, -1);
    VMW(5); GBARS(ab, an, 4); c_issue(P.woB, 0, AA + 2 * A_SLOT);
    C_RD(wA, AA); C_CHUNK(AA, 12, -1);
    VMW(5); GBARS(ab, an, 4); c_issue(P.woB, 1, AA);
    C_RD(wA, AA + A_SLOT); C_CHUNK(AA + A_SLOT, 16, -1);
    u32x2 cA[5];
#pragma unroll
    for (int j = 0; j < 5; ++j) { cA[j] = (u32x2){pk2(cc[j][0], cc[j][1]), pk2(cc[j][2], cc[j][3])}; cc[j] = (f32x4){0.f, 0.f, 0.f, 0.f}; }
    LAS u32x2* park = (LAS u32x2*)((lds_ptr)lds + 36864 + a * 2560) + lane;
    GBARS(ab, an, 4);
    c_issue(P.woB, 2, AA + A_SLOT);
    VMW(0); lds_signal(ctl + C_WB, lane);
    __builtin_amdgcn_s_setprio(3);
    lds_wait(ctl + C_D, 8u);
#pragma unroll
    for (int j = 0; j < 5; ++j) park[j * 64] = cA[j];
    __builtin_amdgcn_s_sleep(6);
    ok &= ob_part<0>(lds, ctl, rw, fo, lane, cc);
#undef C_READY
#undef C_RD
#undef C_MF
#undef C_CHUNK
    if (!ok) ctl[C_BAD] = 1u;
    lds_wait<false>(ctl + C_R + a, 1u);
    { LAS f32x4* red = (LAS f32x4*)((lds_ptr)lds + a * 5120) + lane;
#pragma unroll
      for (int j = 0; j < 5; ++j) cc[j] += red[j * 64]; }
    const float poison = (rfl(ctl[C_BAD]) || !ok) ? __builtin_nanf("") : 0.f;
    unsigned ln = __builtin_amdgcn_mbcnt_hi(~0u, __builtin_amdgcn_mbcnt_lo(~0u, 0u)); asm volatile("" : "+v"(ln));
#pragma unroll
    for (int j = 0; j < 5; ++j) { const u32x2 pa = *((LAS u32x2*)((lds_ptr)lds + 36864 + a * 2560) + ln + j * 64); const f32x4 av = {lo2f(pa[0]), hi2f(pa[0]), lo2f(pa[1]), hi2f(pa[1])}; const f32x4 v = av * gg + cc[j] * (1.f - gg) + poison;
        *(u32x2*)(P.out + (size_t)(s0 + 16 * a + (ln & 15)) * 640 + 80 * c4 + 16 * j + 4 * (ln >> 4)) = (u32x2){pk2(v[0], v[1]), pk2(v[2], v[3])}; }
}

__global__ __launch_bounds__(768, 3) void fused(Params P) {
    extern __shared__ __attribute__((aligned(16))) char lds[];
    char* const ws = P.ws;
    const unsigned tid = threadIdx.x, wid = rfl(tid >> 6);
    const unsigned lane = __builtin_amdgcn_mbcnt_hi(~0u, __builtin_amdgcn_mbcnt_lo(~0u, 0u));
    const unsigned b = blockIdx.x & 255, xg = b & 7, li = b >> 3;
    const unsigned rt64 = (xg >> 2) * 16 + (li >> 1), c4 = (xg & 3) * 2 + (li & 1);
    const rsrc_t rw = __builtin_amdgcn_make_buffer_rsrc(ws, 0, WS_END, 0x00020000);
    LAS unsigned* ctl = (LAS unsigned*)((lds_ptr)lds + CTL);
    if (tid < 64) ctl[tid] = 0u;
    LBAR();
    if (wid < 8) b_waves(P, lds, ctl, rw, wid & 7, lane, b, li, rt64, c4);
    else { __builtin_amdgcn_s_setprio(2); a_waves(P, lds, ctl, rw, wid & 3, lane, b, li, rt64, c4); }
}

static inline Params mk_params(void* const* d_in, void* d_out, void* d_ws) {
    Params P; P.x = (const u16*)d_in[0]; P.wqkv = (const u16*)d_in[1]; P.woA = (const u16*)d_in[2]; P.wq = (const u16*)d_in[4]; P.wk = (const u16*)d_in[5]; P.wv = (const u16*)d_in[6];
    P.woB = (const u16*)d_in[7]; P.gw = (const u16*)d_in[8]; P.ws = (char*)d_ws; P.out = (u16*)d_out; return P; }
extern "C" void kernel_launch(void* const* d_in, const int* in_sizes, int n_in, void* d_out, int out_size, void* d_ws, size_t ws_size, hipStream_t stream) {
    (void)hipFuncSetAttribute((const void*)fused, hipFuncAttributeMaxDynamicSharedMemorySize, LDS_TOTAL);
    fused<<<256, 768, LDS_TOTAL, stream>>>(mk_params(d_in, d_out, d_ws));
}
```

```cpp
#include <hip/hip_runtime.h>
#include <stdint.h>

typedef unsigned short u16;
typedef unsigned long long u64;
typedef short bf16x8 __attribute__((ext_vector_type(8)));
typedef short s16x4 __attribute__((ext_vector_type(4)));
typedef float f32x16 __attribute__((ext_vector_type(16)));
typedef float f32x4 __attribute__((ext_vector_type(4)));
typedef float f32x2 __attribute__((ext_vector_type(2)));
typedef unsigned u32x4 __attribute__((ext_vector_type(4)));
typedef unsigned u32x2 __attribute__((ext_vector_type(2)));
typedef __bf16 bf2_t __attribute__((ext_vector_type(2)));
#define LAS __attribute__((address_space(3)))
typedef LAS const char* lds_cptr;
typedef LAS char* lds_ptr;
typedef __amdgpu_buffer_rsrc_t rsrc_t;

__device__ __forceinline__ float bf2f(u16 b) { return __uint_as_float(((unsigned)b) << 16); }
__device__ __forceinline__ unsigned pk2(float lo, float hi) { f32x2 v = {lo, hi}; bf2_t r = __builtin_convertvector(v, bf2_t); return __builtin_bit_cast(unsigned, r); }
__device__ __forceinline__ u16 f2bf(float f) { return (u16)(pk2(f, 0.f) & 0xffffu); }
__device__ __forceinline__ float lo2f(unsigned u) { return __uint_as_float(u << 16); }
__device__ __forceinline__ float hi2f(unsigned u) { return __uint_as_float(u & 0xffff0000u); }
__device__ __forceinline__ float dot2(unsigned a, unsigned b, float c) { return __builtin_amdgcn_fdot2_f32_bf16(__builtin_bit_cast(bf2_t, a), __builtin_bit_cast(bf2_t, b), c, false); }

constexpr int S = 2048, D = 640, H = 32, DH = 20;
constexpr float SCALE = 0.22360679774997896f, LOG2E = 1.4426950408889634f;
constexpr float QSCALE = SCALE * LOG2E;

constexpr int STG_KM = 0, STG_KT = 8192, STG_VT = 12288, VT_ROW = 528, STG_BYTES = 24576;
constexpr int WS_FKV_AT = 262144;
constexpr int WS_EPOCH = 0;
constexpr int WS_FKV = WS_FKV_AT;
constexpr int WS_FO = 256;
constexpr int WS_FA = WS_FO + 2048;
constexpr int WS_NRM = 8192;
constexpr int WS_G = WS_NRM + 2048;
constexpr int WS_KV = 32768;
static_assert(WS_KV + 8 * STG_BYTES <= WS_FKV_AT, "map");
constexpr int WS_O = WS_FKV_AT + 32768;
constexpr int WS_END = WS_O + S * 1280 * 2;
constexpr int AUX_SC1 = 16;

#define MFMA32(a, b, c) __builtin_amdgcn_mfma_f32_32x32x16_bf16((a), (b), (c), 0, 0, 0)
#define MFMA16(a, b, c) __builtin_amdgcn_mfma_f32_16x16x32_bf16((a), (b), (c), 0, 0, 0)
#define SBAR() __builtin_amdgcn_sched_barrier(0)
#define EXP2(x) __builtin_amdgcn_exp2f(x)
#define LBAR() asm volatile("s_waitcnt lgkmcnt(0)\n\ts_barrier" ::: "memory")
__device__ __forceinline__ float wave_max_pos(float f) {
    unsigned x = __float_as_uint(f);
    x = max(x, (unsigned)__builtin_amdgcn_update_dpp(0, (int)x, 0x111, 0xf, 0xf, false));
    x = max(x, (unsigned)__builtin_amdgcn_update_dpp(0, (int)x, 0x112, 0xf, 0xf, false));
    x = max(x, (unsigned)__builtin_amdgcn_update_dpp(0, (int)x, 0x114, 0xf, 0xf, false));
    x = max(x, (unsigned)__builtin_amdgcn_update_dpp(0, (int)x, 0x118, 0xf, 0xf, false));
    x = max(x, (unsigned)__builtin_amdgcn_update_dpp(0, (int)x, 0x142, 0xa, 0xf, false));
    x = max(x, (unsigned)__builtin_amdgcn_update_dpp(0, (int)x, 0x143, 0xc, 0xf, false));
    return __uint_as_float((unsigned)__builtin_amdgcn_readlane((int)x, 63));
}
__device__ __forceinline__ float sum16(float f) { auto r = __builtin_amdgcn_permlane16_swap(__float_as_uint(f), __float_as_uint(f), false, false); return __uint_as_float(r[0]) + __uint_as_float(r[1]); }
__device__ __forceinline__ float sum32(float f) { auto r = __builtin_amdgcn_permlane32_swap(__float_as_uint(f), __float_as_uint(f), false, false); return __uint_as_float(r[0]) + __uint_as_float(r[1]); }
__device__ __forceinline__ float max32(float f) { auto r = __builtin_amdgcn_permlane32_swap(__float_as_uint(f), __float_as_uint(f), false, false); return fmaxf(__uint_as_float(r[0]), __uint_as_float(r[1])); }
__device__ __forceinline__ int swap23(int x) { return (x & ~12) | ((x & 4) << 1) | ((x & 8) >> 1); }
__device__ __forceinline__ void glds16(const void* g, unsigned lds_base) {
    unsigned sv; asm volatile("s_mov_b32 %0, m0\n\ts_mov_b32 m0, %2\n\ts_nop 0\n\tglobal_load_lds_dwordx4 %1, off\n\ts_mov_b32 m0, %0" : "=&s"(sv) : "v"(g), "s"(lds_base) : "memory"); }
__device__ __forceinline__ void gldsb(const void* sbase, unsigned voff, unsigned lds_base) {
    unsigned sv; asm volatile("s_mov_b32 %0, m0\n\ts_mov_b32 m0, %3\n\ts_nop 0\n\tglobal_load_lds_dwordx4 %1, %2\n\ts_mov_b32 m0, %0" : "=&s"(sv) : "v"(voff), "s"(sbase), "s"(lds_base) : "memory"); }
__device__ __forceinline__ void gldsb_sc1(const void* sbase, unsigned voff, unsigned lds_base) {
    unsigned sv; asm volatile("s_mov_b32 %0, m0\n\ts_mov_b32 m0, %3\n\ts_nop 0\n\tglobal_load_lds_dwordx4 %1, %2 sc1\n\ts_mov_b32 m0, %0" : "=&s"(sv) : "v"(voff), "s"(sbase), "s"(lds_base) : "memory"); }
__device__ __forceinline__ bf16x8 lds128(lds_cptr p) { return *(const LAS bf16x8*)p; }
typedef short v4i16_t __attribute__((ext_vector_type(4)));
__device__ __forceinline__ s16x4 ldstr(lds_cptr p) { return __builtin_bit_cast(s16x4, __builtin_amdgcn_ds_read_tr16_b64_v4i16((LAS v4i16_t*)p)); }
__device__ __forceinline__ bf16x8 ldstr2(lds_cptr p0, lds_cptr p1) { const s16x4 a = ldstr(p0), b = ldstr(p1); return __builtin_shufflevector(a, b, 0, 1, 2, 3, 4, 5, 6, 7); }
__device__ __forceinline__ unsigned rfl(unsigned x) { return (unsigned)__builtin_amdgcn_readfirstlane(x); }
__device__ __forceinline__ void st32(rsrc_t r, int off, unsigned v) { __builtin_amdgcn_raw_buffer_store_b32(v, r, off, 0, AUX_SC1); }
__device__ __forceinline__ void st64(rsrc_t r, int off, u32x2 v) { __builtin_amdgcn_raw_buffer_store_b64(v, r, off, 0, AUX_SC1); }
__device__ __forceinline__ void st128(rsrc_t r, int off, u32x4 v) { __builtin_amdgcn_raw_buffer_store_b128(v, r, off, 0, AUX_SC1); }
__device__ __forceinline__ unsigned ld32(rsrc_t r, int off) { return __builtin_amdgcn_raw_buffer_load_b32(r, off, 0, AUX_SC1); }
__device__ __forceinline__ u32x2 ld64(rsrc_t r, int off) { return __builtin_amdgcn_raw_buffer_load_b64(r, off, 0, AUX_SC1); }
__device__ __forceinline__ u32x4 ld128(rsrc_t r, int off) { return __builtin_amdgcn_raw_buffer_load_b128(r, off, 0, AUX_SC1); }
__device__ __forceinline__ u32x4 ld128s(rsrc_t r, int voff, int soff) { return __builtin_amdgcn_raw_buffer_load_b128(r, voff, soff, AUX_SC1); }


struct Params { const u16 *x, *wqkv, *woA, *wq, *wk, *wv, *woB, *gw; char* ws; u16* out; };

constexpr int LDS_TOTAL = 163840, CTL = LDS_TOTAL - 256, DUMMY = CTL - 256;
constexpr int KVB = 2 * STG_BYTES;
constexpr int Q_SLOT = 18432, Q_W = 8192, Q_ST = 73728, QB_ROW = 208;
constexpr int AA = 98304;
constexpr int P0W = AA, P0X = AA + 25664, P0G = P0X + 20480;
constexpr int A_SLOT = 20480, A_X = 15360;
constexpr int ST_QK = AA, ST_V = AA + 32000, QK_ROW = 400, V_ROW = 336;
constexpr int C_S3 = 49152, C_S4 = 69632;
constexpr int C_FALL = 0, C_BB = 1, C_DONE = 2, C_LAND = 4, C_X = 6, C_BD = 7, C_D = 12, C_AB = 13, C_KVOK = 16, C_BAD = 17, C_P0I = 18, C_QL = 20, C_QD = 24, C_TAG = 28, C_QS = 19, C_MKD = 15, C_P = 32, C_WB = 40, C_NRM = 42, C_P0L = 14, C_R = 44;
constexpr unsigned CODE_LINES = 296;
constexpr unsigned EMPTY = 0xFFFFFFFFu;
constexpr unsigned SPIN_CAP = 1u << 18;

template <int N, int SLEEP = 4>
__device__ __forceinline__ bool poll_tags(rsrc_t r, int off, bool active, u64 tag) {
#pragma unroll 1
    for (unsigned spins = 0; spins < SPIN_CAP; ++spins) {
        bool ok = true;
        if (active) {
#pragma unroll
            for (int i = 0; i < N; ++i) { const u32x2 v = ld64(r, off + 8 * i); ok &= (v[0] == (unsigned)tag) & (v[1] == (unsigned)(tag >> 32)); }
        }
        if (__all(ok)) return true;
        __builtin_amdgcn_s_sleep(SLEEP);
    }
    return false;
}
__device__ __forceinline__ void lds_signal(LAS unsigned* cnt, int lane) {
    (void)lane;
    __builtin_amdgcn_sched_barrier(0);
    asm volatile("s_mov_b64 exec, 1\n\tds_add_u32 %0, %1\n\ts_mov_b64 exec, -1\n\ts_nop 3" :: "v"((unsigned)(uintptr_t)cnt), "v"(1u) : "memory");
    __builtin_amdgcn_sched_barrier(0); }
template <bool SLEEP = true>
__device__ __forceinline__ void lds_wait(LAS unsigned* cnt, unsigned n) {
    unsigned v, sv, left;
    if (SLEEP)
        asm volatile("s_mov_b32 %2, 0x100000\n1:\n\tds_read_b32 %0, %3\n\ts_waitcnt lgkmcnt(0)\n\tv_readfirstlane_b32 %1, %0\n\ts_cmp_ge_u32 %1, %4\n\ts_cbranch_scc1 2f\n\ts_sleep 1\n\ts_sub_u32 %2, %2, 1\n\ts_cbranch_scc0 1b\n2:"
                     : "=&v"(v), "=&s"(sv), "=&s"(left) : "v"((unsigned)(uintptr_t)cnt), "s"(n) : "memory", "scc");
    else
        asm volatile("s_mov_b32 %2, 0x100000\n1:\n\tds_read_b32 %0, %3\n\ts_waitcnt lgkmcnt(0)\n\tv_readfirstlane_b32 %1, %0\n\ts_cmp_ge_u32 %1, %4\n\ts_cbranch_scc1 2f\n\ts_sub_u32 %2, %2, 1\n\ts_cbranch_scc0 1b\n2:"
                     : "=&v"(v), "=&s"(sv), "=&s"(left) : "v"((unsigned)(uintptr_t)cnt), "s"(n) : "memory", "scc");
}
template <bool SLEEP = false>
__device__ __forceinline__ void lds_wait4(LAS unsigned* cnt, unsigned n) {
#pragma unroll 1
    for (unsigned spins = 0; spins < (1u << 20); ++spins) {
        const u32x4 v = *(volatile LAS u32x4*)cnt;
        const unsigned lo = min(min(v[0], v[1]), min(v[2], v[3]));
        if (rfl(lo) >= n) break;
        if (SLEEP) __builtin_amdgcn_s_sleep(1);
    }
    asm volatile("" ::: "memory");
}
#define VMW(N) asm volatile("s_waitcnt vmcnt(" #N ")" ::: "memory")
#define GBAR(cnt, n, step) do { lds_signal(cnt, lane); n += (step); lds_wait<false>(cnt, n); } while (0)
#define GBARS(cnt, n, step) do { lds_signal(cnt, lane); n += (step); lds_wait<true>(cnt, n); } while (0)

#define KV_GLDS gldsb_sc1
__device__ __forceinline__ void att_stage_load(const char* kv, int st, unsigned ldsbuf, int w4, int lane) {
    const char* src = kv + (size_t)st * STG_BYTES + w4 * 1024;
#pragma unroll
    for (int i = 0; i < 6; ++i) KV_GLDS(src + i * 4096, lane * 16, ldsbuf + (w4 + 4 * i) * 1024);
}

__device__ __forceinline__ void att_maxpass(const char* kv, char* ldsh, LAS unsigned* bb, unsigned& bn, int kh, int w4, int lane, const bf16x8 (&qm)[2], const bf16x8 (&qt)[2][2], float (&mx)[2]) {
    const unsigned lds0 = (unsigned)(uintptr_t)ldsh;
    const lds_cptr kbase = (lds_cptr)ldsh + lane * 16;
    const f32x16 zero = {};
#pragma unroll 1
    for (int rnd = 0; rnd < 3; ++rnd) {
        if (rnd) { GBAR(bb, bn, 8); att_stage_load(kv, 4 * kh + 2 * (rnd & 1), lds0, w4, lane); att_stage_load(kv, 4 * kh + 2 * (rnd & 1) + 1, lds0 + KVB, w4, lane); }
        VMW(0); GBAR(bb, bn, 8);
        if (rnd < 2) {
#pragma unroll 1
            for (int pr = 0; pr < 8; ++pr) {
                const lds_cptr kb = kbase + (pr >> 2) * KVB; const int p = pr & 3;
                const bf16x8 kt = lds128(kb + STG_KT + p * 1024), k0 = lds128(kb + STG_KM + (2 * p) * 1024), k1 = lds128(kb + STG_KM + (2 * p + 1) * 1024);
#pragma unroll
                for (int h = 0; h < 2; ++h) {
                    f32x16 s0 = MFMA32(k0, qm[h], zero); s0 = MFMA32(kt, qt[h][0], s0);
                    f32x16 s1 = MFMA32(k1, qm[h], zero); s1 = MFMA32(kt, qt[h][1], s1);
#pragma unroll
                    for (int i = 0; i < 16; ++i) mx[h] = fmaxf(mx[h], fmaxf(s0[i], s1[i]));
                }
            }
        }
    }
}

__device__ __forceinline__ void att_main(const char* kv, char* ldsh, LAS unsigned* ctl, rsrc_t rw, int mark_off, int kbase0, int fm_off, bool st1_issued, int kh, int w4, int lane, const bf16x8 (&qm)[2], const bf16x8 (&qt)[2][2], f32x16& O0, f32x16& O1) {
    const unsigned lds0 = (unsigned)(uintptr_t)ldsh;
    LAS unsigned* cdone = ctl + C_DONE + kh; LAS unsigned* cland = ctl + C_LAND + kh;
    const int r = lane & 31, hh = lane >> 5;
    const int voff = r <= 20 ? r * VT_ROW : (r < 28 ? 21 * VT_ROW : 21 * VT_ROW + 112);
    const lds_cptr kbase = (lds_cptr)ldsh + lane * 16;
    const lds_cptr vbase = (lds_cptr)ldsh + STG_VT + voff + hh * 16;
    const f32x16 zero = {};
    f32x16 sA, sB; u32x4 pA0, pA1, pB0, pB1; bf16x8 kmA, kmB, ktA, ktB, vA0, vA1, vB0, vB1;
    ktA = lds128(kbase + STG_KT); kmA = lds128(kbase + STG_KM);
    sA = MFMA32(kmA, qm[0], zero); sA = MFMA32(ktA, qt[0][0], sA);
    pB0 = (u32x4){0u, 0u, 0u, 0u}; pB1 = pB0; vB0 = kmA; vB1 = kmA; ktB = ktA; vA0 = kmA; vA1 = kmA; kmB = kmA;
    float ee0 = 0.f, ee1 = 0.f, ee2 = 0.f, ee3 = 0.f;
#define GRP(MF, LD, SC, I, PD, J) do { MF; SBAR(); LD; const float e0_ = EXP2(SC[I]); unsigned a_ = pk2(ee0, ee1); const float e1_ = EXP2(SC[I + 1]); unsigned b_ = pk2(ee2, ee3); const float e2_ = EXP2(SC[I + 2]), e3_ = EXP2(SC[I + 3]); \
        ee0 = e0_; ee1 = e1_; ee2 = e2_; ee3 = e3_; asm volatile("" : "+v"(a_), "+v"(b_), "+v"(ee0), "+v"(ee1), "+v"(ee2), "+v"(ee3)); PD[J] = a_; PD[J + 1] = b_; SBAR(); } while (0)
#define TILE(KM, KMN, KT, VJ, KTN, VJN, VC0, VC1, VP0, VP1, VADDR, LDK, LDT) do { \
        GRP(sB = MFMA32(KM, qm[1], zero), VC0 = lds128(VADDR), sA, 0, pB1, 2); \
        GRP(O1 = MFMA32(VP0, __builtin_bit_cast(bf16x8, pB0), O1), VC1 = lds128((VADDR) + 32), sA, 4, pA0, 0); \
        GRP(sB = MFMA32(KT, qt[1][VJ], sB), LDK, sA, 8, pA0, 2); \
        GRP(O1 = MFMA32(VP1, __builtin_bit_cast(bf16x8, pB1), O1), LDT, sA, 12, pA1, 0); \
        GRP(sA = MFMA32(KMN, qm[0], zero), (void)0, sB, 0, pA1, 2); \
        GRP(O0 = MFMA32(VC0, __builtin_bit_cast(bf16x8, pA0), O0), (void)0, sB, 4, pB0, 0); \
        GRP(sA = MFMA32(KTN, qt[0][VJN], sA), (void)0, sB, 8, pB0, 2); \
        GRP(O0 = MFMA32(VC1, __builtin_bit_cast(bf16x8, pA1), O0), (void)0, sB, 12, pB1, 0); } while (0)
#pragma unroll 1
    for (int st = 0; st < 4; ++st) {
        if (kh == 1) { if (st == 0) __builtin_amdgcn_s_setprio(1); if (2 * st == 5) __builtin_amdgcn_s_setprio(0); }
        const int buf = st & 1;
        const lds_cptr kb = kbase + buf * KVB, vb = vbase + buf * KVB, kn = kbase + (buf ^ 1) * KVB;
        TILE(kmA, kmB, ktA, 0, ktA, 1, vA0, vA1, vB0, vB1, vb + 0 * 64, kmB = lds128(kb + STG_KM + 1 * 1024), (void)0);
        if (st) { lds_signal(st == 3 ? ctl + C_X : cdone, lane); SBAR(); }
        else if (!st1_issued) { att_stage_load(kv, 4 * kh + 1, lds0 + KVB, w4, lane); SBAR(); }
        TILE(kmB, kmA, ktA, 1, ktB, 0, vB0, vB1, vA0, vA1, vb + 1 * 64, kmA = lds128(kb + STG_KM + 2 * 1024), ktB = lds128(kb + STG_KT + 1024));
        if (st == 0 && kh == 0) {
#pragma unroll 1
            for (int pc = w4; pc < 20; pc += 4) { const int k = kbase0 + 8 * pc + (pc < 10 ? 0 : 560); st128(rw, mark_off + ((k >> 5) * 64 + ((k >> 3) & 3) * 16) * 16, (u32x4){EMPTY, EMPTY, EMPTY, EMPTY}); }
            SBAR(); }
        if (st == 1 || st == 2) { lds_wait(cdone, 4u * st); att_stage_load(kv, 4 * kh + st + 1, lds0 + (buf ^ 1) * KVB, w4, lane); SBAR(); }
        TILE(kmA, kmB, ktB, 0, ktB, 1, vA0, vA1, vB0, vB1, vb + 2 * 64, kmB = lds128(kb + STG_KM + 3 * 1024), (void)0);
        TILE(kmB, kmA, ktB, 1, ktA, 0, vB0, vB1, vA0, vA1, vb + 3 * 64, kmA = lds128(kb + STG_KM + 4 * 1024), ktA = lds128(kb + STG_KT + 2048));
        if (kh == 1 && 2 * st + 1 == 5) __builtin_amdgcn_s_setprio(0);
        TILE(kmA, kmB, ktA, 0, ktA, 1, vA0, vA1, vB0, vB1, vb + 4 * 64, kmB = lds128(kb + STG_KM + 5 * 1024), (void)0);
        TILE(kmB, kmA, ktA, 1, ktB, 0, vB0, vB1, vA0, vA1, vb + 5 * 64, kmA = lds128(kb + STG_KM + 6 * 1024), ktB = lds128(kb + STG_KT + 3072));
        if (st < 3) { VMW(0); lds_signal(cland, lane); SBAR(); }
        TILE(kmA, kmB, ktB, 0, ktB, 1, vA0, vA1, vB0, vB1, vb + 6 * 64, kmB = lds128(kb + STG_KM + 7 * 1024), (void)0);
        if (st < 3) { lds_wait(cland, 4u * (st + 1));
            if (st == 0 && kh == 0 && w4 == 0) { if (lane == 0) st64(rw, fm_off, (u32x2){ctl[C_TAG + 1], ctl[C_TAG + 2]}); lds_signal(ctl + C_MKD, lane); }
            SBAR(); }
        TILE(kmB, kmA, ktB, 1, ktA, 0, vB0, vB1, vA0, vA1, vb + 7 * 64, kmA = lds128(kn + STG_KM + 0 * 1024), ktA = lds128(kn + STG_KT));
    }
    pB1[2] = pk2(ee0, ee1); pB1[3] = pk2(ee2, ee3);
    O1 = MFMA32(vB0, __builtin_bit_cast(bf16x8, pB0), O1);
    O1 = MFMA32(vB1, __builtin_bit_cast(bf16x8, pB1), O1);
#undef GRP
#undef TILE
}

__device__ __forceinline__ int ofrag_off(int s, int k) { return WS_O + ((((s >> 4) * 40 + (k >> 5)) * 64 + ((k >> 3) & 3) * 16 + (s & 15)) * 16 + (k & 7) * 2); }
__device__ __forceinline__ void touch(const void* g, unsigned lds_dummy) {
    unsigned sv; asm volatile("s_mov_b32 %0, m0\n\ts_mov_b32 m0, %2\n\ts_nop 0\n\tglobal_load_lds_dword %1, off\n\ts_mov_b32 m0, %0" : "=&s"(sv) : "v"(g), "s"(lds_dummy) : "memory"); }


__device__ __forceinline__ constexpr int wb_at(int ks) { return ((ks >> 2) == 0 ? AA + 2 * A_SLOT : (ks >> 2) == 1 ? AA : (ks >> 2) == 2 ? AA + A_SLOT : (ks >> 2) == 3 ? C_S3 : C_S4) + (ks & 3) * 5120; }
template <int KS0>
__device__ __forceinline__ bool ob_part(char* lds, LAS unsigned* ctl, rsrc_t rw, int fo, unsigned lane, f32x4 (&cc)[5]) {
    const unsigned i16 = lane & 15, g = lane >> 4, q4 = i16 >> 2, p4 = i16 & 3;
    const lds_cptr cw0 = (lds_cptr)lds + (8 * g + q4 + 4 * (0 ^ (g & 1))) * 160 + p4 * 8, cw1 = (lds_cptr)lds + (8 * g + q4 + 4 * (1 ^ (g & 1))) * 160 + p4 * 8;
    const int fl = lane * 16;
    bf16x8 of[10], wA[5], wB[5]; bool ok = true;
#pragma unroll
    for (int u = 0; u < 10; ++u) of[u] = __builtin_bit_cast(bf16x8, ld128s(rw, fl, fo + (20 + KS0 + u) * 1024));
    lds_wait(ctl + C_WB, 4u);
    if (KS0) lds_wait(ctl + C_WB + 1, 4u);
#define O_READY(n0, N) do { bool got = false; \
        _Pragma("unroll 1") for (unsigned spins = 0; spins < SPIN_CAP; ++spins) { unsigned mx = 0; \
            _Pragma("unroll") for (int u = 0; u < (N); ++u) { const u32x4 v = __builtin_bit_cast(u32x4, of[(n0) + u]); mx = max(max(mx, v[0]), max(max(v[1], v[2]), v[3])); } \
            if (!__any(mx == EMPTY)) { got = true; break; } \
            __builtin_amdgcn_s_sleep(2); \
            _Pragma("unroll") for (int u = 0; u < (N); ++u) of[(n0) + u] = __builtin_bit_cast(bf16x8, ld128s(rw, fl, fo + (20 + KS0 + (n0) + u) * 1024)); } \
        ok &= got; } while (0)
#define O_RD(W, u) do { lds_cptr b0_ = cw0 + wb_at(KS0 + (u)), b1_ = cw1 + wb_at(KS0 + (u)); asm volatile("" : "+v"(b0_), "+v"(b1_)); _Pragma("unroll") for (int j = 0; j < 5; ++j) W[j] = ldstr2(b0_ + j * 32, b1_ + j * 32); SBAR(); } while (0)
#define O_MF(W, u) do { _Pragma("unroll") for (int j = 0; j < 5; ++j) cc[j] = MFMA16(W[j], of[u], cc[j]); asm volatile("" : "+v"(cc[0]), "+v"(cc[1]), "+v"(cc[2]), "+v"(cc[3]), "+v"(cc[4])); SBAR(); } while (0)
    O_RD(wA, 0);
    O_READY(0, 5);
    O_RD(wB, 1); O_MF(wA, 0); O_RD(wA, 2); O_MF(wB, 1); O_RD(wB, 3); O_MF(wA, 2); O_RD(wA, 4); O_MF(wB, 3); O_RD(wB, 5); O_MF(wA, 4);
    O_READY(5, 5);
    O_RD(wA, 6); O_MF(wB, 5); O_RD(wB, 7); O_MF(wA, 6); O_RD(wA, 8); O_MF(wB, 7); O_RD(wB, 9); O_MF(wA, 8); O_MF(wB, 9);
#undef O_READY
#undef O_RD
#undef O_MF
    return ok;
}

__device__ __forceinline__ void p0_issue(const Params& P, unsigned lds0, unsigned pw, unsigned lane, unsigned part, unsigned r0) {
    const char* wsrc = (const char*)(part ? P.wv : P.wk); const char* xsrc = (const char*)(P.x + (size_t)r0 * D);
#pragma unroll 1
    for (unsigned i = 0; i < 6; ++i) { unsigned id = pw + 8 * i; if (id == 47) id = 39;
        if (id < 25) gldsb(wsrc, id * 1024 + lane * 16, lds0 + P0W + id * 1024);
        else if (id < 45) { const unsigned p = (id - 25) * 64 + lane, row = p / 80, cpos = p - row * 80, ch = (cpos & ~15u) | ((cpos & 15) ^ row);
            gldsb(xsrc, row * 1280 + ch * 16, lds0 + P0X + (id - 25) * 1024); }
        else { const unsigned off = (id - 45) * 1024 + lane * 16; gldsb(P.gw, off < 1280 ? off : 1264, lds0 + P0G + (id - 45) * 1024); } }
}

__device__ __forceinline__ void b_waves(const Params& P, char* lds, LAS unsigned* ctl, rsrc_t rw, unsigned wid, unsigned lane, unsigned b, unsigned li, unsigned rt64, unsigned c4) {
    char* const ws = P.ws;
    const unsigned lds0 = (unsigned)(uintptr_t)lds;
    const int s0 = 64 * rt64;
    const unsigned i16 = lane & 15, g = lane >> 4, q4 = i16 >> 2, p4 = i16 & 3;
    unsigned bn = 0; LAS unsigned* bb = ctl + C_BB;
    f32x4 qa[6];
#pragma unroll
    for (int j = 0; j < 6; ++j) qa[j] = (f32x4){0.f, 0.f, 0.f, 0.f};
    if (wid >= 4) {
        const unsigned l = wid - 4;
        unsigned off[5];
#pragma unroll
        for (unsigned j = 0; j < 5; ++j) { const unsigned id = l + 4 * j;
            if (j < 2) { const unsigned p = id * 64 + lane, row = p >> 3, ch = (p & 7) ^ ((row >> 1) & 7); off[j] = row * 1280 + ch * 16; }
            else { const unsigned p = ((id < 18 ? id : 8) - 8) * 64 + lane, pos = p / 10, j10 = p - pos * 10, k = pos ^ (4 * ((pos >> 3) & 1)); off[j] = k * 1280 + j10 * 16; } }
        const char* xs = (const char*)(P.x + (size_t)s0 * D); const char* wsq = (const char*)(P.wq + 80 * c4);
        int slot = 0;
        lds_wait<false>(ctl + C_P0I, 8u);
#pragma unroll 1
        for (int t = 0; t < 10; ++t) {
            if (t == 5) { VMW(16); lds_signal(ctl + C_QL + l, lane); }
            if (t >= 5) { VMW(12); lds_signal(ctl + C_QL + l, lane);
                lds_wait4(ctl + C_QD, t - 4); }
            const unsigned so = lds0 + slot * Q_SLOT + l * 1024;
            gldsb(xs, off[0], so); gldsb(xs, off[1], so + 4096); gldsb(wsq, off[2], so + 8192); gldsb(wsq, off[3], so + 12288);
            if (l < 2) gldsb(wsq, off[4], so + 16384);
            xs += 128; wsq += 64 * 1280;
            slot = slot == 4 ? 0 : slot + 1;
        }
        VMW(12); lds_signal(ctl + C_QL + l, lane); VMW(8); lds_signal(ctl + C_QL + l, lane); VMW(4); lds_signal(ctl + C_QL + l, lane); VMW(0); lds_signal(ctl + C_QL + l, lane);
    } else {
        p0_issue(P, lds0, 4 + wid, lane, c4 & 1, s0 + 16 * (c4 >> 1));
        lds_signal(ctl + C_P0I, lane);
        VMW(0); lds_signal(ctl + C_P0L, lane);
        const unsigned rp = wid & 1, cgq = wid >> 1;
        const lds_cptr qx0 = (lds_cptr)lds + (32 * rp + i16) * 128 + ((g ^ (i16 >> 1)) * 16), qx1 = (lds_cptr)lds + (32 * rp + i16) * 128 + (((4 + g) ^ (i16 >> 1)) * 16);
        const lds_cptr qw0 = (lds_cptr)lds + Q_W + (8 * g + q4 + 4 * (0 ^ (g & 1))) * 160 + p4 * 8 + cgq * 96, qw1 = (lds_cptr)lds + Q_W + (8 * g + q4 + 4 * (1 ^ (g & 1))) * 160 + p4 * 8 + cgq * 96;
        if (wid < 3) { const unsigned idx = wid * 64 + lane, row = (li >> 1) + 16 * (idx >> 1);
            if (idx < 160) touch((const char*)((row < 640 ? P.woA + (size_t)row * 640 : P.woB + (size_t)(row - 640) * 640) + 80 * c4) + (idx & 1) * 156, lds0 + DUMMY); }
        bf16x8 xa[4], wa[6], xb[4], wb_[6];
#define Q_READ(X, W, t_, so_) do { lds_wait4(ctl + C_QL, (t_) + 1); X[0] = lds128(qx0 + (so_)); X[1] = lds128(qx0 + (so_) + 2048); X[2] = lds128(qx1 + (so_)); X[3] = lds128(qx1 + (so_) + 2048); \
            _Pragma("unroll") for (int j = 0; j < 6; ++j) W[j] = ldstr2(qw0 + (so_) + (j / 3) * 5120 + (j % 3) * 32, qw1 + (so_) + (j / 3) * 5120 + (j % 3) * 32); SBAR(); } while (0)
#define Q_MUL(X, W) do { _Pragma("unroll") for (int j = 0; j < 3; ++j) { qa[j] = MFMA16(W[j], X[0], qa[j]); qa[3 + j] = MFMA16(W[j], X[1], qa[3 + j]); } \
            _Pragma("unroll") for (int j = 0; j < 3; ++j) { qa[j] = MFMA16(W[3 + j], X[2], qa[j]); qa[3 + j] = MFMA16(W[3 + j], X[3], qa[3 + j]); } \
            asm volatile("" : "+v"(qa[0]), "+v"(qa[1]), "+v"(qa[2]), "+v"(qa[3]), "+v"(qa[4]), "+v"(qa[5])); SBAR(); } while (0)
#define Q_DONE() do { asm volatile("s_waitcnt lgkmcnt(0)" ::: "memory"); lds_signal(ctl + C_QD + wid, lane); } while (0)
        __builtin_amdgcn_s_setprio(3);
        Q_READ(xa, wa, 0, 0); Q_DONE();
        int slot = 1;
#pragma unroll 1
        for (int t = 0; t < 10; t += 2) {
            Q_READ(xb, wb_, t + 1, slot * Q_SLOT); slot = slot == 4 ? 0 : slot + 1;
            Q_MUL(xa, wa); Q_DONE();
            if (t + 2 < 10) { Q_READ(xa, wa, t + 2, slot * Q_SLOT); slot = slot == 4 ? 0 : slot + 1; }
            Q_MUL(xb, wb_);
            if (t + 2 < 10) Q_DONE();
        }
#undef Q_READ
#undef Q_MUL
#undef Q_DONE
    }
    if (wid == 4) {
        lds_wait<true>(ctl + C_TAG, 1u);
        const u64 tag = (u64)ctl[C_TAG + 1] | ((u64)ctl[C_TAG + 2] << 32);
        bool ok = false;
#pragma unroll 1
        for (unsigned spins = 0; spins < SPIN_CAP; ++spins) {
            const u32x4 v = ld128(rw, WS_FKV + li * 1024 + lane * 16);
            if (__all((v[0] == (unsigned)tag) & (v[1] == (unsigned)tag) & (v[2] == (unsigned)tag) & (v[3] == (unsigned)tag))) { ok = true; break; }
            __builtin_amdgcn_s_sleep(4);
        }
        if (!ok && lane == 0) ctl[C_BAD] = 1u;
        lds_signal(ctl + C_KVOK, lane);
        if (b == 0 && lane == 0) st64(rw, WS_EPOCH, (u32x2){(unsigned)tag, (unsigned)(tag >> 32)});
    }
    const char* kv = ws + WS_KV;
    const unsigned w4 = wid & 3, kh = wid >> 2;
    char* ldsh = lds + kh * STG_BYTES;
    const unsigned ldsh0 = lds0 + kh * STG_BYTES;
    u32x4 nr0 = {0u, 0u, 0u, 0u}, nr1 = nr0;
    if (wid < 4) {
        lds_wait4(ctl + C_QD, 10u);
#pragma unroll
        for (int j = 0; j < 6; ++j) { const unsigned jt = 3 * (wid >> 1) + j % 3, cq = 4 * jt + g, hl = (cq * 13) >> 6, dq = cq - 5 * hl;
            if (jt < 5) *(LAS u32x2*)((lds_ptr)lds + Q_ST + (32 * (wid & 1) + 16 * (j / 3) + i16) * QB_ROW + hl * 48 + dq * 8) = (u32x2){pk2(qa[j][0] * QSCALE, qa[j][1] * QSCALE), pk2(qa[j][2] * QSCALE, qa[j][3] * QSCALE)}; }
        lds_signal(ctl + C_QS, lane);
    } else {
        __builtin_amdgcn_s_setprio(3);
        lds_wait<false>(ctl + C_KVOK, 1u);
        asm volatile("global_load_dwordx4 %0, %2, %3 sc1\n\tglobal_load_dwordx4 %1, %2, %3 offset:1024 sc1" : "=&v"(nr0), "=&v"(nr1) : "v"(lane * 16), "s"(ws + WS_NRM) : "memory");
        lds_wait4(ctl + C_QD, 8u);
        att_stage_load(kv, 0, lds0, w4, lane); att_stage_load(kv, 4, lds0 + STG_BYTES, w4, lane);
    }
    lds_wait<false>(ctl + C_QS, 4u);
    const int r = lane & 31, hh = lane >> 5;
    bf16x8 qm[2]; u32x2 qtl[2];
#pragma unroll
    for (int h = 0; h < 2; ++h) {
        const lds_cptr qp = (lds_cptr)lds + Q_ST + (32 * (w4 & 1) + r) * QB_ROW + (2 * (w4 >> 1) + h) * 48;
        qm[h] = lds128(qp + hh * 16); qtl[h] = *(const LAS u32x2*)(qp + 32);
    }
    float q2[2];
#pragma unroll
    for (int h = 0; h < 2; ++h) {
        q2[h] = 0.f;
#pragma unroll
        for (int i = 0; i < 8; ++i) { const float v = bf2f((u16)qm[h][i]); q2[h] += v * v; }
        if (hh == 0) { const float a0 = lo2f(qtl[h][0]), a1 = hi2f(qtl[h][0]), a2 = lo2f(qtl[h][1]), a3 = hi2f(qtl[h][1]); q2[h] += a0 * a0 + a1 * a1 + a2 * a2 + a3 * a3; }
        q2[h] = sum32(q2[h]);
    }
    if (wid == 7) {
        asm volatile("s_waitcnt vmcnt(12)" : "+v"(nr0), "+v"(nr1) :: "memory");
        const float kmax2 = wave_max_pos(fmaxf(__uint_as_float(nr0[0]), __uint_as_float(nr1[0]))) + wave_max_pos(fmaxf(__uint_as_float(nr0[1]), __uint_as_float(nr1[1])));
        const float vmax = wave_max_pos(fmaxf(fmaxf(__uint_as_float(nr0[2]), __uint_as_float(nr1[2])), fmaxf(__uint_as_float(nr0[3]), __uint_as_float(nr1[3]))));
        if (lane == 0) { ctl[C_NRM] = __float_as_uint(kmax2); ctl[C_NRM + 1] = __float_as_uint(112.f - fmaxf(0.f, __builtin_amdgcn_logf(fmaxf(vmax, 1.f)))); }
    }
    VMW(0);
    asm volatile("" :: "v"(nr0), "v"(nr1));
    GBAR(bb, bn, 8);
    const float kmax2 = __uint_as_float(ctl[C_NRM]), lim = __uint_as_float(ctl[C_NRM + 1]);
    const float bnd0 = __builtin_sqrtf(q2[0] * kmax2) * 1.001f + 1.f, bnd1 = __builtin_sqrtf(q2[1] * kmax2) * 1.001f + 1.f;
    const u32x4 z4 = {0u, 0u, 0u, 0u};
    bf16x8 qt[2][2];
#pragma unroll
    for (int h = 0; h < 2; ++h) { const u32x4 a = {qtl[h][0], qtl[h][1], 0u, 0u}; qt[h][0] = __builtin_bit_cast(bf16x8, hh ? z4 : a); qt[h][1] = __builtin_bit_cast(bf16x8, hh ? a : z4); }
    const f32x16 zero = {};
    float m[2]; bool unsafe = false;
    { const lds_cptr kb = (lds_cptr)ldsh + lane * 16; const bf16x8 k0 = lds128(kb + STG_KM), kt = lds128(kb + STG_KT);
#pragma unroll
      for (int h = 0; h < 2; ++h) {
        f32x16 sc = MFMA32(k0, qm[h], zero); sc = MFMA32(kt, qt[h][0], sc);
        float mm = sc[0];
#pragma unroll
        for (int i = 1; i < 16; ++i) mm = fmaxf(mm, sc[i]);
        mm = max32(mm);
        m[h] = bf2f(f2bf(mm));
        unsafe |= !((h ? bnd1 : bnd0) - m[h] <= lim);
      } }
    if (__any(unsafe) && lane == 0) ctl[C_FALL] = 1u;
    GBAR(bb, bn, 8);
    bool st1_issued = false;
    if (rfl(ctl[C_FALL]) != 0u) {
        att_stage_load(kv, 4 * kh + 1, ldsh0 + KVB, w4, lane); st1_issued = true;
        att_maxpass(kv, ldsh, bb, bn, kh, w4, lane, qm, qt, m);
#pragma unroll
        for (int h = 0; h < 2; ++h) m[h] = bf2f(f2bf(max32(m[h])));
    }
#pragma unroll
    for (int h = 0; h < 2; ++h) { const unsigned nm = f2bf(-m[h]); const u32x4 a = {qtl[h][0], qtl[h][1], nm, 0u}; qt[h][0] = __builtin_bit_cast(bf16x8, hh ? z4 : a); qt[h][1] = __builtin_bit_cast(bf16x8, hh ? a : z4); }
    __builtin_amdgcn_s_setprio(0);
    f32x16 O[2] = {{}, {}};
    att_main(kv, ldsh, ctl, rw, ofrag_off(s0 + lane, 0), 80 * c4, WS_FA + (rt64 * 8 + c4) * 8, st1_issued, kh, w4, lane, qm, qt, O[0], O[1]);
    lds_signal(ctl + C_BD, lane);
    __builtin_amdgcn_s_setprio(3);
    {
        LAS f32x4* xo = (LAS f32x4*)((lds_ptr)lds + (2 * w4 + kh) * 4096) + lane;
        LAS f32x4* xi = (LAS f32x4*)((lds_ptr)lds + (2 * w4 + (kh ^ 1)) * 4096) + lane;
        const f32x16& Og = kh ? O[0] : O[1]; const f32x16& Ok = kh ? O[1] : O[0];
        const int s = s0 + 32 * (w4 & 1) + r, k0 = 640 + (4 * c4 + 2 * (w4 >> 1) + kh) * DH;
        const int a0 = ofrag_off(s, k0 + 4 * hh), a1 = ofrag_off(s, k0 + 8 + 4 * hh), a2 = ofrag_off(s, k0 + 16);
        lds_wait<false>(ctl + C_X, 8u);
        xo[0] = (f32x4){Og[0], Og[1], Og[2], Og[3]}; xo[64] = (f32x4){Og[4], Og[5], Og[6], Og[7]}; xo[128] = (f32x4){Og[8], Og[9], Og[10], Og[11]};
        xo[192] = (f32x4){kh ? m[0] : m[1], 0.f, 0.f, 0.f};
        lds_signal(ctl + C_P + 2 * w4 + kh, lane);
        lds_wait<false>(ctl + C_P + 2 * w4 + (kh ^ 1), 1u);
        const f32x4 p0 = xi[0], p1 = xi[64], p2 = xi[128]; const float m1 = xi[192][0];
        const float mo = kh ? m[1] : m[0];
        const float M = fmaxf(mo, m1), f0 = EXP2(mo - M), f1 = EXP2(m1 - M);
        float o[12];
#pragma unroll
        for (int i = 0; i < 4; ++i) { o[i] = Ok[i] * f0 + p0[i] * f1; o[4 + i] = Ok[4 + i] * f0 + p1[i] * f1; o[8 + i] = Ok[8 + i] * f0 + p2[i] * f1; }
        auto lr = __builtin_amdgcn_permlane32_swap(__float_as_uint(o[8]), __float_as_uint(o[8]), false, false);
        const float inv = __builtin_amdgcn_rcpf(__uint_as_float(lr[1]));
#define NE(x) ((x) == EMPTY ? 0x7fc07fc0u : (x))
        const unsigned w0 = pk2(o[0] * inv, o[1] * inv), w1 = pk2(o[2] * inv, o[3] * inv), w2 = pk2(o[4] * inv, o[5] * inv), w3 = pk2(o[6] * inv, o[7] * inv), w4_ = pk2(o[8] * inv, o[9] * inv), w5 = pk2(o[10] * inv, o[11] * inv);
        st64(rw, a0, (u32x2){NE(w0), NE(w1)});
        st64(rw, a1, (u32x2){NE(w2), NE(w3)});
        if (hh == 0) st64(rw, a2, (u32x2){NE(w4_), NE(w5)});
#undef NE
        lds_signal(ctl + C_D, lane);
    }
    if (kh == 1) {
        lds_wait<false>(ctl + C_BD, 8u);
#pragma unroll
        for (unsigned i = 0; i < 5; ++i) { const unsigned p = 256 * i + w4 * 64 + lane, pos = p / 10, j10 = p - pos * 10, off = (pos ^ (4 * ((pos >> 3) & 1))) * 1280 + j10 * 16;
            const char* sb = (const char*)(P.woB + 80 * c4) + 3 * (128 * 1280);
            gldsb(sb, off, rfl(lds0 + C_S3 + (4 * i + w4) * 1024)); gldsb(sb + 128 * 1280, off, rfl(lds0 + C_S4 + (4 * i + w4) * 1024)); }
        VMW(0); lds_signal(ctl + C_WB + 1, lane);
    }
    if (kh == 0) {
        f32x4 cc[5];
#pragma unroll
        for (int j = 0; j < 5; ++j) cc[j] = (f32x4){0.f, 0.f, 0.f, 0.f};
        lds_wait(ctl + C_D, 8u);
        __builtin_amdgcn_s_sleep(6);
        const bool ok = ob_part<10>(lds, ctl, rw, WS_O + (4 * rt64 + w4) * 40 * 1024, lane, cc);
        if (!ok) ctl[C_BAD] = 1u;
        LAS f32x4* red = (LAS f32x4*)((lds_ptr)lds + w4 * 5120) + lane;
#pragma unroll
        for (int j = 0; j < 5; ++j) red[j * 64] = cc[j];
        lds_signal(ctl + C_R + w4, lane);
    }
    asm volatile("s_waitcnt lgkmcnt(0)" ::: "memory");
}

__device__ __forceinline__ void a_waves(const Params& P, char* lds, LAS unsigned* ctl, rsrc_t rw, unsigned a, unsigned lane, unsigned b, unsigned li, unsigned rt64, unsigned c4) {
    const unsigned lds0 = (unsigned)(uintptr_t)lds;
    const int s0 = 64 * rt64;
    const unsigned i16 = lane & 15, g = lane >> 4, q4 = i16 >> 2, p4 = i16 & 3;
    unsigned an = 0; LAS unsigned* ab = ctl + C_AB;
    const unsigned part = c4 & 1, r0 = s0 + 16 * (c4 >> 1);
    {
        if (a == 3) { u64 pc; asm volatile("s_getpc_b64 %0" : "=s"(pc)); const unsigned ln = li * 10 + (lane < 10 ? lane : 0); touch((const char*)(pc & ~127ull) + (ln < CODE_LINES ? ln : 0) * 128, lds0 + DUMMY); }
    }
    p0_issue(P, lds0, a, lane, part, r0);
    lds_signal(ctl + C_P0I, lane);
    const u32x2 ep = ld64(rw, WS_EPOCH);
    const u64 tag = ((u64)ep[0] | ((u64)ep[1] << 32)) + 0x9E3779B97F4A7C15ull;
    const u32x2 tagv = {(unsigned)tag, (unsigned)(tag >> 32)};
    VMW(0);
    lds_signal(ctl + C_P0L, lane); lds_wait<false>(ctl + C_P0L, 8u);
    if (a < 2 || (a == 2 && part == 0)) {
        const lds_cptr p0x = (lds_cptr)lds + P0X + i16 * 1280;
        f32x4 pacc = {0.f, 0.f, 0.f, 0.f};
#define P0XF(ks) lds128(p0x + ((((4 * (ks) + g) & ~15u) | (((4 * (ks) + g) & 15) ^ i16)) * 16))
        const int ks0 = r0 & 255, img = WS_KV + (r0 >> 8) * STG_BYTES, nrm = WS_NRM + (r0 >> 4) * 16;
        if (a == 2) {
            const bf16x8 z = {0, 0, 0, 0, 0, 0, 0, 0};
#pragma unroll
            for (int ks = 0; ks < 20; ++ks) { const bf16x8 gv = lds128((lds_cptr)lds + P0G + (32 * ks + 8 * g) * 2); pacc = MFMA16(i16 == 0 ? gv : z, P0XF(ks), pacc); }
            if (g == 0) st32(rw, WS_G + (r0 + i16) * 4, __float_as_uint(1.f / (1.f + __expf(-pacc[0]))));
        } else {
            const lds_cptr p0w = (lds_cptr)lds + P0W + (8 * g + q4) * 40 + (16 * a + 4 * p4) * 2;
            if (part == 0) {
#pragma unroll
                for (int ks = 0; ks < 20; ++ks) pacc = MFMA16(ldstr2(p0w + ks * 1280, p0w + ks * 1280 + 160), P0XF(ks), pacc);
                const unsigned w0 = pk2(pacc[0], pacc[1]), w1 = pk2(pacc[2], pacc[3]);
                const int key = ks0 + i16, tile = key >> 5, r = swap23(key & 31);
                float n2 = lo2f(w0) * lo2f(w0) + hi2f(w0) * hi2f(w0) + lo2f(w1) * lo2f(w1) + hi2f(w1) * hi2f(w1);
                if (a == 0) { st64(rw, img + STG_KM + tile * 1024 + (g >> 1) * 512 + r * 16 + (g & 1) * 8, (u32x2){w0, w1});
                    n2 = sum16(n2); n2 = sum32(n2); }
                else { if (g == 0) st128(rw, img + STG_KT + (tile >> 1) * 1024 + (tile & 1) * 512 + r * 16, (u32x4){w0, w1, 0x3f80u, 0u}); if (g != 0) n2 = 0.f; }
                n2 = wave_max_pos(n2);
                if (lane == 0) st32(rw, nrm + a * 4, __float_as_uint(n2));
            } else {
#pragma unroll
                for (int ks = 0; ks < 20; ++ks) pacc = MFMA16(P0XF(ks), ldstr2(p0w + ks * 1280, p0w + ks * 1280 + 160), pacc);
                const unsigned w0 = pk2(pacc[0], pacc[1]), w1 = pk2(pacc[2], pacc[3]);
                const int d = 16 * a + i16;
                float vm = fmaxf(fmaxf(fabsf(lo2f(w0)), fabsf(hi2f(w0))), fmaxf(fabsf(lo2f(w1)), fabsf(hi2f(w1))));
                if (d >= 20) vm = 0.f;
                if (d <= 20) st64(rw, img + STG_VT + d * VT_ROW + (ks0 + 4 * g) * 2, d == 20 ? (u32x2){0x3f803f80u, 0x3f803f80u} : (u32x2){w0, w1});
                if (a == 1 && ks0 == 0 && lane < 40) st128(rw, img + STG_VT + 21 * VT_ROW + lane * 16, (u32x4){0u, 0u, 0u, 0u});
                vm = wave_max_pos(vm);
                if (lane == 0) st32(rw, nrm + (2 + a) * 4, __float_as_uint(vm));
            }
        }
#undef P0XF
        VMW(0);
    }
    GBARS(ab, an, 4);
    bool ok = true;
    if (a == 3) { if (lane < 32) st32(rw, WS_FKV + lane * 1024 + b * 4, tagv[0]);
        if (lane == 0) { ctl[C_TAG + 1] = tagv[0]; ctl[C_TAG + 2] = tagv[1]; }
        lds_signal(ctl + C_TAG, lane);
    }
    unsigned aoff[5];
#pragma unroll
    for (unsigned j = 0; j < 5; ++j) { const unsigned id = a + 4 * j;
        if (id < 15) { const unsigned seg = id / 5, p = (id - 5 * seg) * 64 + lane, pos = p / 10, j10 = p - pos * 10, k = pos ^ (4 * ((pos >> 3) & 1)); aoff[j] = k * 3840 + seg * 1280 + j10 * 16; }
        else { const unsigned p = (id - 15) * 64 + lane, row = p >> 2, ch = (p & 3) ^ (3 * ((row >> 2) & 1)); int gr = s0 - 16 + (int)row; gr = gr < 0 ? 0 : gr; aoff[j] = gr * 1280 + ch * 16; } }
    const char* awb = (const char*)(P.wqkv + 80 * c4); const char* axb = (const char*)P.x;
#define A_ISSUE(slot) do { gldsb(awb, aoff[0], lds0 + AA + (slot) * A_SLOT + a * 1024); gldsb(awb, aoff[1], lds0 + AA + (slot) * A_SLOT + (a + 4) * 1024); gldsb(awb, aoff[2], lds0 + AA + (slot) * A_SLOT + (a + 8) * 1024); \
        gldsb(a < 3 ? awb : axb, aoff[3], lds0 + AA + (slot) * A_SLOT + (a + 12) * 1024); gldsb(axb, aoff[4], lds0 + AA + (slot) * A_SLOT + (a + 16) * 1024); awb += 32 * 3840; axb += 64; } while (0)
    lds_wait4<true>(ctl + C_QL, 8u);
    A_ISSUE(0); A_ISSUE(1);
    f32x4 acc[5][4];
#pragma unroll
    for (int i = 0; i < 5; ++i)
#pragma unroll
        for (int j = 0; j < 4; ++j) acc[i][j] = (f32x4){0.f, 0.f, 0.f, 0.f};
    const lds_cptr ax = (lds_cptr)lds + AA + A_X + i16 * 64 + ((g ^ (3 * ((i16 >> 2) & 1))) * 16);
    lds_cptr aw0[4], aw1[4];
#pragma unroll
    for (unsigned j = 0; j < 4; ++j) { unsigned jt = 4 * a + j; jt = jt < 15 ? jt : 14; const unsigned sg = jt / 5, wi = jt - 5 * sg;
        aw0[j] = (lds_cptr)lds + AA + sg * 5120 + wi * 32 + (8 * g + q4 + 4 * (0 ^ (g & 1))) * 160 + p4 * 8; aw1[j] = (lds_cptr)lds + AA + sg * 5120 + wi * 32 + (8 * g + q4 + 4 * (1 ^ (g & 1))) * 160 + p4 * 8; }
    VMW(5); lds_signal(ab, lane); an += 4;
    { int slot = 0;
      bf16x8 xa[5], xb[5], w0, w1, wc, wd;
#define A_FIRST(X, so_) do { lds_wait<true>(ab, an); w0 = ldstr2(aw0[0] + (so_), aw1[0] + (so_)); \
            _Pragma("unroll") for (int i = 0; i < 5; ++i) X[i] = lds128(ax + (so_) + i * 1024); \
            w1 = ldstr2(aw0[1] + (so_), aw1[1] + (so_)); SBAR(); } while (0)
#define A_MF(X, i, j, w) acc[i][j] = MFMA16(w, X[i], acc[i][j])
#define A_LD(j) do { if (t + 2 < 20) gldsb((j) < 3 || ((j) == 3 && a < 3) ? awb : axb, aoff[j], lds0 + AA + fs * A_SLOT + (a + 4 * (j)) * 1024); } while (0)
#define A_STAGE(X, Y) do { \
        const int so = slot * A_SLOT, fs = slot == 0 ? 2 : slot - 1; \
        wc = ldstr2(aw0[2] + so, aw1[2] + so); \
        A_MF(X, 0, 0, w0); A_LD(0); A_MF(X, 1, 0, w0); A_MF(X, 2, 0, w0); A_LD(1); A_MF(X, 3, 0, w0); A_MF(X, 4, 0, w0); SBAR(); \
        wd = ldstr2(aw0[3] + so, aw1[3] + so); \
        A_MF(X, 0, 1, w1); A_LD(2); A_MF(X, 1, 1, w1); A_MF(X, 2, 1, w1); A_LD(3); A_MF(X, 3, 1, w1); A_MF(X, 4, 1, w1); SBAR(); \
        A_MF(X, 0, 2, wc); A_LD(4); A_MF(X, 1, 2, wc); SBAR(); \
        awb += 32 * 3840; axb += 64; \
        if (t < 18) VMW(5); else VMW(0);          \
        lds_signal(ab, lane); an += 4; \
        A_MF(X, 2, 2, wc); A_MF(X, 3, 2, wc); A_MF(X, 4, 2, wc); SBAR(); \
        slot = slot == 2 ? 0 : slot + 1; \
        if (t + 1 < 20) A_FIRST(Y, slot * A_SLOT); \
        A_MF(X, 0, 3, wd); A_MF(X, 1, 3, wd); A_MF(X, 2, 3, wd); A_MF(X, 3, 3, wd); A_MF(X, 4, 3, wd); SBAR(); } while (0)
      A_FIRST(xa, 0);
#pragma unroll 1
      for (int t2 = 0; t2 < 20; t2 += 2) {
        { const int t = t2; A_STAGE(xa, xb); }
        { const int t = t2 + 1; A_STAGE(xb, xa); }
      } }
#undef A_FIRST
#undef A_STAGE
#undef A_MF
#undef A_LD
    ok &= poll_tags<1>(rw, WS_FA + (rt64 * 8 + (lane & 7)) * 8, lane < 8, tag);
    lds_wait<true>(ab, an);
    { lds_ptr st = (lds_ptr)lds;
#pragma unroll
      for (unsigned j = 0; j < 4; ++j) { const unsigned jt = 4 * a + j, sg = jt / 5, wi = jt - 5 * sg, cq = 4 * wi + g, hl = (cq * 13) >> 6, dq = cq - 5 * hl;
        if (jt < 15) {
#pragma unroll
            for (int i = 0; i < 5; ++i) { const int row = 16 * i + i16; const f32x4 v = acc[i][j];
                if (sg < 2) *(LAS u32x2*)(st + ST_QK + row * QK_ROW + sg * 192 + hl * 48 + dq * 8) = (u32x2){pk2(v[0], v[1]), pk2(v[2], v[3])};
                else *(LAS f32x4*)(st + ST_V + row * V_ROW + hl * 80 + dq * 16) = v; } } } }
    GBARS(ab, an, 4);
    {
        const int sl = lane, row = 16 + sl, s = s0 + sl;
        const lds_cptr qp = (lds_cptr)lds + ST_QK + row * QK_ROW + a * 48;
        const lds_cptr kp = qp + 192 - 14 * QK_ROW;
        const u32x4 qa = *(const LAS u32x4*)qp, qb = *(const LAS u32x4*)(qp + 16); const u32x2 qc = *(const LAS u32x2*)(qp + 32);
        u32x4 ka[8], kb[8]; u32x2 kc[8];
#pragma unroll
        for (int w = 0; w < 8; ++w) { ka[w] = *(const LAS u32x4*)(kp + (14 - 2 * w) * QK_ROW); kb[w] = *(const LAS u32x4*)(kp + (14 - 2 * w) * QK_ROW + 16); kc[w] = *(const LAS u32x2*)(kp + (14 - 2 * w) * QK_ROW + 32); }
        SBAR();
        const lds_cptr vp = (lds_cptr)lds + ST_V + (row - 14) * V_ROW + a * 80;
        float lg[8]; float m = -3.0e38f;
#pragma unroll
        for (int w = 0; w < 8; ++w) {
            float x = dot2(qa[0], ka[w][0], 0.f), x2 = dot2(qa[1], ka[w][1], 0.f);
            x = dot2(qa[2], ka[w][2], x); x2 = dot2(qa[3], ka[w][3], x2); x = dot2(qb[0], kb[w][0], x); x2 = dot2(qb[1], kb[w][1], x2);
            x = dot2(qb[2], kb[w][2], x); x2 = dot2(qb[3], kb[w][3], x2); x = dot2(qc[0], kc[w][0], x); x2 = dot2(qc[1], kc[w][1], x2);
            x = (x + x2) * QSCALE;
            lg[w] = (s - 2 * w >= 0) ? x : -1.0e9f * LOG2E; m = fmaxf(m, lg[w]);
        }
        f32x4 va[3][5], vb[3][5];
#define LV(dst, w0, n) do { _Pragma("unroll") for (int w = 0; w < (n); ++w) _Pragma("unroll") for (int j = 0; j < 5; ++j) dst[w][j] = *(const LAS f32x4*)(vp + (14 - 2 * ((w0) + w)) * V_ROW + j * 16); SBAR(); } while (0)
#define PIN5(o) asm volatile("" : "+v"(o[0]), "+v"(o[1]), "+v"(o[2]), "+v"(o[3]), "+v"(o[4]))
        LV(va, 0, 3);
        float l = 0.f;
#pragma unroll
        for (int w = 0; w < 8; ++w) { lg[w] = EXP2(lg[w] - m); l += lg[w]; }
        const float inv = __builtin_amdgcn_rcpf(l);
        LV(vb, 3, 3);
        f32x4 o[5];
#pragma unroll
        for (int j = 0; j < 5; ++j) o[j] = va[0][j] * lg[0] + va[1][j] * lg[1] + va[2][j] * lg[2];
        PIN5(o); SBAR();
        LV(va, 6, 2);
#pragma unroll
        for (int j = 0; j < 5; ++j) o[j] += vb[0][j] * lg[3] + vb[1][j] * lg[4] + vb[2][j] * lg[5];
        PIN5(o); SBAR();
#pragma unroll
        for (int j = 0; j < 5; ++j) o[j] += va[0][j] * lg[6] + va[1][j] * lg[7];
#undef LV
#undef PIN5
        lds_wait(ctl + C_MKD, 1u);
#pragma unroll
        for (int j = 0; j < 5; ++j) { const unsigned w0 = pk2(o[j][0] * inv, o[j][1] * inv), w1 = pk2(o[j][2] * inv, o[j][3] * inv);
            st64(rw, ofrag_off(s, (4 * c4 + a) * DH + 4 * j), (u32x2){w0 == EMPTY ? 0x7fc07fc0u : w0, w1 == EMPTY ? 0x7fc07fc0u : w1}); }
    }
    GBARS(ab, an, 4);
    unsigned coff[5];
#pragma unroll
    for (unsigned i = 0; i < 5; ++i) { const unsigned p = 256 * i + a * 64 + lane, pos = p / 10, j10 = p - pos * 10; coff[i] = (pos ^ (4 * ((pos >> 3) & 1))) * 1280 + j10 * 16; }
    auto c_issue = [&](const u16* W, unsigned chunk, unsigned dst) {
        const char* sb = (const char*)(W + 80 * c4) + (size_t)chunk * (128 * 1280);
#pragma unroll
        for (unsigned i = 0; i < 5; ++i) gldsb(sb, coff[i], lds0 + dst + (4 * i + a) * 1024);
    };
    c_issue(P.woA, 0, AA); c_issue(P.woA, 1, AA + A_SLOT); c_issue(P.woA, 2, AA + 2 * A_SLOT);
    const int fo = WS_O + (4 * rt64 + a) * 40 * 1024, fl = lane * 16;
    bf16x8 of[20];
#pragma unroll
    for (int ks = 0; ks < 20; ++ks) of[ks] = __builtin_bit_cast(bf16x8, ld128s(rw, fl, fo + ks * 1024));
    const float gg = __uint_as_float(ld32(rw, WS_G + (s0 + 16 * a + i16) * 4));
    const unsigned cbo = (8 * g + q4) * 160 + p4 * 8, ct0 = 4 * (0 ^ (g & 1)) * 160, ct1 = 4 * (1 ^ (g & 1)) * 160;
    const lds_cptr cw0 = (lds_cptr)lds + cbo + ct0, cw1 = (lds_cptr)lds + cbo + ct1;
    f32x4 cc[5]; bf16x8 wA[5], wB[5];
#define C_READY(F, n0, N) do { \
        bool got = false; \
        _Pragma("unroll 1") for (unsigned spins = 0; spins < SPIN_CAP; ++spins) { \
            unsigned mx = 0; \
            _Pragma("unroll") for (int u = 0; u < (N); ++u) { const u32x4 v = __builtin_bit_cast(u32x4, of[(n0) + u]); mx = max(max(mx, v[0]), max(max(v[1], v[2]), v[3])); } \
            if (!__any(mx == EMPTY)) { got = true; break; } \
            __builtin_amdgcn_s_sleep(2); \
            _Pragma("unroll") for (int u = 0; u < (N); ++u) of[(n0) + u] = __builtin_bit_cast(bf16x8, ld128s(rw, fl, fo + ((F) + (n0) + u) * 1024)); \
        } \
        ok &= got; } while (0)
#define C_RD(W, off) do { lds_cptr b0_ = cw0 + (off), b1_ = cw1 + (off); asm volatile("" : "+v"(b0_), "+v"(b1_)); _Pragma("unroll") for (int j = 0; j < 5; ++j) W[j] = ldstr2(b0_ + j * 32, b1_ + j * 32); SBAR(); } while (0)
#define C_MF(W, ks) do { _Pragma("unroll") for (int j = 0; j < 5; ++j) cc[j] = MFMA16(W[j], of[ks], cc[j]); asm volatile("" : "+v"(cc[0]), "+v"(cc[1]), "+v"(cc[2]), "+v"(cc[3]), "+v"(cc[4])); SBAR(); } while (0)
#define C_CHUNK(base, ks0, next) do { C_RD(wB, (base) + 5120); C_MF(wA, (ks0)); C_RD(wA, (base) + 10240); C_MF(wB, (ks0) + 1); C_RD(wB, (base) + 15360); C_MF(wA, (ks0) + 2); \
        if ((next) >= 0) C_RD(wA, (next) >= 0 ? (next) : 0); C_MF(wB, (ks0) + 3); } while (0)
#pragma unroll
    for (int j = 0; j < 5; ++j) cc[j] = (f32x4){0.f, 0.f, 0.f, 0.f};
    C_READY(0, 0, 10); C_READY(0, 10, 10);
    VMW(0); GBARS(ab, an, 4);
    C_RD(wA, AA); C_CHUNK(AA, 0, AA + A_SLOT);
    GBARS(ab, an, 4); c_issue(P.woA, 3, AA);
    C_CHUNK(AA + A_SLOT, 4, AA + 2 * A_SLOT);
    GBARS(ab, an, 4); c_issue(P.woA, 4, AA + A_SLOT);
    C_CHUNK(AA + 2 * A_SLOT, 8, -1);
    VMW(5); GBARS(ab, an, 4); c_issue(P.woB, 0, AA + 2 * A_SLOT);
    C_RD(wA, AA); C_CHUNK(AA, 12, -1);
    VMW(5); GBARS(ab, an, 4); c_issue(P.woB, 1, AA);
    C_RD(wA, AA + A_SLOT); C_CHUNK(AA + A_SLOT, 16, -1);
    u32x2 cA[5];
#pragma unroll
    for (int j = 0; j < 5; ++j) { cA[j] = (u32x2){pk2(cc[j][0], cc[j][1]), pk2(cc[j][2], cc[j][3])}; cc[j] = (f32x4){0.f, 0.f, 0.f, 0.f}; }
    LAS u32x2* park = (LAS u32x2*)((lds_ptr)lds + 36864 + a * 2560) + lane;
    GBARS(ab, an, 4);
    c_issue(P.woB, 2, AA + A_SLOT);
    VMW(0); lds_signal(ctl + C_WB, lane);
    __builtin_amdgcn_s_setprio(3);
    lds_wait(ctl + C_D, 8u);
#pragma unroll
    for (int j = 0; j < 5; ++j) park[j * 64] = cA[j];
    __builtin_amdgcn_s_sleep(6);
    ok &= ob_part<0>(lds, ctl, rw, fo, lane, cc);
#undef C_READY
#undef C_RD
#undef C_MF
#undef C_CHUNK
    if (!ok) ctl[C_BAD] = 1u;
    lds_wait<false>(ctl + C_R + a, 1u);
    { LAS f32x4* red = (LAS f32x4*)((lds_ptr)lds + a * 5120) + lane;
#pragma unroll
      for (int j = 0; j < 5; ++j) cc[j] += red[j * 64]; }
    const float poison = (rfl(ctl[C_BAD]) || !ok) ? __builtin_nanf("") : 0.f;
    unsigned ln = __builtin_amdgcn_mbcnt_hi(~0u, __builtin_amdgcn_mbcnt_lo(~0u, 0u)); asm volatile("" : "+v"(ln));
#pragma unroll
    for (int j = 0; j < 5; ++j) { const u32x2 pa = *((LAS u32x2*)((lds_ptr)lds + 36864 + a * 2560) + ln + j * 64); const f32x4 av = {lo2f(pa[0]), hi2f(pa[0]), lo2f(pa[1]), hi2f(pa[1])}; const f32x4 v = av * gg + cc[j] * (1.f - gg) + poison;
        *(u32x2*)(P.out + (size_t)(s0 + 16 * a + (ln & 15)) * 640 + 80 * c4 + 16 * j + 4 * (ln >> 4)) = (u32x2){pk2(v[0], v[1]), pk2(v[2], v[3])}; }
}

__global__ __launch_bounds__(768, 3) void fused(Params P) {
    extern __shared__ __attribute__((aligned(16))) char lds[];
    char* const ws = P.ws;
    const unsigned tid = threadIdx.x, wid = rfl(tid >> 6);
    const unsigned lane = __builtin_amdgcn_mbcnt_hi(~0u, __builtin_amdgcn_mbcnt_lo(~0u, 0u));
    const unsigned b = blockIdx.x & 255, xg = b & 7, li = b >> 3;
    const unsigned rt64 = (xg >> 2) * 16 + (li >> 1), c4 = (xg & 3) * 2 + (li & 1);
    const rsrc_t rw = __builtin_amdgcn_make_buffer_rsrc(ws, 0, WS_END, 0x00020000);
    LAS unsigned* ctl = (LAS unsigned*)((lds_ptr)lds + CTL);
    if (tid < 64) ctl[tid] = 0u;
    LBAR();
    if (wid < 8) b_waves(P, lds, ctl, rw, wid & 7, lane, b, li, rt64, c4);
    else { __builtin_amdgcn_s_setprio(2); a_waves(P, lds, ctl, rw, wid & 3, lane, b, li, rt64, c4); }
}

static inline Params mk_params(void* const* d_in, void* d_out, void* d_ws) {
    Params P; P.x = (const u16*)d_in[0]; P.wqkv = (const u16*)d_in[1]; P.woA = (const u16*)d_in[2]; P.wq = (const u16*)d_in[4]; P.wk = (const u16*)d_in[5]; P.wv = (const u16*)d_in[6];
    P.woB = (const u16*)d_in[7]; P.gw = (const u16*)d_in[8]; P.ws = (char*)d_ws; P.out = (u16*)d_out; return P; }
extern "C" void kernel_launch(void* const* d_in, const int* in_sizes, int n_in, void* d_out, int out_size, void* d_ws, size_t ws_size, hipStream_t stream) {
    (void)hipFuncSetAttribute((const void*)fused, hipFuncAttributeMaxDynamicSharedMemorySize, LDS_TOTAL);
    fused<<<256, 768, LDS_TOTAL, stream>>>(mk_params(d_in, d_out, d_ws));
}
```

```cpp
#include <hip/hip_runtime.h>
#include <stdint.h>

typedef unsigned short u16;
typedef unsigned long long u64;
typedef short bf16x8 __attribute__((ext_vector_type(8)));
typedef short s16x4 __attribute__((ext_vector_type(4)));
typedef float f32x16 __attribute__((ext_vector_type(16)));
typedef float f32x4 __attribute__((ext_vector_type(4)));
typedef float f32x2 __attribute__((ext_vector_type(2)));
typedef unsigned u32x4 __attribute__((ext_vector_type(4)));
typedef unsigned u32x2 __attribute__((ext_vector_type(2)));
typedef __bf16 bf2_t __attribute__((ext_vector_type(2)));
#define LAS __attribute__((address_space(3)))
typedef LAS const char* lds_cptr;
typedef LAS char* lds_ptr;
typedef __amdgpu_buffer_rsrc_t rsrc_t;

__device__ __forceinline__ float bf2f(u16 b) { return __uint_as_float(((unsigned)b) << 16); }
__device__ __forceinline__ unsigned pk2(float lo, float hi) { f32x2 v = {lo, hi}; bf2_t r = __builtin_convertvector(v, bf2_t); return __builtin_bit_cast(unsigned, r); }
__device__ __forceinline__ u16 f2bf(float f) { return (u16)(pk2(f, 0.f) & 0xffffu); }
__device__ __forceinline__ float lo2f(unsigned u) { return __uint_as_float(u << 16); }
__device__ __forceinline__ float hi2f(unsigned u) { return __uint_as_float(u & 0xffff0000u); }
__device__ __forceinline__ float dot2(unsigned a, unsigned b, float c) { return __builtin_amdgcn_fdot2_f32_bf16(__builtin_bit_cast(bf2_t, a), __builtin_bit_cast(bf2_t, b), c, false); }

constexpr int S = 2048, D = 640, H = 32, DH = 20;
constexpr float SCALE = 0.22360679774997896f, LOG2E = 1.4426950408889634f;
constexpr float QSCALE = SCALE * LOG2E;

constexpr int STG_KM = 0, STG_KT = 8192, STG_VT = 12288, VT_ROW = 528, STG_BYTES = 24576;
constexpr int WS_EPOCH = 0;
constexpr int WS_FKV = 262144;
constexpr int WS_FA = 2304;
constexpr int WS_NRM = 8192;
constexpr int WS_G = WS_NRM + 2048;
constexpr int WS_KV = 32768;
constexpr int WS_O = WS_FKV + 32768;
constexpr int WS_END = WS_O + S * 1280 * 2;
constexpr int AUX_SC1 = 16;

#define MFMA32(a, b, c) __builtin_amdgcn_mfma_f32_32x32x16_bf16((a), (b), (c), 0, 0, 0)
#define MFMA16(a, b, c) __builtin_amdgcn_mfma_f32_16x16x32_bf16((a), (b), (c), 0, 0, 0)
#define SBAR() __builtin_amdgcn_sched_barrier(0)
#define EXP2(x) __builtin_amdgcn_exp2f(x)
#define LBAR() asm volatile("s_waitcnt lgkmcnt(0)\n\ts_barrier" ::: "memory")
__device__ __forceinline__ float wave_max_pos(float f) {
    unsigned x = __float_as_uint(f);
    x = max(x, (unsigned)__builtin_amdgcn_update_dpp(0, (int)x, 0x111, 0xf, 0xf, false));
    x = max(x, (unsigned)__builtin_amdgcn_update_dpp(0, (int)x, 0x112, 0xf, 0xf, false));
    x = max(x, (unsigned)__builtin_amdgcn_update_dpp(0, (int)x, 0x114, 0xf, 0xf, false));
    x = max(x, (unsigned)__builtin_amdgcn_update_dpp(0, (int)x, 0x118, 0xf, 0xf, false));
    x = max(x, (unsigned)__builtin_amdgcn_update_dpp(0, (int)x, 0x142, 0xa, 0xf, false));
    x = max(x, (unsigned)__builtin_amdgcn_update_dpp(0, (int)x, 0x143, 0xc, 0xf, false));
    return __uint_as_float((unsigned)__builtin_amdgcn_readlane((int)x, 63));
}
__device__ __forceinline__ float sum16(float f) { auto r = __builtin_amdgcn_permlane16_swap(__float_as_uint(f), __float_as_uint(f), false, false); return __uint_as_float(r[0]) + __uint_as_float(r[1]); }
__device__ __forceinline__ float sum32(float f) { auto r = __builtin_amdgcn_permlane32_swap(__float_as_uint(f), __float_as_uint(f), false, false); return __uint_as_float(r[0]) + __uint_as_float(r[1]); }
__device__ __forceinline__ float max32(float f) { auto r = __builtin_amdgcn_permlane32_swap(__float_as_uint(f), __float_as_uint(f), false, false); return fmaxf(__uint_as_float(r[0]), __uint_as_float(r[1])); }
__device__ __forceinline__ int swap23(int x) { return (x & ~12) | ((x & 4) << 1) | ((x & 8) >> 1); }
__device__ __forceinline__ void gldsb(const void* sbase, unsigned voff, unsigned lds_base) {
    unsigned sv; asm volatile("s_mov_b32 %0, m0\n\ts_mov_b32 m0, %3\n\ts_nop 0\n\tglobal_load_lds_dwordx4 %1, %2\n\ts_mov_b32 m0, %0" : "=&s"(sv) : "v"(voff), "s"(sbase), "s"(lds_base) : "memory"); }
__device__ __forceinline__ void gldsb_sc1(const void* sbase, unsigned voff, unsigned lds_base) {
    unsigned sv; asm volatile("s_mov_b32 %0, m0\n\ts_mov_b32 m0, %3\n\ts_nop 0\n\tglobal_load_lds_dwordx4 %1, %2 sc1\n\ts_mov_b32 m0, %0" : "=&s"(sv) : "v"(voff), "s"(sbase), "s"(lds_base) : "memory"); }
__device__ __forceinline__ bf16x8 lds128(lds_cptr p) { return *(const LAS bf16x8*)p; }
typedef short v4i16_t __attribute__((ext_vector_type(4)));
__device__ __forceinline__ s16x4 ldstr(lds_cptr p) { return __builtin_bit_cast(s16x4, __builtin_amdgcn_ds_read_tr16_b64_v4i16((LAS v4i16_t*)p)); }
__device__ __forceinline__ bf16x8 ldstr2(lds_cptr p0, lds_cptr p1) { const s16x4 a = ldstr(p0), b = ldstr(p1); return __builtin_shufflevector(a, b, 0, 1, 2, 3, 4, 5, 6, 7); }
__device__ __forceinline__ unsigned rfl(unsigned x) { return (unsigned)__builtin_amdgcn_readfirstlane(x); }
__device__ __forceinline__ void st32(rsrc_t r, int off, unsigned v) { __builtin_amdgcn_raw_buffer_store_b32(v, r, off, 0, AUX_SC1); }
__device__ __forceinline__ void st64(rsrc_t r, int off, u32x2 v) { __builtin_amdgcn_raw_buffer_store_b64(v, r, off, 0, AUX_SC1); }
__device__ __forceinline__ void st128(rsrc_t r, int off, u32x4 v) { __builtin_amdgcn_raw_buffer_store_b128(v, r, off, 0, AUX_SC1); }
__device__ __forceinline__ unsigned ld32(rsrc_t r, int off) { return __builtin_amdgcn_raw_buffer_load_b32(r, off, 0, AUX_SC1); }
__device__ __forceinline__ u32x2 ld64(rsrc_t r, int off) { return __builtin_amdgcn_raw_buffer_load_b64(r, off, 0, AUX_SC1); }
__device__ __forceinline__ u32x4 ld128(rsrc_t r, int off) { return __builtin_amdgcn_raw_buffer_load_b128(r, off, 0, AUX_SC1); }
__device__ __forceinline__ u32x4 ld128s(rsrc_t r, int voff, int soff) { return __builtin_amdgcn_raw_buffer_load_b128(r, voff, soff, AUX_SC1); }

struct Params { const u16 *x, *wqkv, *woA, *wq, *wk, *wv, *woB, *gw; char* ws; u16* out; };

constexpr int LDS_TOTAL = 163840, CTL = LDS_TOTAL - 256, DUMMY = CTL - 256;
constexpr int KVB = 2 * STG_BYTES;
constexpr int Q_SLOT = 18432, Q_W = 8192, Q_ST = 73728, QB_ROW = 208;
constexpr int AA = 98304;
constexpr int P0W = AA, P0X = AA + 25664, P0G = P0X + 20480;
constexpr int A_SLOT = 20480, A_X = 15360;
constexpr int ST_QK = AA, ST_V = AA + 32000, QK_ROW = 400, V_ROW = 336;
constexpr int C_S3 = 49152, C_S4 = 69632;
constexpr int C_FALL = 0, C_BB = 1, C_DONE = 2, C_LAND = 4, C_X = 6, C_BD = 7, C_D = 12, C_AB = 13, C_KVOK = 16, C_BAD = 17, C_P0I = 18, C_QL = 20, C_QD = 24, C_TAG = 28, C_QS = 19, C_MKD = 15, C_P = 32, C_WB = 40, C_NRM = 42, C_P0L = 14, C_FM = 43, C_R = 44;
constexpr unsigned CODE_LINES = 296;
constexpr unsigned EMPTY = 0xFFFFFFFFu;
constexpr unsigned SPIN_CAP = 1u << 18;

template <int N, int SLEEP = 4>
__device__ __forceinline__ bool poll_tags(rsrc_t r, int off, bool active, u64 tag) {
#pragma unroll 1
    for (unsigned spins = 0; spins < SPIN_CAP; ++spins) {
        bool ok = true;
        if (active) {
#pragma unroll
            for (int i = 0; i < N; ++i) { const u32x2 v = ld64(r, off + 8 * i); ok &= (v[0] == (unsigned)tag) & (v[1] == (unsigned)(tag >> 32)); }
        }
        if (__all(ok)) return true;
        __builtin_amdgcn_s_sleep(SLEEP);
    }
    return false;
}
__device__ __forceinline__ void lds_signal(LAS unsigned* cnt, int lane) {
    (void)lane;
    __builtin_amdgcn_sched_barrier(0);
    asm volatile("s_mov_b64 exec, 1\n\tds_add_u32 %0, %1\n\ts_mov_b64 exec, -1\n\ts_nop 3" :: "v"((unsigned)(uintptr_t)cnt), "v"(1u) : "memory");
    __builtin_amdgcn_sched_barrier(0); }
template <bool SLEEP = true>
__device__ __forceinline__ void lds_wait(LAS unsigned* cnt, unsigned n) {
    unsigned v, sv, left;
    if (SLEEP)
        asm volatile("s_mov_b32 %2, 0x100000\n1:\n\tds_read_b32 %0, %3\n\ts_waitcnt lgkmcnt(0)\n\tv_readfirstlane_b32 %1, %0\n\ts_cmp_ge_u32 %1, %4\n\ts_cbranch_scc1 2f\n\ts_sleep 1\n\ts_sub_u32 %2, %2, 1\n\ts_cbranch_scc0 1b\n2:"
                     : "=&v"(v), "=&s"(sv), "=&s"(left) : "v"((unsigned)(uintptr_t)cnt), "s"(n) : "memory", "scc");
    else
        asm volatile("s_mov_b32 %2, 0x100000\n1:\n\tds_read_b32 %0, %3\n\ts_waitcnt lgkmcnt(0)\n\tv_readfirstlane_b32 %1, %0\n\ts_cmp_ge_u32 %1, %4\n\ts_cbranch_scc1 2f\n\ts_sub_u32 %2, %2, 1\n\ts_cbranch_scc0 1b\n2:"
                     : "=&v"(v), "=&s"(sv), "=&s"(left) : "v"((unsigned)(uintptr_t)cnt), "s"(n) : "memory", "scc");
}
template <bool SLEEP = false>
__device__ __forceinline__ void lds_wait4(LAS unsigned* cnt, unsigned n) {
#pragma unroll 1
    for (unsigned spins = 0; spins < (1u << 20); ++spins) {
        const u32x4 v = *(volatile LAS u32x4*)cnt;
        const unsigned lo = min(min(v[0], v[1]), min(v[2], v[3]));
        if (rfl(lo) >= n) break;
        if (SLEEP) __builtin_amdgcn_s_sleep(1);
    }
    asm volatile("" ::: "memory");
}
#define VMW(N) asm volatile("s_waitcnt vmcnt(" #N ")" ::: "memory")
#define GBAR(cnt, n, step) do { lds_signal(cnt, lane); n += (step); lds_wait<false>(cnt, n); } while (0)
#define GBARS(cnt, n, step) do { lds_signal(cnt, lane); n += (step); lds_wait<true>(cnt, n); } while (0)

__device__ __forceinline__ void att_stage_load(const char* kv, int st, unsigned ldsbuf, int w4, int lane) {
    const char* src = kv + (size_t)st * STG_BYTES + w4 * 1024;
#pragma unroll
    for (int i = 0; i < 6; ++i) gldsb_sc1(src + i * 4096, lane * 16, ldsbuf + (w4 + 4 * i) * 1024);
}

__device__ __forceinline__ void att_maxpass(const char* kv, char* ldsh, LAS unsigned* bb, unsigned& bn, int kh, int w4, int lane, const bf16x8 (&qm)[2], const bf16x8 (&qt)[2][2], float (&mx)[2]) {
    const unsigned lds0 = (unsigned)(uintptr_t)ldsh;
    const lds_cptr kbase = (lds_cptr)ldsh + lane * 16;
    const f32x16 zero = {};
#pragma unroll 1
    for (int rnd = 0; rnd < 3; ++rnd) {
        if (rnd) { GBAR(bb, bn, 8); att_stage_load(kv, 4 * kh + 2 * (rnd & 1), lds0, w4, lane); att_stage_load(kv, 4 * kh + 2 * (rnd & 1) + 1, lds0 + KVB, w4, lane); }
        VMW(0); GBAR(bb, bn, 8);
        if (rnd < 2) {
#pragma unroll 1
            for (int pr = 0; pr < 8; ++pr) {
                const lds_cptr kb = kbase + (pr >> 2) * KVB; const int p = pr & 3;
                const bf16x8 kt = lds128(kb + STG_KT + p * 1024), k0 = lds128(kb + STG_KM + (2 * p) * 1024), k1 = lds128(kb + STG_KM + (2 * p + 1) * 1024);
#pragma unroll
                for (int h = 0; h < 2; ++h) {
                    f32x16 s0 = MFMA32(k0, qm[h], zero); s0 = MFMA32(kt, qt[h][0], s0);
                    f32x16 s1 = MFMA32(k1, qm[h], zero); s1 = MFMA32(kt, qt[h][1], s1);
#pragma unroll
                    for (int i = 0; i < 16; ++i) mx[h] = fmaxf(mx[h], fmaxf(s0[i], s1[i]));
                }
            }
        }
    }
}

__device__ __forceinline__ void att_main(const char* kv, char* ldsh, LAS unsigned* ctl, rsrc_t rw, int mark_off, int kbase0, int fm_off, bool st1_issued, int kh, int w4, int lane, const bf16x8 (&qm)[2], const bf16x8 (&qt)[2][2], f32x16& O0, f32x16& O1) {
    const unsigned lds0 = (unsigned)(uintptr_t)ldsh;
    LAS unsigned* cdone = ctl + C_DONE + kh; LAS unsigned* cland = ctl + C_LAND + kh;
    const int r = lane & 31, hh = lane >> 5;
    const int voff = r <= 20 ? r * VT_ROW : (r < 28 ? 21 * VT_ROW : 21 * VT_ROW + 112);
    const lds_cptr kbase = (lds_cptr)ldsh + lane * 16;
    const lds_cptr vbase = (lds_cptr)ldsh + STG_VT + voff + hh * 16;
    const f32x16 zero = {};
    f32x16 sA, sB; u32x4 pA0, pA1, pB0, pB1; bf16x8 kmA, kmB, ktA, ktB, vA0, vA1, vB0, vB1;
    ktA = lds128(kbase + STG_KT); kmA = lds128(kbase + STG_KM);
    sA = MFMA32(kmA, qm[0], zero); sA = MFMA32(ktA, qt[0][0], sA);
    pB0 = (u32x4){0u, 0u, 0u, 0u}; pB1 = pB0; vB0 = kmA; vB1 = kmA; ktB = ktA; vA0 = kmA; vA1 = kmA; kmB = kmA;
    float ee0 = 0.f, ee1 = 0.f, ee2 = 0.f, ee3 = 0.f;
#define GRP(MF, LD, SC, I, PD, J) do { MF; SBAR(); LD; const float e0_ = EXP2(SC[I]); unsigned a_ = pk2(ee0, ee1); const float e1_ = EXP2(SC[I + 1]); unsigned b_ = pk2(ee2, ee3); const float e2_ = EXP2(SC[I + 2]), e3_ = EXP2(SC[I + 3]); \
        ee0 = e0_; ee1 = e1_; ee2 = e2_; ee3 = e3_; asm volatile("" : "+v"(a_), "+v"(b_), "+v"(ee0), "+v"(ee1), "+v"(ee2), "+v"(ee3)); PD[J] = a_; PD[J + 1] = b_; SBAR(); } while (0)
#define TILE(KM, KMN, KT, VJ, KTN, VJN, VC0, VC1, VP0, VP1, VADDR, LDK, LDT) do { \
        GRP(sB = MFMA32(KM, qm[1], zero), VC0 = lds128(VADDR), sA, 0, pB1, 2); \
        GRP(O1 = MFMA32(VP0, __builtin_bit_cast(bf16x8, pB0), O1), VC1 = lds128((VADDR) + 32), sA, 4, pA0, 0); \
        GRP(sB = MFMA32(KT, qt[1][VJ], sB), LDK, sA, 8, pA0, 2); \
        GRP(O1 = MFMA32(VP1, __builtin_bit_cast(bf16x8, pB1), O1), LDT, sA, 12, pA1, 0); \
        GRP(sA = MFMA32(KMN, qm[0], zero), (void)0, sB, 0, pA1, 2); \
        GRP(O0 = MFMA32(VC0, __builtin_bit_cast(bf16x8, pA0), O0), (void)0, sB, 4, pB0, 0); \
        GRP(sA = MFMA32(KTN, qt[0][VJN], sA), (void)0, sB, 8, pB0, 2); \
        GRP(O0 = MFMA32(VC1, __builtin_bit_cast(bf16x8, pA1), O0), (void)0, sB, 12, pB1, 0); } while (0)
#pragma unroll 1
    for (int st = 0; st < 4; ++st) {
        if (kh == 1) { if (st == 0) __builtin_amdgcn_s_setprio(1); if (2 * st == 5) __builtin_amdgcn_s_setprio(0); }
        const int buf = st & 1;
        const lds_cptr kb = kbase + buf * KVB, vb = vbase + buf * KVB, kn = kbase + (buf ^ 1) * KVB;
        TILE(kmA, kmB, ktA, 0, ktA, 1, vA0, vA1, vB0, vB1, vb + 0 * 64, kmB = lds128(kb + STG_KM + 1 * 1024), (void)0);
        if (st) { lds_signal(st == 3 ? ctl + C_X : cdone, lane); SBAR(); }
        else if (!st1_issued) { att_stage_load(kv, 4 * kh + 1, lds0 + KVB, w4, lane); SBAR(); }
        TILE(kmB, kmA, ktA, 1, ktB, 0, vB0, vB1, vA0, vA1, vb + 1 * 64, kmA = lds128(kb + STG_KM + 2 * 1024), ktB = lds128(kb + STG_KT + 1024));
        if (st == 0 && kh == 0) {
#pragma unroll 1
            for (int pc = w4; pc < 20; pc += 4) { const int k = kbase0 + 8 * pc + (pc < 10 ? 0 : 560); st128(rw, mark_off + ((k >> 5) * 64 + ((k >> 3) & 3) * 16) * 16, (u32x4){EMPTY, EMPTY, EMPTY, EMPTY}); }
            SBAR(); }
        if (st == 1 || st == 2) { lds_wait(cdone, 4u * st); att_stage_load(kv, 4 * kh + st + 1, lds0 + (buf ^ 1) * KVB, w4, lane); SBAR(); }
        TILE(kmA, kmB, ktB, 0, ktB, 1, vA0, vA1, vB0, vB1, vb + 2 * 64, kmB = lds128(kb + STG_KM + 3 * 1024), (void)0);
        TILE(kmB, kmA, ktB, 1, ktA, 0, vB0, vB1, vA0, vA1, vb + 3 * 64, kmA = lds128(kb + STG_KM + 4 * 1024), ktA = lds128(kb + STG_KT + 2048));
        if (kh == 1 && 2 * st + 1 == 5) __builtin_amdgcn_s_setprio(0);
        TILE(kmA, kmB, ktA, 0, ktA, 1, vA0, vA1, vB0, vB1, vb + 4 * 64, kmB = lds128(kb + STG_KM + 5 * 1024), (void)0);
        TILE(kmB, kmA, ktA, 1, ktB, 0, vB0, vB1, vA0, vA1, vb + 5 * 64, kmA = lds128(kb + STG_KM + 6 * 1024), ktB = lds128(kb + STG_KT + 3072));
        if (st < 3) { VMW(0); lds_signal(cland, lane); SBAR(); }
        TILE(kmA, kmB, ktB, 0, ktB, 1, vA0, vA1, vB0, vB1, vb + 6 * 64, kmB = lds128(kb + STG_KM + 7 * 1024), (void)0);
        if (st < 3) { lds_wait(cland, 4u * (st + 1));
            if (st == 0 && kh == 0 && w4 == 0) { if (lane == 0) st64(rw, fm_off, (u32x2){ctl[C_TAG + 1], ctl[C_TAG + 2]}); lds_signal(ctl + C_MKD, lane); }
            SBAR(); }
        TILE(kmB, kmA, ktB, 1, ktA, 0, vB0, vB1, vA0, vA1, vb + 7 * 64, kmA = lds128(kn + STG_KM + 0 * 1024), ktA = lds128(kn + STG_KT));
    }
    pB1[2] = pk2(ee0, ee1); pB1[3] = pk2(ee2, ee3);
    O1 = MFMA32(vB0, __builtin_bit_cast(bf16x8, pB0), O1);
    O1 = MFMA32(vB1, __builtin_bit_cast(bf16x8, pB1), O1);
#undef GRP
#undef TILE
}

__device__ __forceinline__ int ofrag_off(int s, int k) { return WS_O + ((((s >> 4) * 40 + (k >> 5)) * 64 + ((k >> 3) & 3) * 16 + (s & 15)) * 16 + (k & 7) * 2); }
__device__ __forceinline__ void touch(const void* g, unsigned lds_dummy) {
    unsigned sv; asm volatile("s_mov_b32 %0, m0\n\ts_mov_b32 m0, %2\n\ts_nop 0\n\tglobal_load_lds_dword %1, off\n\ts_mov_b32 m0, %0" : "=&s"(sv) : "v"(g), "s"(lds_dummy) : "memory"); }

__device__ __forceinline__ constexpr int wb_at(int ks) { return ((ks >> 2) == 0 ? AA + 2 * A_SLOT : (ks >> 2) == 1 ? AA : (ks >> 2) == 2 ? AA + A_SLOT : (ks >> 2) == 3 ? C_S3 : C_S4) + (ks & 3) * 5120; }
template <int KS0>
__device__ __forceinline__ bool ob_part(char* lds, LAS unsigned* ctl, rsrc_t rw, int fo, unsigned lane, f32x4 (&cc)[5]) {
    const unsigned i16 = lane & 15, g = lane >> 4, q4 = i16 >> 2, p4 = i16 & 3;
    const lds_cptr cw0 = (lds_cptr)lds + (8 * g + q4 + 4 * (0 ^ (g & 1))) * 160 + p4 * 8, cw1 = (lds_cptr)lds + (8 * g + q4 + 4 * (1 ^ (g & 1))) * 160 + p4 * 8;
    const int fl = lane * 16;
    bf16x8 of[10], wA[5], wB[5]; bool ok = true;
#pragma unroll
    for (int u = 0; u < 10; ++u) of[u] = __builtin_bit_cast(bf16x8, ld128s(rw, fl, fo + (20 + KS0 + u) * 1024));
    lds_wait(ctl + C_WB, 4u);
    if (KS0) lds_wait(ctl + C_WB + 1, 4u);
#define O_READY(n0, N) do { bool got = false; \
        _Pragma("unroll 1") for (unsigned spins = 0; spins < SPIN_CAP; ++spins) { unsigned mx = 0; \
            _Pragma("unroll") for (int u = 0; u < (N); ++u) { const u32x4 v = __builtin_bit_cast(u32x4, of[(n0) + u]); mx = max(max(mx, v[0]), max(max(v[1], v[2]), v[3])); } \
            if (!__any(mx == EMPTY)) { got = true; break; } \
            __builtin_amdgcn_s_sleep(2); \
            _Pragma("unroll") for (int u = 0; u < (N); ++u) of[(n0) + u] = __builtin_bit_cast(bf16x8, ld128s(rw, fl, fo + (20 + KS0 + (n0) + u) * 1024)); } \
        ok &= got; } while (0)
#define O_RD(W, u) do { lds_cptr b0_ = cw0 + wb_at(KS0 + (u)), b1_ = cw1 + wb_at(KS0 + (u)); asm volatile("" : "+v"(b0_), "+v"(b1_)); _Pragma("unroll") for (int j = 0; j < 5; ++j) W[j] = ldstr2(b0_ + j * 32, b1_ + j * 32); SBAR(); } while (0)
#define O_MF(W, u) do { _Pragma("unroll") for (int j = 0; j < 5; ++j) cc[j] = MFMA16(W[j], of[u], cc[j]); asm volatile("" : "+v"(cc[0]), "+v"(cc[1]), "+v"(cc[2]), "+v"(cc[3]), "+v"(cc[4])); SBAR(); } while (0)
    O_RD(wA, 0);
    O_READY(0, 5);
    O_RD(wB, 1); O_MF(wA, 0); O_RD(wA, 2); O_MF(wB, 1); O_RD(wB, 3); O_MF(wA, 2); O_RD(wA, 4); O_MF(wB, 3); O_RD(wB, 5); O_MF(wA, 4);
    O_READY(5, 5);
    O_RD(wA, 6); O_MF(wB, 5); O_RD(wB, 7); O_MF(wA, 6); O_RD(wA, 8); O_MF(wB, 7); O_RD(wB, 9); O_MF(wA, 8); O_MF(wB, 9);
#undef O_READY
#undef O_RD
#undef O_MF
    return ok;
}

__device__ __forceinline__ void p0_issue(const Params& P, unsigned lds0, unsigned pw, unsigned lane, unsigned part, unsigned r0) {
    const char* wsrc = (const char*)(part ? P.wv : P.wk); const char* xsrc = (const char*)(P.x + (size_t)r0 * D);
#pragma unroll 1
    for (unsigned i = 0; i < 6; ++i) { unsigned id = pw + 8 * i; if (id == 47) id = 39;
        if (id < 25) gldsb(wsrc, id * 1024 + lane * 16, lds0 + P0W + id * 1024);
        else if (id < 45) { const unsigned p = (id - 25) * 64 + lane, row = p / 80, cpos = p - row * 80, ch = (cpos & ~15u) | ((cpos & 15) ^ row);
            gldsb(xsrc, row * 1280 + ch * 16, lds0 + P0X + (id - 25) * 1024); }
        else { const unsigned off = (id - 45) * 1024 + lane * 16; gldsb(P.gw, off < 1280 ? off : 1264, lds0 + P0G + (id - 45) * 1024); } }
}

__device__ __forceinline__ void b_waves(const Params& P, char* lds, LAS unsigned* ctl, rsrc_t rw, unsigned wid, unsigned lane, unsigned b, unsigned li, unsigned rt64, unsigned c4) {
    char* const ws = P.ws;
    const unsigned lds0 = (unsigned)(uintptr_t)lds;
    const int s0 = 64 * rt64;
    const unsigned i16 = lane & 15, g = lane >> 4, q4 = i16 >> 2, p4 = i16 & 3;
    unsigned bn = 0; LAS unsigned* bb = ctl + C_BB;
    f32x4 qa[6];
#pragma unroll
    for (int j = 0; j < 6; ++j) qa[j] = (f32x4){0.f, 0.f, 0.f, 0.f};
    if (wid >= 4) {
        const unsigned l = wid - 4;
        unsigned off[5];
#pragma unroll
        for (unsigned j = 0; j < 5; ++j) { const unsigned id = l + 4 * j;
            if (j < 2) { const unsigned p = id * 64 + lane, row = p >> 3, ch = (p & 7) ^ ((row >> 1) & 7); off[j] = row * 1280 + ch * 16; }
            else { const unsigned p = ((id < 18 ? id : 8) - 8) * 64 + lane, pos = p / 10, j10 = p - pos * 10, k = pos ^ (4 * ((pos >> 3) & 1)); off[j] = k * 1280 + j10 * 16; } }
        const char* xs = (const char*)(P.x + (size_t)s0 * D); const char* wsq = (const char*)(P.wq + 80 * c4);
        int slot = 0;
        lds_wait<false>(ctl + C_P0I, 8u);
#pragma unroll 1
        for (int t = 0; t < 10; ++t) {
            if (t == 5) { VMW(16); lds_signal(ctl + C_QL + l, lane); }
            if (t >= 5) { VMW(12); lds_signal(ctl + C_QL + l, lane);
                lds_wait4(ctl + C_QD, t - 4); }
            const unsigned so = lds0 + slot * Q_SLOT + l * 1024;
            gldsb(xs, off[0], so); gldsb(xs, off[1], so + 4096); gldsb(wsq, off[2], so + 8192); gldsb(wsq, off[3], so + 12288);
            if (l < 2) gldsb(wsq, off[4], so + 16384);
            xs += 128; wsq += 64 * 1280;
            slot = slot == 4 ? 0 : slot + 1;
        }
        VMW(12); lds_signal(ctl + C_QL + l, lane); VMW(8); lds_signal(ctl + C_QL + l, lane); VMW(4); lds_signal(ctl + C_QL + l, lane); VMW(0); lds_signal(ctl + C_QL + l, lane);
    } else {
        p0_issue(P, lds0, 4 + wid, lane, c4 & 1, s0 + 16 * (c4 >> 1));
        lds_signal(ctl + C_P0I, lane);
        VMW(0); lds_signal(ctl + C_P0L, lane);
        const unsigned rp = wid & 1, cgq = wid >> 1;
        const lds_cptr qx0 = (lds_cptr)lds + (32 * rp + i16) * 128 + ((g ^ (i16 >> 1)) * 16), qx1 = (lds_cptr)lds + (32 * rp + i16) * 128 + (((4 + g) ^ (i16 >> 1)) * 16);
        const lds_cptr qw0 = (lds_cptr)lds + Q_W + (8 * g + q4 + 4 * (0 ^ (g & 1))) * 160 + p4 * 8 + cgq * 96, qw1 = (lds_cptr)lds + Q_W + (8 * g + q4 + 4 * (1 ^ (g & 1))) * 160 + p4 * 8 + cgq * 96;
        if (wid < 3) { const unsigned idx = wid * 64 + lane, row = (li >> 1) + 16 * (idx >> 1);
            if (idx < 160) touch((const char*)((row < 640 ? P.woA + (size_t)row * 640 : P.woB + (size_t)(row - 640) * 640) + 80 * c4) + (idx & 1) * 156, lds0 + DUMMY); }
        bf16x8 xa[4], wa[6], xb[4], wb_[6];
#define Q_READ(X, W, t_, so_) do { lds_wait4(ctl + C_QL, (t_) + 1); X[0] = lds128(qx0 + (so_)); X[1] = lds128(qx0 + (so_) + 2048); X[2] = lds128(qx1 + (so_)); X[3] = lds128(qx1 + (so_) + 2048); \
            _Pragma("unroll") for (int j = 0; j < 6; ++j) W[j] = ldstr2(qw0 + (so_) + (j / 3) * 5120 + (j % 3) * 32, qw1 + (so_) + (j / 3) * 5120 + (j % 3) * 32); SBAR(); } while (0)
#define Q_MUL(X, W) do { _Pragma("unroll") for (int j = 0; j < 3; ++j) { qa[j] = MFMA16(W[j], X[0], qa[j]); qa[3 + j] = MFMA16(W[j], X[1], qa[3 + j]); } \
            _Pragma("unroll") for (int j = 0; j < 3; ++j) { qa[j] = MFMA16(W[3 + j], X[2], qa[j]); qa[3 + j] = MFMA16(W[3 + j], X[3], qa[3 + j]); } \
            asm volatile("" : "+v"(qa[0]), "+v"(qa[1]), "+v"(qa[2]), "+v"(qa[3]), "+v"(qa[4]), "+v"(qa[5])); SBAR(); } while (0)
#define Q_DONE() do { asm volatile("s_waitcnt lgkmcnt(0)" ::: "memory"); lds_signal(ctl + C_QD + wid, lane); } while (0)
        __builtin_amdgcn_s_setprio(3);
        Q_READ(xa, wa, 0, 0); Q_DONE();
        int slot = 1;
#pragma unroll 1
        for (int t = 0; t < 10; t += 2) {
            Q_READ(xb, wb_, t + 1, slot * Q_SLOT); slot = slot == 4 ? 0 : slot + 1;
            Q_MUL(xa, wa); Q_DONE();
            if (t + 2 < 10) { Q_READ(xa, wa, t + 2, slot * Q_SLOT); slot = slot == 4 ? 0 : slot + 1; }
            Q_MUL(xb, wb_);
            if (t + 2 < 10) Q_DONE();
        }
#undef Q_READ
#undef Q_MUL
#undef Q_DONE
    }
    if (wid == 4) {
        lds_wait<true>(ctl + C_TAG, 1u);
        const u64 tag = (u64)ctl[C_TAG + 1] | ((u64)ctl[C_TAG + 2] << 32);
        bool ok = false;
#pragma unroll 1
        for (unsigned spins = 0; spins < SPIN_CAP; ++spins) {
            const u32x4 v = ld128(rw, WS_FKV + li * 1024 + lane * 16);
            if (__all((v[0] == (unsigned)tag) & (v[1] == (unsigned)tag) & (v[2] == (unsigned)tag) & (v[3] == (unsigned)tag))) { ok = true; break; }
            __builtin_amdgcn_s_sleep(4);
        }
        if (!ok && lane == 0) ctl[C_BAD] = 1u;
        lds_signal(ctl + C_KVOK, lane);
        if (b == 0 && lane == 0) st64(rw, WS_EPOCH, (u32x2){(unsigned)tag, (unsigned)(tag >> 32)});
    }
    const char* kv = ws + WS_KV;
    const unsigned w4 = wid & 3, kh = wid >> 2;
    char* ldsh = lds + kh * STG_BYTES;
    const unsigned ldsh0 = lds0 + kh * STG_BYTES;
    u32x4 nr0 = {0u, 0u, 0u, 0u}, nr1 = nr0;
    if (wid < 4) {
        lds_wait4(ctl + C_QD, 10u);
#pragma unroll
        for (int j = 0; j < 6; ++j) { const unsigned jt = 3 * (wid >> 1) + j % 3, cq = 4 * jt + g, hl = (cq * 13) >> 6, dq = cq - 5 * hl;
            if (jt < 5) *(LAS u32x2*)((lds_ptr)lds + Q_ST + (32 * (wid & 1) + 16 * (j / 3) + i16) * QB_ROW + hl * 48 + dq * 8) = (u32x2){pk2(qa[j][0] * QSCALE, qa[j][1] * QSCALE), pk2(qa[j][2] * QSCALE, qa[j][3] * QSCALE)}; }
        lds_signal(ctl + C_QS, lane);
    } else {
        __builtin_amdgcn_s_setprio(3);
        lds_wait<false>(ctl + C_KVOK, 1u);
        asm volatile("global_load_dwordx4 %0, %2, %3 sc1\n\tglobal_load_dwordx4 %1, %2, %3 offset:1024 sc1" : "=&v"(nr0), "=&v"(nr1) : "v"(lane * 16), "s"(ws + WS_NRM) : "memory");
        lds_wait4(ctl + C_QD, 8u);
        att_stage_load(kv, 0, lds0, w4, lane); att_stage_load(kv, 4, lds0 + STG_BYTES, w4, lane);
    }
    lds_wait<false>(ctl + C_QS, 4u);
    const int r = lane & 31, hh = lane >> 5;
    bf16x8 qm[2]; u32x2 qtl[2];
#pragma unroll
    for (int h = 0; h < 2; ++h) {
        const lds_cptr qp = (lds_cptr)lds + Q_ST + (32 * (w4 & 1) + r) * QB_ROW + (2 * (w4 >> 1) + h) * 48;
        qm[h] = lds128(qp + hh * 16); qtl[h] = *(const LAS u32x2*)(qp + 32);
    }
    float q2[2];
#pragma unroll
    for (int h = 0; h < 2; ++h) {
        q2[h] = 0.f;
#pragma unroll
        for (int i = 0; i < 8; ++i) { const float v = bf2f((u16)qm[h][i]); q2[h] += v * v; }
        if (hh == 0) { const float a0 = lo2f(qtl[h][0]), a1 = hi2f(qtl[h][0]), a2 = lo2f(qtl[h][1]), a3 = hi2f(qtl[h][1]); q2[h] += a0 * a0 + a1 * a1 + a2 * a2 + a3 * a3; }
        q2[h] = sum32(q2[h]);
    }
    if (wid == 7) {
        asm volatile("s_waitcnt vmcnt(12)" : "+v"(nr0), "+v"(nr1) :: "memory");
        const float kmax2 = wave_max_pos(fmaxf(__uint_as_float(nr0[0]), __uint_as_float(nr1[0]))) + wave_max_pos(fmaxf(__uint_as_float(nr0[1]), __uint_as_float(nr1[1])));
        const float vmax = wave_max_pos(fmaxf(fmaxf(__uint_as_float(nr0[2]), __uint_as_float(nr1[2])), fmaxf(__uint_as_float(nr0[3]), __uint_as_float(nr1[3]))));
        if (lane == 0) { ctl[C_NRM] = __float_as_uint(kmax2); ctl[C_NRM + 1] = __float_as_uint(112.f - fmaxf(0.f, __builtin_amdgcn_logf(fmaxf(vmax, 1.f)))); }
    }
    VMW(0);
    asm volatile("" :: "v"(nr0), "v"(nr1));
    GBAR(bb, bn, 8);
    const float kmax2 = __uint_as_float(ctl[C_NRM]), lim = __uint_as_float(ctl[C_NRM + 1]);
    const float bnd0 = __builtin_sqrtf(q2[0] * kmax2) * 1.001f + 1.f, bnd1 = __builtin_sqrtf(q2[1] * kmax2) * 1.001f + 1.f;
    const u32x4 z4 = {0u, 0u, 0u, 0u};
    bf16x8 qt[2][2];
#pragma unroll
    for (int h = 0; h < 2; ++h) { const u32x4 a = {qtl[h][0], qtl[h][1], 0u, 0u}; qt[h][0] = __builtin_bit_cast(bf16x8, hh ? z4 : a); qt[h][1] = __builtin_bit_cast(bf16x8, hh ? a : z4); }
    const f32x16 zero = {};
    float m[2]; bool unsafe = false;
    { const lds_cptr kb = (lds_cptr)ldsh + lane * 16; const bf16x8 k0 = lds128(kb + STG_KM), kt = lds128(kb + STG_KT);
#pragma unroll
      for (int h = 0; h < 2; ++h) {
        f32x16 sc = MFMA32(k0, qm[h], zero); sc = MFMA32(kt, qt[h][0], sc);
        float mm = sc[0];
#pragma unroll
        for (int i = 1; i < 16; ++i) mm = fmaxf(mm, sc[i]);
        mm = max32(mm);
        m[h] = mm;
        unsafe |= !((h ? bnd1 : bnd0) - m[h] <= lim);
      } }
    if (__any(unsafe) && lane == 0) ctl[C_FALL] = 1u;
    GBAR(bb, bn, 8);
    bool st1_issued = false;
    if (rfl(ctl[C_FALL]) != 0u) {
        att_stage_load(kv, 4 * kh + 1, ldsh0 + KVB, w4, lane); st1_issued = true;
        att_maxpass(kv, ldsh, bb, bn, kh, w4, lane, qm, qt, m);
#pragma unroll
        for (int h = 0; h < 2; ++h) m[h] = max32(m[h]);
    }
#pragma unroll
    for (int h = 0; h < 2; ++h) {
        const unsigned u0 = __float_as_uint(-m[h]) & 0xffff0000u; const float r1 = -m[h] - __uint_as_float(u0);
        const unsigned u1 = __float_as_uint(r1) & 0xffff0000u; const float r2 = r1 - __uint_as_float(u1);
        const u32x4 a = {qtl[h][0], qtl[h][1], (u0 >> 16) | u1, __float_as_uint(r2) >> 16};
        qt[h][0] = __builtin_bit_cast(bf16x8, hh ? z4 : a); qt[h][1] = __builtin_bit_cast(bf16x8, hh ? a : z4); }
    __builtin_amdgcn_s_setprio(0);
    f32x16 O[2] = {{}, {}};
    att_main(kv, ldsh, ctl, rw, ofrag_off(s0 + lane, 0), 80 * c4, WS_FA + (rt64 * 8 + c4) * 8, st1_issued, kh, w4, lane, qm, qt, O[0], O[1]);
    lds_signal(ctl + C_BD, lane);
    __builtin_amdgcn_s_setprio(3);
    {
        LAS f32x4* xo = (LAS f32x4*)((lds_ptr)lds + (2 * w4 + kh) * 4096) + lane;
        LAS f32x4* xi = (LAS f32x4*)((lds_ptr)lds + (2 * w4 + (kh ^ 1)) * 4096) + lane;
        const f32x16& Og = kh ? O[0] : O[1]; const f32x16& Ok = kh ? O[1] : O[0];
        const int s = s0 + 32 * (w4 & 1) + r, k0 = 640 + (4 * c4 + 2 * (w4 >> 1) + kh) * DH;
        const int a0 = ofrag_off(s, k0 + 4 * hh), a1 = ofrag_off(s, k0 + 8 + 4 * hh), a2 = ofrag_off(s, k0 + 16);
        lds_wait<false>(ctl + C_X, 8u);
        xo[0] = (f32x4){Og[0], Og[1], Og[2], Og[3]}; xo[64] = (f32x4){Og[4], Og[5], Og[6], Og[7]}; xo[128] = (f32x4){Og[8], Og[9], Og[10], Og[11]};
        xo[192] = (f32x4){kh ? m[0] : m[1], 0.f, 0.f, 0.f};
        lds_signal(ctl + C_P + 2 * w4 + kh, lane);
        lds_wait<false>(ctl + C_P + 2 * w4 + (kh ^ 1), 1u);
        const f32x4 p0 = xi[0], p1 = xi[64], p2 = xi[128]; const float m1 = xi[192][0];
        const float mo = kh ? m[1] : m[0];
        const float M = fmaxf(mo, m1), f0 = EXP2(mo - M), f1 = EXP2(m1 - M);
        float o[12];
#pragma unroll
        for (int i = 0; i < 4; ++i) { o[i] = Ok[i] * f0 + p0[i] * f1; o[4 + i] = Ok[4 + i] * f0 + p1[i] * f1; o[8 + i] = Ok[8 + i] * f0 + p2[i] * f1; }
        auto lr = __builtin_amdgcn_permlane32_swap(__float_as_uint(o[8]), __float_as_uint(o[8]), false, false);
        const float inv = __builtin_amdgcn_rcpf(__uint_as_float(lr[1]));
#define NE(x) ((x) == EMPTY ? 0x7fc07fc0u : (x))
        const unsigned w0 = pk2(o[0] * inv, o[1] * inv), w1 = pk2(o[2] * inv, o[3] * inv), w2 = pk2(o[4] * inv, o[5] * inv), w3 = pk2(o[6] * inv, o[7] * inv), w4_ = pk2(o[8] * inv, o[9] * inv), w5 = pk2(o[10] * inv, o[11] * inv);
        st64(rw, a0, (u32x2){NE(w0), NE(w1)});
        st64(rw, a1, (u32x2){NE(w2), NE(w3)});
        if (hh == 0) st64(rw, a2, (u32x2){NE(w4_), NE(w5)});
#undef NE
        lds_signal(ctl + C_D, lane);
    }
    if (kh == 1) {
        lds_wait<false>(ctl + C_BD, 8u);
#pragma unroll
        for (unsigned i = 0; i < 5; ++i) { const unsigned p = 256 * i + w4 * 64 + lane, pos = p / 10, j10 = p - pos * 10, off = (pos ^ (4 * ((pos >> 3) & 1))) * 1280 + j10 * 16;
            const char* sb = (const char*)(P.woB + 80 * c4) + 3 * (128 * 1280);
            gldsb(sb, off, rfl(lds0 + C_S3 + (4 * i + w4) * 1024)); gldsb(sb + 128 * 1280, off, rfl(lds0 + C_S4 + (4 * i + w4) * 1024)); }
        VMW(0); lds_signal(ctl + C_WB + 1, lane);
    }
    if (kh == 0) {
        f32x4 cc[5];
#pragma unroll
        for (int j = 0; j < 5; ++j) cc[j] = (f32x4){0.f, 0.f, 0.f, 0.f};
        lds_wait(ctl + C_FM, 1u);
        lds_wait(ctl + C_D, 8u);
        __builtin_amdgcn_s_sleep(6);
        const bool ok = ob_part<10>(lds, ctl, rw, WS_O + (4 * rt64 + w4) * 40 * 1024, lane, cc);
        if (!ok) ctl[C_BAD] = 1u;
        LAS f32x4* red = (LAS f32x4*)((lds_ptr)lds + w4 * 5120) + lane;
#pragma unroll
        for (int j = 0; j < 5; ++j) red[j * 64] = cc[j];
        lds_signal(ctl + C_R + w4, lane);
    }
    asm volatile("s_waitcnt lgkmcnt(0)" ::: "memory");
}

__device__ __forceinline__ void a_waves(const Params& P, char* lds, LAS unsigned* ctl, rsrc_t rw, unsigned a, unsigned lane, unsigned b, unsigned li, unsigned rt64, unsigned c4) {
    const unsigned lds0 = (unsigned)(uintptr_t)lds;
    const int s0 = 64 * rt64;
    const unsigned i16 = lane & 15, g = lane >> 4, q4 = i16 >> 2, p4 = i16 & 3;
    unsigned an = 0; LAS unsigned* ab = ctl + C_AB;
    const unsigned part = c4 & 1, r0 = s0 + 16 * (c4 >> 1);
    {
        if (a == 3) { u64 pc; asm volatile("s_getpc_b64 %0" : "=s"(pc)); const unsigned ln = li * 10 + (lane < 10 ? lane : 0); touch((const char*)(pc & ~127ull) + (ln < CODE_LINES ? ln : 0) * 128, lds0 + DUMMY); }
    }
    p0_issue(P, lds0, a, lane, part, r0);
    lds_signal(ctl + C_P0I, lane);
    const u32x2 ep = ld64(rw, WS_EPOCH);
    const u64 tag = ((u64)ep[0] | ((u64)ep[1] << 32)) + 0x9E3779B97F4A7C15ull;
    const u32x2 tagv = {(unsigned)tag, (unsigned)(tag >> 32)};
    VMW(0);
    lds_signal(ctl + C_P0L, lane); lds_wait<false>(ctl + C_P0L, 8u);
    if (a < 2 || (a == 2 && part == 0)) {
        const lds_cptr p0x = (lds_cptr)lds + P0X + i16 * 1280;
        f32x4 pacc = {0.f, 0.f, 0.f, 0.f};
#define P0XF(ks) lds128(p0x + ((((4 * (ks) + g) & ~15u) | (((4 * (ks) + g) & 15) ^ i16)) * 16))
        const int ks0 = r0 & 255, img = WS_KV + (r0 >> 8) * STG_BYTES, nrm = WS_NRM + (r0 >> 4) * 16;
        if (a == 2) {
            const bf16x8 z = {0, 0, 0, 0, 0, 0, 0, 0};
#pragma unroll
            for (int ks = 0; ks < 20; ++ks) { const bf16x8 gv = lds128((lds_cptr)lds + P0G + (32 * ks + 8 * g) * 2); pacc = MFMA16(i16 == 0 ? gv : z, P0XF(ks), pacc); }
            if (g == 0) st32(rw, WS_G + (r0 + i16) * 4, __float_as_uint(1.f / (1.f + __expf(-pacc[0]))));
        } else {
            const lds_cptr p0w = (lds_cptr)lds + P0W + (8 * g + q4) * 40 + (16 * a + 4 * p4) * 2;
            if (part == 0) {
#pragma unroll
                for (int ks = 0; ks < 20; ++ks) pacc = MFMA16(ldstr2(p0w + ks * 1280, p0w + ks * 1280 + 160), P0XF(ks), pacc);
                const unsigned w0 = pk2(pacc[0], pacc[1]), w1 = pk2(pacc[2], pacc[3]);
                const int key = ks0 + i16, tile = key >> 5, r = swap23(key & 31);
                float n2 = lo2f(w0) * lo2f(w0) + hi2f(w0) * hi2f(w0) + lo2f(w1) * lo2f(w1) + hi2f(w1) * hi2f(w1);
                if (a == 0) { st64(rw, img + STG_KM + tile * 1024 + (g >> 1) * 512 + r * 16 + (g & 1) * 8, (u32x2){w0, w1});
                    n2 = sum16(n2); n2 = sum32(n2); }
                else { if (g == 0) st128(rw, img + STG_KT + (tile >> 1) * 1024 + (tile & 1) * 512 + r * 16, (u32x4){w0, w1, 0x3f803f80u, 0x3f80u}); if (g != 0) n2 = 0.f; }
                n2 = wave_max_pos(n2);
                if (lane == 0) st32(rw, nrm + a * 4, __float_as_uint(n2));
            } else {
#pragma unroll
                for (int ks = 0; ks < 20; ++ks) pacc = MFMA16(P0XF(ks), ldstr2(p0w + ks * 1280, p0w + ks * 1280 + 160), pacc);
                const unsigned w0 = pk2(pacc[0], pacc[1]), w1 = pk2(pacc[2], pacc[3]);
                const int d = 16 * a + i16;
                float vm = fmaxf(fmaxf(fabsf(lo2f(w0)), fabsf(hi2f(w0))), fmaxf(fabsf(lo2f(w1)), fabsf(hi2f(w1))));
                if (d >= 20) vm = 0.f;
                if (d <= 20) st64(rw, img + STG_VT + d * VT_ROW + (ks0 + 4 * g) * 2, d == 20 ? (u32x2){0x3f803f80u, 0x3f803f80u} : (u32x2){w0, w1});
                if (a == 1 && ks0 == 0 && lane < 40) st128(rw, img + STG_VT + 21 * VT_ROW + lane * 16, (u32x4){0u, 0u, 0u, 0u});
                vm = wave_max_pos(vm);
                if (lane == 0) st32(rw, nrm + (2 + a) * 4, __float_as_uint(vm));
            }
        }
#undef P0XF
        VMW(0);
    }
    GBARS(ab, an, 4);
    bool ok = true;
    if (a == 3) { if (lane < 32) st32(rw, WS_FKV + lane * 1024 + b * 4, tagv[0]);
        if (lane == 0) { ctl[C_TAG + 1] = tagv[0]; ctl[C_TAG + 2] = tagv[1]; }
        lds_signal(ctl + C_TAG, lane);
    }
    unsigned aoff[5];
#pragma unroll
    for (unsigned j = 0; j < 5; ++j) { const unsigned id = a + 4 * j;
        if (id < 15) { const unsigned seg = id / 5, p = (id - 5 * seg) * 64 + lane, pos = p / 10, j10 = p - pos * 10, k = pos ^ (4 * ((pos >> 3) & 1)); aoff[j] = k * 3840 + seg * 1280 + j10 * 16; }
        else { const unsigned p = (id - 15) * 64 + lane, row = p >> 2, ch = (p & 3) ^ (3 * ((row >> 2) & 1)); int gr = s0 - 16 + (int)row; gr = gr < 0 ? 0 : gr; aoff[j] = gr * 1280 + ch * 16; } }
    const char* awb = (const char*)(P.wqkv + 80 * c4); const char* axb = (const char*)P.x;
#define A_ISSUE(slot) do { gldsb(awb, aoff[0], lds0 + AA + (slot) * A_SLOT + a * 1024); gldsb(awb, aoff[1], lds0 + AA + (slot) * A_SLOT + (a + 4) * 1024); gldsb(awb, aoff[2], lds0 + AA + (slot) * A_SLOT + (a + 8) * 1024); \
        gldsb(a < 3 ? awb : axb, aoff[3], lds0 + AA + (slot) * A_SLOT + (a + 12) * 1024); gldsb(axb, aoff[4], lds0 + AA + (slot) * A_SLOT + (a + 16) * 1024); awb += 32 * 3840; axb += 64; } while (0)
    lds_wait4<true>(ctl + C_QL, 8u);
    A_ISSUE(0); A_ISSUE(1);
    f32x4 acc[5][4];
#pragma unroll
    for (int i = 0; i < 5; ++i)
#pragma unroll
        for (int j = 0; j < 4; ++j) acc[i][j] = (f32x4){0.f, 0.f, 0.f, 0.f};
    const lds_cptr ax = (lds_cptr)lds + AA + A_X + i16 * 64 + ((g ^ (3 * ((i16 >> 2) & 1))) * 16);
    lds_cptr aw0[4], aw1[4];
#pragma unroll
    for (unsigned j = 0; j < 4; ++j) { unsigned jt = 4 * a + j; jt = jt < 15 ? jt : 14; const unsigned sg = jt / 5, wi = jt - 5 * sg;
        aw0[j] = (lds_cptr)lds + AA + sg * 5120 + wi * 32 + (8 * g + q4 + 4 * (0 ^ (g & 1))) * 160 + p4 * 8; aw1[j] = (lds_cptr)lds + AA + sg * 5120 + wi * 32 + (8 * g + q4 + 4 * (1 ^ (g & 1))) * 160 + p4 * 8; }
    VMW(5); lds_signal(ab, lane); an += 4;
    { int slot = 0;
      bf16x8 xa[5], xb[5], w0, w1, wc, wd;
#define A_FIRST(X, so_) do { lds_wait<true>(ab, an); w0 = ldstr2(aw0[0] + (so_), aw1[0] + (so_)); \
            _Pragma("unroll") for (int i = 0; i < 5; ++i) X[i] = lds128(ax + (so_) + i * 1024); \
            w1 = ldstr2(aw0[1] + (so_), aw1[1] + (so_)); SBAR(); } while (0)
#define A_MF(X, i, j, w) acc[i][j] = MFMA16(w, X[i], acc[i][j])
#define A_LD(j) do { if (t + 2 < 20) gldsb((j) < 3 || ((j) == 3 && a < 3) ? awb : axb, aoff[j], lds0 + AA + fs * A_SLOT + (a + 4 * (j)) * 1024); } while (0)
#define A_STAGE(X, Y) do { \
        const int so = slot * A_SLOT, fs = slot == 0 ? 2 : slot - 1; \
        wc = ldstr2(aw0[2] + so, aw1[2] + so); \
        A_MF(X, 0, 0, w0); A_LD(0); A_MF(X, 1, 0, w0); A_MF(X, 2, 0, w0); A_LD(1); A_MF(X, 3, 0, w0); A_MF(X, 4, 0, w0); SBAR(); \
        wd = ldstr2(aw0[3] + so, aw1[3] + so); \
        A_MF(X, 0, 1, w1); A_LD(2); A_MF(X, 1, 1, w1); A_MF(X, 2, 1, w1); A_LD(3); A_MF(X, 3, 1, w1); A_MF(X, 4, 1, w1); SBAR(); \
        A_MF(X, 0, 2, wc); A_LD(4); A_MF(X, 1, 2, wc); SBAR(); \
        awb += 32 * 3840; axb += 64; \
        if (t < 18) VMW(5); else VMW(0);          \
        lds_signal(ab, lane); an += 4; \
        A_MF(X, 2, 2, wc); A_MF(X, 3, 2, wc); A_MF(X, 4, 2, wc); SBAR(); \
        slot = slot == 2 ? 0 : slot + 1; \
        if (t + 1 < 20) A_FIRST(Y, slot * A_SLOT); \
        A_MF(X, 0, 3, wd); A_MF(X, 1, 3, wd); A_MF(X, 2, 3, wd); A_MF(X, 3, 3, wd); A_MF(X, 4, 3, wd); SBAR(); } while (0)
      A_FIRST(xa, 0);
#pragma unroll 1
      for (int t2 = 0; t2 < 20; t2 += 2) {
        { const int t = t2; A_STAGE(xa, xb); }
        { const int t = t2 + 1; A_STAGE(xb, xa); }
      } }
#undef A_FIRST
#undef A_STAGE
#undef A_MF
#undef A_LD
    ok &= poll_tags<1>(rw, WS_FA + (rt64 * 8 + (lane & 7)) * 8, lane < 8, tag);
    if (!ok) ctl[C_BAD] = 1u;
    lds_signal(ctl + C_FM, lane);
    lds_wait<true>(ab, an);
    { lds_ptr st = (lds_ptr)lds;
#pragma unroll
      for (unsigned j = 0; j < 4; ++j) { const unsigned jt = 4 * a + j, sg = jt / 5, wi = jt - 5 * sg, cq = 4 * wi + g, hl = (cq * 13) >> 6, dq = cq - 5 * hl;
        if (jt < 15) {
#pragma unroll
            for (int i = 0; i < 5; ++i) { const int row = 16 * i + i16; const f32x4 v = acc[i][j];
                if (sg < 2) *(LAS u32x2*)(st + ST_QK + row * QK_ROW + sg * 192 + hl * 48 + dq * 8) = (u32x2){pk2(v[0], v[1]), pk2(v[2], v[3])};
                else *(LAS f32x4*)(st + ST_V + row * V_ROW + hl * 80 + dq * 16) = v; } } } }
    GBARS(ab, an, 4);
    {
        const int sl = lane, row = 16 + sl, s = s0 + sl;
        const lds_cptr qp = (lds_cptr)lds + ST_QK + row * QK_ROW + a * 48;
        const lds_cptr kp = qp + 192 - 14 * QK_ROW;
        const u32x4 qa = *(const LAS u32x4*)qp, qb = *(const LAS u32x4*)(qp + 16); const u32x2 qc = *(const LAS u32x2*)(qp + 32);
        u32x4 ka[8], kb[8]; u32x2 kc[8];
#pragma unroll
        for (int w = 0; w < 8; ++w) { ka[w] = *(const LAS u32x4*)(kp + (14 - 2 * w) * QK_ROW); kb[w] = *(const LAS u32x4*)(kp + (14 - 2 * w) * QK_ROW + 16); kc[w] = *(const LAS u32x2*)(kp + (14 - 2 * w) * QK_ROW + 32); }
        SBAR();
        const lds_cptr vp = (lds_cptr)lds + ST_V + (row - 14) * V_ROW + a * 80;
        float lg[8]; float m = -3.0e38f;
#pragma unroll
        for (int w = 0; w < 8; ++w) {
            float x = dot2(qa[0], ka[w][0], 0.f), x2 = dot2(qa[1], ka[w][1], 0.f);
            x = dot2(qa[2], ka[w][2], x); x2 = dot2(qa[3], ka[w][3], x2); x = dot2(qb[0], kb[w][0], x); x2 = dot2(qb[1], kb[w][1], x2);
            x = dot2(qb[2], kb[w][2], x); x2 = dot2(qb[3], kb[w][3], x2); x = dot2(qc[0], kc[w][0], x); x2 = dot2(qc[1], kc[w][1], x2);
            x = (x + x2) * QSCALE;
            lg[w] = (s - 2 * w >= 0) ? x : -1.0e9f * LOG2E; m = fmaxf(m, lg[w]);
        }
        f32x4 va[3][5], vb[3][5];
#define LV(dst, w0, n) do { _Pragma("unroll") for (int w = 0; w < (n); ++w) _Pragma("unroll") for (int j = 0; j < 5; ++j) dst[w][j] = *(const LAS f32x4*)(vp + (14 - 2 * ((w0) + w)) * V_ROW + j * 16); SBAR(); } while (0)
#define PIN5(o) asm volatile("" : "+v"(o[0]), "+v"(o[1]), "+v"(o[2]), "+v"(o[3]), "+v"(o[4]))
        LV(va, 0, 3);
        float l = 0.f;
#pragma unroll
        for (int w = 0; w < 8; ++w) { lg[w] = EXP2(lg[w] - m); l += lg[w]; }
        const float inv = __builtin_amdgcn_rcpf(l);
        LV(vb, 3, 3);
        f32x4 o[5];
#pragma unroll
        for (int j = 0; j < 5; ++j) o[j] = va[0][j] * lg[0] + va[1][j] * lg[1] + va[2][j] * lg[2];
        PIN5(o); SBAR();
        LV(va, 6, 2);
#pragma unroll
        for (int j = 0; j < 5; ++j) o[j] += vb[0][j] * lg[3] + vb[1][j] * lg[4] + vb[2][j] * lg[5];
        PIN5(o); SBAR();
#pragma unroll
        for (int j = 0; j < 5; ++j) o[j] += va[0][j] * lg[6] + va[1][j] * lg[7];
#undef LV
#undef PIN5
        lds_wait(ctl + C_MKD, 1u);
#pragma unroll
        for (int j = 0; j < 5; ++j) { const unsigned w0 = pk2(o[j][0] * inv, o[j][1] * inv), w1 = pk2(o[j][2] * inv, o[j][3] * inv);
            st64(rw, ofrag_off(s, (4 * c4 + a) * DH + 4 * j), (u32x2){w0 == EMPTY ? 0x7fc07fc0u : w0, w1 == EMPTY ? 0x7fc07fc0u : w1}); }
    }
    GBARS(ab, an, 4);
    unsigned coff[5];
#pragma unroll
    for (unsigned i = 0; i < 5; ++i) { const unsigned p = 256 * i + a * 64 + lane, pos = p / 10, j10 = p - pos * 10; coff[i] = (pos ^ (4 * ((pos >> 3) & 1))) * 1280 + j10 * 16; }
    auto c_issue = [&](const u16* W, unsigned chunk, unsigned dst) {
        const char* sb = (const char*)(W + 80 * c4) + (size_t)chunk * (128 * 1280);
#pragma unroll
        for (unsigned i = 0; i < 5; ++i) gldsb(sb, coff[i], lds0 + dst + (4 * i + a) * 1024);
    };
    c_issue(P.woA, 0, AA); c_issue(P.woA, 1, AA + A_SLOT); c_issue(P.woA, 2, AA + 2 * A_SLOT);
    const int fo = WS_O + (4 * rt64 + a) * 40 * 1024, fl = lane * 16;
    bf16x8 of[20];
#pragma unroll
    for (int ks = 0; ks < 20; ++ks) of[ks] = __builtin_bit_cast(bf16x8, ld128s(rw, fl, fo + ks * 1024));
    const float gg = __uint_as_float(ld32(rw, WS_G + (s0 + 16 * a + i16) * 4));
    const unsigned cbo = (8 * g + q4) * 160 + p4 * 8, ct0 = 4 * (0 ^ (g & 1)) * 160, ct1 = 4 * (1 ^ (g & 1)) * 160;
    const lds_cptr cw0 = (lds_cptr)lds + cbo + ct0, cw1 = (lds_cptr)lds + cbo + ct1;
    f32x4 cc[5]; bf16x8 wA[5], wB[5];
#define C_READY(F, n0, N) do { \
        bool got = false; \
        _Pragma("unroll 1") for (unsigned spins = 0; spins < SPIN_CAP; ++spins) { \
            unsigned mx = 0; \
            _Pragma("unroll") for (int u = 0; u < (N); ++u) { const u32x4 v = __builtin_bit_cast(u32x4, of[(n0) + u]); mx = max(max(mx, v[0]), max(max(v[1], v[2]), v[3])); } \
            if (!__any(mx == EMPTY)) { got = true; break; } \
            __builtin_amdgcn_s_sleep(2); \
            _Pragma("unroll") for (int u = 0; u < (N); ++u) of[(n0) + u] = __builtin_bit_cast(bf16x8, ld128s(rw, fl, fo + ((F) + (n0) + u) * 1024)); \
        } \
        ok &= got; } while (0)
#define C_RD(W, off) do { lds_cptr b0_ = cw0 + (off), b1_ = cw1 + (off); asm volatile("" : "+v"(b0_), "+v"(b1_)); _Pragma("unroll") for (int j = 0; j < 5; ++j) W[j] = ldstr2(b0_ + j * 32, b1_ + j * 32); SBAR(); } while (0)
#define C_MF(W, ks) do { _Pragma("unroll") for (int j = 0; j < 5; ++j) cc[j] = MFMA16(W[j], of[ks], cc[j]); asm volatile("" : "+v"(cc[0]), "+v"(cc[1]), "+v"(cc[2]), "+v"(cc[3]), "+v"(cc[4])); SBAR(); } while (0)
#define C_CHUNK(base, ks0, next) do { C_RD(wB, (base) + 5120); C_MF(wA, (ks0)); C_RD(wA, (base) + 10240); C_MF(wB, (ks0) + 1); C_RD(wB, (base) + 15360); C_MF(wA, (ks0) + 2); \
        if ((next) >= 0) C_RD(wA, (next) >= 0 ? (next) : 0); C_MF(wB, (ks0) + 3); } while (0)
#pragma unroll
    for (int j = 0; j < 5; ++j) cc[j] = (f32x4){0.f, 0.f, 0.f, 0.f};
    C_READY(0, 0, 10); C_READY(0, 10, 10);
    VMW(0); GBARS(ab, an, 4);
    C_RD(wA, AA); C_CHUNK(AA, 0, AA + A_SLOT);
    GBARS(ab, an, 4); c_issue(P.woA, 3, AA);
    C_CHUNK(AA + A_SLOT, 4, AA + 2 * A_SLOT);
    GBARS(ab, an, 4); c_issue(P.woA, 4, AA + A_SLOT);
    C_CHUNK(AA + 2 * A_SLOT, 8, -1);
    VMW(5); GBARS(ab, an, 4); c_issue(P.woB, 0, AA + 2 * A_SLOT);
    C_RD(wA, AA); C_CHUNK(AA, 12, -1);
    VMW(5); GBARS(ab, an, 4); c_issue(P.woB, 1, AA);
    C_RD(wA, AA + A_SLOT); C_CHUNK(AA + A_SLOT, 16, -1);
    u32x2 cA[5];
#pragma unroll
    for (int j = 0; j < 5; ++j) { cA[j] = (u32x2){pk2(cc[j][0], cc[j][1]), pk2(cc[j][2], cc[j][3])}; cc[j] = (f32x4){0.f, 0.f, 0.f, 0.f}; }
    LAS u32x2* park = (LAS u32x2*)((lds_ptr)lds + 36864 + a * 2560) + lane;
    GBARS(ab, an, 4);
    c_issue(P.woB, 2, AA + A_SLOT);
    VMW(0); lds_signal(ctl + C_WB, lane);
    __builtin_amdgcn_s_setprio(3);
    lds_wait(ctl + C_D, 8u);
#pragma unroll
    for (int j = 0; j < 5; ++j) park[j * 64] = cA[j];
    __builtin_amdgcn_s_sleep(6);
    ok &= ob_part<0>(lds, ctl, rw, fo, lane, cc);
#undef C_READY
#undef C_RD
#undef C_MF
#undef C_CHUNK
    if (!ok) ctl[C_BAD] = 1u;
    lds_wait<false>(ctl + C_R + a, 1u);
    { LAS f32x4* red = (LAS f32x4*)((lds_ptr)lds + a * 5120) + lane;
#pragma unroll
      for (int j = 0; j < 5; ++j) cc[j] += red[j * 64]; }
    const float poison = (rfl(ctl[C_BAD]) || !ok) ? __builtin_nanf("") : 0.f;
    unsigned ln = __builtin_amdgcn_mbcnt_hi(~0u, __builtin_amdgcn_mbcnt_lo(~0u, 0u)); asm volatile("" : "+v"(ln));
#pragma unroll
    for (int j = 0; j < 5; ++j) { const u32x2 pa = *((LAS u32x2*)((lds_ptr)lds + 36864 + a * 2560) + ln + j * 64); const f32x4 av = {lo2f(pa[0]), hi2f(pa[0]), lo2f(pa[1]), hi2f(pa[1])}; const f32x4 v = av * gg + cc[j] * (1.f - gg) + poison;
        *(u32x2*)(P.out + (size_t)(s0 + 16 * a + (ln & 15)) * 640 + 80 * c4 + 16 * j + 4 * (ln >> 4)) = (u32x2){pk2(v[0], v[1]), pk2(v[2], v[3])}; }
}

__global__ __launch_bounds__(768, 3) void fused(Params P) {
    extern __shared__ __attribute__((aligned(16))) char lds[];
    char* const ws = P.ws;
    const unsigned tid = threadIdx.x, wid = rfl(tid >> 6);
    const unsigned lane = __builtin_amdgcn_mbcnt_hi(~0u, __builtin_amdgcn_mbcnt_lo(~0u, 0u));
    const unsigned b = blockIdx.x & 255, xg = b & 7, li = b >> 3;
    const unsigned rt64 = (xg >> 2) * 16 + (li >> 1), c4 = (xg & 3) * 2 + (li & 1);
    const rsrc_t rw = __builtin_amdgcn_make_buffer_rsrc(ws, 0, WS_END, 0x00020000);
    LAS unsigned* ctl = (LAS unsigned*)((lds_ptr)lds + CTL);
    if (tid < 64) ctl[tid] = 0u;
    LBAR();
    if (wid < 8) b_waves(P, lds, ctl, rw, wid & 7, lane, b, li, rt64, c4);
    else { __builtin_amdgcn_s_setprio(2); a_waves(P, lds, ctl, rw, wid & 3, lane, b, li, rt64, c4); }
}

static inline Params mk_params(void* const* d_in, void* d_out, void* d_ws) {
    Params P; P.x = (const u16*)d_in[0]; P.wqkv = (const u16*)d_in[1]; P.woA = (const u16*)d_in[2]; P.wq = (const u16*)d_in[4]; P.wk = (const u16*)d_in[5]; P.wv = (const u16*)d_in[6];
    P.woB = (const u16*)d_in[7]; P.gw = (const u16*)d_in[8]; P.ws = (char*)d_ws; P.out = (u16*)d_out; return P; }
extern "C" void kernel_launch(void* const* d_in, const int* in_sizes, int n_in, void* d_out, int out_size, void* d_ws, size_t ws_size, hipStream_t stream) {
    (void)hipFuncSetAttribute((const void*)fused, hipFuncAttributeMaxDynamicSharedMemorySize, LDS_TOTAL);
    fused<<<256, 768, LDS_TOTAL, stream>>>(mk_params(d_in, d_out, d_ws));
}
```
